```python
import torch

def _softmax(x, axis=-1):
    return torch.softmax(x, dim=axis)
def _gelu(x):
    return torch.nn.functional.gelu(x, approximate="tanh")
def _silu(x):
    return torch.nn.functional.silu(x)
def _sigmoid(x):
    return torch.sigmoid(x)
def _layernorm(x, axis=-1, eps=1e-5):
    m = x.mean(dim=axis, keepdim=True)
    v = x.var(dim=axis, keepdim=True, unbiased=False)
    return (x - m) / torch.sqrt(v + eps)
def _scatter_add(tgt, idx, src, axis=0):
    return tgt.clone().index_add_(axis, idx, src)
def _segment_sum(src, seg, n):
    out = torch.zeros((n,) + src.shape[1:], dtype=src.dtype)
    return out.index_add_(0, seg, src)
def _pad(x, pw):
    flat = tuple(v for pair in reversed(pw) for v in pair)
    return torch.nn.functional.pad(x, flat)

kernel_name = 'concat_heads__gqa_block_cached__mha_block_cached__near_ridge'

def setup_inputs():

    _w = (lambda a: (lambda t: t.to(torch.bfloat16) if t.is_floating_point() else t)(torch.as_tensor(a)))
    def _A_setup():
        B, Sq, D = 8, 64, 512
        Hq, Hkv, Sk, Dh = 32, 8, 1024, 128
        assert Hq % Hkv == 0, (Hq, Hkv)
        return {
            "x": _w((lambda *s: torch.randn(*s, dtype=torch.bfloat16) * 0.8)(B, Sq, D)),
            "k_cache": _w((lambda *s: torch.randn(*s, dtype=torch.bfloat16) * 0.8)(B, Hkv, Sk, Dh)),
            "v_cache": _w((lambda *s: torch.randn(*s, dtype=torch.bfloat16) * 0.05)(B, Hkv, Sk, Dh)),
            "wq": _w((lambda *s: torch.randn(*s, dtype=torch.bfloat16) * 0.8)(D, Hq * Dh) * (D ** -0.5)),
            "wo": _w((lambda *s: torch.randn(*s, dtype=torch.bfloat16) * 0.05)(Hq * Dh, D) * ((Hq * Dh) ** -0.5)),
        }
    _A_in = _A_setup()

    def _B_setup():
        B, Sq, D = 8, 64, 512
        H, Sk, Dh = 32, 1024, 128
        return {
            "x": _w((lambda *s: torch.randn(*s, dtype=torch.bfloat16) * 0.8)(B, Sq, D)),
            "k_cache": _w((lambda *s: torch.randn(*s, dtype=torch.bfloat16) * 0.8)(B, H, Sk, Dh)),
            "v_cache": _w((lambda *s: torch.randn(*s, dtype=torch.bfloat16) * 0.05)(B, H, Sk, Dh)),
            "wq": _w((lambda *s: torch.randn(*s, dtype=torch.bfloat16) * 0.8)(D, H * Dh) * (D ** -0.5)),
            "wo": _w((lambda *s: torch.randn(*s, dtype=torch.bfloat16) * 0.05)(H * Dh, D) * ((H * Dh) ** -0.5)),
        }
    _B_in = _B_setup()

    return {
        "A_x": _A_in["x"],
        "A_k_cache": _A_in["k_cache"],
        "A_v_cache": _A_in["v_cache"],
        "A_wq": _A_in["wq"],
        "A_wo": _A_in["wo"],
        "B_x": _B_in["x"],
        "B_k_cache": _B_in["k_cache"],
        "B_v_cache": _B_in["v_cache"],
        "B_wq": _B_in["wq"],
        "B_wo": _B_in["wo"],
    }

def reference(A_x, A_k_cache, A_v_cache, A_wq, A_wo, B_x, B_k_cache, B_v_cache, B_wq, B_wo):
    def _A_ref(x, k_cache, v_cache, wq, wo):
        B, Sq, D = x.shape
        Hq, Dh = 32, 128
        Hkv = k_cache.shape[1]
        rep = Hq // Hkv
        q = (lambda x, *d: x.permute(*d))((x @ wq).reshape(B, Sq, Hq, Dh), 0, 2, 1, 3)
        k = (lambda x, n, axis: torch.repeat_interleave(x, n, dim=axis))(k_cache, rep, axis=1)
        v = (lambda x, n, axis: torch.repeat_interleave(x, n, dim=axis))(v_cache, rep, axis=1)
        scores = torch.einsum("bhqd,bhkd->bhqk", q, k) * (Dh ** -0.5)
        p = _softmax(scores, axis=-1)
        o = torch.einsum("bhqk,bhkd->bhqd", p, v)
        return (lambda x, *d: x.permute(*d))(o, 0, 2, 1, 3).reshape(B, Sq, Hq * Dh) @ wo

    def _B_ref(x, k_cache, v_cache, wq, wo):
        B, Sq, D = x.shape
        H, Dh = 32, 128
        q = (lambda x, *d: x.permute(*d))((x @ wq).reshape(B, Sq, H, Dh), 0, 2, 1, 3)
        scores = torch.einsum("bhqd,bhkd->bhqk", q, k_cache) * (Dh ** -0.5)
        p = _softmax(scores, axis=-1)
        o = torch.einsum("bhqk,bhkd->bhqd", p, v_cache)
        return (lambda x, *d: x.permute(*d))(o, 0, 2, 1, 3).reshape(B, Sq, H * Dh) @ wo

    a = _A_ref(A_x, A_k_cache, A_v_cache, A_wq, A_wo)
    b = _B_ref(B_x, B_k_cache, B_v_cache, B_wq, B_wo)
    return torch.concatenate([a, b], axis=-1)
```

```cpp
#include <hip/hip_runtime.h>
#include <hip/hip_bf16.h>
#include <stdint.h>

#define MODE 0
#define POL_KV " nt"
#define POL_W " nt"
#define STAMPS 0

typedef __bf16 bf16x8 __attribute__((ext_vector_type(8)));
typedef __bf16 bf16x2 __attribute__((ext_vector_type(2)));
typedef short s16x4 __attribute__((ext_vector_type(4)));
typedef short s16x8 __attribute__((ext_vector_type(8)));
typedef float f32x4 __attribute__((ext_vector_type(4)));
typedef float f32x2 __attribute__((ext_vector_type(2)));
typedef unsigned u32x4 __attribute__((ext_vector_type(4)));
typedef unsigned long long u64;
#define LAS __attribute__((address_space(3)))
#define GAS __attribute__((address_space(1)))
#define CAS __attribute__((address_space(4)))
#define DI __device__ __forceinline__
#define SBAR() __builtin_amdgcn_sched_barrier(0)
#define MFMA(a, b, c) __builtin_amdgcn_mfma_f32_16x16x32_bf16((a), (b), (c), 0, 0, 0)
#define RSRC(p, n) __builtin_amdgcn_make_buffer_rsrc((void*)(p), 0, (n), 0x00020000)
typedef __amdgpu_buffer_rsrc_t rsrc_t;

constexpr float C1 = 0.08838834764831845f * 1.4426950408889634f;
constexpr float THR = 8.0f;
constexpr int RING = 32768, SLOT = 8192, SCR0 = 131072;
constexpr int LDS_BYTES = 163840;
constexpr size_t WS_FLAG = 0, WS_EPOCH = 65536, WS_STAMP = 131072, WS_PART = 1u << 20;

struct Params {
  const char* x[2]; const char* k[2]; const char* v[2]; const char* wq[2]; const char* wo[2];
  char* out; char* ws;
};

DI unsigned cvtpk(float lo, float hi) { f32x2 v = {lo, hi}; return __builtin_bit_cast(unsigned, __builtin_convertvector(v, bf16x2)); }
DI bf16x8 pack8(f32x4 a, f32x4 b) { u32x4 w = {cvtpk(a[0], a[1]), cvtpk(a[2], a[3]), cvtpk(b[0], b[1]), cvtpk(b[2], b[3])}; return __builtin_bit_cast(bf16x8, w); }
DI float bflo(unsigned u) { return __builtin_bit_cast(float, u << 16); }
DI float bfhi(unsigned u) { return __builtin_bit_cast(float, u & 0xffff0000u); }
DI float max3f(float a, float b, float c) { float r; asm("v_max3_f32 %0, %1, %2, %3" : "=v"(r) : "v"(a), "v"(b), "v"(c)); return r; }
DI float xg_max(float x) {
  auto s = __builtin_amdgcn_permlane16_swap(__float_as_uint(x), __float_as_uint(x), false, false);
  x = fmaxf(__uint_as_float(s[0]), __uint_as_float(s[1]));
  auto t = __builtin_amdgcn_permlane32_swap(__float_as_uint(x), __float_as_uint(x), false, false);
  return fmaxf(__uint_as_float(t[0]), __uint_as_float(t[1]));
}
template <int N> DI void wait_vm() { asm volatile("s_waitcnt vmcnt(%0)" ::"i"(N) : "memory"); }
DI void wg_barrier() { asm volatile("s_waitcnt lgkmcnt(0)\n\ts_barrier" ::: "memory"); }

#define DMA_CONTIG(POL) \
  asm volatile("s_waitcnt lgkmcnt(0)\n\ts_mov_b32 %0, m0\n\ts_mov_b32 m0, %7\n\ts_add_u32 %1, %8, 0x1000\n\t" \
               "buffer_load_dwordx4 %2, %6, %8 offen" POL " lds\n\t" \
               "buffer_load_dwordx4 %2, %6, %8 offen offset:1024" POL " lds\n\t" \
               "buffer_load_dwordx4 %3, %6, %8 offen offset:2048" POL " lds\n\t" \
               "buffer_load_dwordx4 %3, %6, %8 offen offset:3072" POL " lds\n\t" \
               "s_add_u32 m0, m0, 0x1000\n\ts_nop 0\n\t" \
               "buffer_load_dwordx4 %4, %6, %1 offen" POL " lds\n\t" \
               "buffer_load_dwordx4 %4, %6, %1 offen offset:1024" POL " lds\n\t" \
               "buffer_load_dwordx4 %5, %6, %1 offen offset:2048" POL " lds\n\t" \
               "buffer_load_dwordx4 %5, %6, %1 offen offset:3072" POL " lds\n\t" \
               "s_mov_b32 m0, %0" \
               : "=&s"(keep), "=&s"(t) : "v"(v0), "v"(v1), "v"(v2), "v"(v3), "s"(srd), "s"(lds), "s"(soff) : "memory", "scc")
DI void dma_kv(rsrc_t srd, unsigned v0, unsigned v1, unsigned v2, unsigned v3, unsigned soff, unsigned lds) {
  unsigned keep, t; DMA_CONTIG(POL_KV);
}
#define P1(V, POL) "buffer_load_dwordx4 " V ", %6, %1 offen" POL " lds\n\ts_add_u32 m0, m0, 0x400\n\ts_add_u32 %1, %1, %9\n\t"
DI void dma_rows(rsrc_t srd, unsigned v0, unsigned v1, unsigned v2, unsigned v3, unsigned soff, unsigned lds, unsigned step) {
  unsigned keep, t;
  asm volatile("s_waitcnt lgkmcnt(0)\n\ts_mov_b32 %0, m0\n\ts_mov_b32 m0, %7\n\ts_mov_b32 %1, %8\n\t"
               P1("%2", POL_W) P1("%3", POL_W) P1("%4", POL_W) P1("%5", POL_W) P1("%2", POL_W) P1("%3", POL_W) P1("%4", POL_W) P1("%5", POL_W)
               "s_mov_b32 m0, %0"
               : "=&s"(keep), "=&s"(t) : "v"(v0), "v"(v1), "v"(v2), "v"(v3), "s"(srd), "s"(lds), "s"(soff), "s"(step) : "memory", "scc");
}

DI bf16x8 rd128(LAS char* p) { return *(LAS bf16x8*)p; }
DI bf16x8 rdtr(LAS char* p) {
  s16x4 lo = __builtin_amdgcn_ds_read_tr16_b64_v4i16((LAS s16x4*)p);
  s16x4 hi = __builtin_amdgcn_ds_read_tr16_b64_v4i16((LAS s16x4*)(p + 1024));
  s16x8 r = {lo[0], lo[1], lo[2], lo[3], hi[0], hi[1], hi[2], hi[3]};
  return __builtin_bit_cast(bf16x8, r);
}

template <int SL> DI void gemm_slot(f32x4 (&acc)[4][2][4], LAS char* const (&va)[4], const bf16x8 (&B)[4]) {
#pragma unroll
  for (int s = 0; s < 4; ++s)
#pragma unroll
    for (int u = 0; u < 2; ++u) {
      bf16x8 a = rdtr(va[s] + SL * SLOT + 8 * u);
#pragma unroll
      for (int qb = 0; qb < 4; ++qb) acc[s][u][qb] = MFMA(a, B[qb], acc[s][u][qb]);
    }
}

struct St {
  f32x4 o[4][2][4]; f32x4 l[4]; f32x4 negm[4]; bf16x8 q[4][4];
};

template <int SL> DI void qk_read(bf16x8 (&kf)[2][4], LAS char* const (&ka)[4]) {
#pragma unroll
  for (int kb = 0; kb < 2; ++kb)
#pragma unroll
    for (int s = 0; s < 4; ++s) kf[kb][s] = rd128(ka[s] + SL * SLOT + kb * 1024);
}
DI void qk_mma(f32x4 (&S)[2][4], const bf16x8 (&kf)[2][4], const St& st) {
#pragma unroll
  for (int kb = 0; kb < 2; ++kb)
#pragma unroll
    for (int qb = 0; qb < 4; ++qb) S[kb][qb] = MFMA(kf[kb][0], st.q[qb][0], st.negm[qb]);
#pragma unroll
  for (int s = 1; s < 4; ++s)
#pragma unroll
    for (int kb = 0; kb < 2; ++kb)
#pragma unroll
      for (int qb = 0; qb < 4; ++qb) S[kb][qb] = MFMA(kf[kb][s], st.q[qb][s], S[kb][qb]);
}
DI void softmax_p(bf16x8 (&P)[4], const f32x4 (&S)[2][4]) {
#pragma unroll
  for (int qb = 0; qb < 4; ++qb) {
    f32x4 a, b;
#pragma unroll
    for (int r = 0; r < 4; ++r) { a[r] = __builtin_amdgcn_exp2f(S[0][qb][r]); b[r] = __builtin_amdgcn_exp2f(S[1][qb][r]); }
    P[qb] = pack8(a, b);
  }
}
DI float qmax(const f32x4 (&S)[2][4], int qb) {
  float m = max3f(S[0][qb][0], S[0][qb][1], S[0][qb][2]);
  m = max3f(m, S[0][qb][3], S[1][qb][0]);
  m = max3f(m, S[1][qb][1], S[1][qb][2]);
  return fmaxf(m, S[1][qb][3]);
}
template <bool FIRST> DI void rescale(f32x4 (&S)[2][4], St& st) {
#pragma unroll
  for (int qb = 0; qb < 4; ++qb) {
    float d = xg_max(qmax(S, qb));
    if (!FIRST) d = fmaxf(d, 0.f);
#pragma unroll
    for (int r = 0; r < 4; ++r) { S[0][qb][r] -= d; S[1][qb][r] -= d; st.negm[qb][r] -= d; }
    if (!FIRST) {
      const float al = __builtin_amdgcn_exp2f(-d);
#pragma unroll
      for (int r = 0; r < 4; ++r) st.l[qb][r] *= al;
#pragma unroll
      for (int s = 0; s < 4; ++s)
#pragma unroll
        for (int u = 0; u < 2; ++u)
#pragma unroll
          for (int r = 0; r < 4; ++r) st.o[s][u][qb][r] *= al;
    }
  }
}
DI void check(f32x4 (&S)[2][4], St& st) {
  float m = max3f(qmax(S, 0), qmax(S, 1), fmaxf(qmax(S, 2), qmax(S, 3)));
  if (__builtin_expect(__any(m > THR), 0)) rescale<false>(S, st);
}
template <int SL> DI void pv(St& st, LAS char* const (&va)[4], const bf16x8 (&P)[4], bf16x8 ones) {
  gemm_slot<SL>(st.o, va, P);
#pragma unroll
  for (int qb = 0; qb < 4; ++qb) st.l[qb] = MFMA(ones, P[qb], st.l[qb]);
}

__global__ __launch_bounds__(256, 1) void fused_attn(Params p) {
  extern __shared__ __attribute__((aligned(16))) char smem[];
  const int tid = threadIdx.x, lane = tid & 63, w = __builtin_amdgcn_readfirstlane(tid >> 6);
  const int c = lane & 15, g = lane >> 4, pr = w >> 1, kh = w & 1;
  const int cu = blockIdx.x, xi = cu & 7, sidx = cu >> 3;
  const int br = sidx & 1, jj = (sidx >> 1) & 1, b = sidx >> 2, h = 4 * xi + 2 * jj + pr;
  const int grp = br * 8 + b, mem = xi * 2 + jj;
  const int egrp = grp;
#define STAMP(i)
  const u64 tag = *(const CAS u64*)(p.ws + WS_EPOCH + egrp * 128) + 1ull;

  const unsigned ldsw = (unsigned)(uintptr_t)(LAS char*)smem + w * RING;
  LAS char* const lw = (LAS char*)smem + w * RING;
  const size_t kvo = (size_t)(br ? b * 32 + h : b * 8 + xi) * 262144 + kh * 131072;
  const rsrc_t rk = RSRC(p.k[br] + kvo, 131072), rv = RSRC(p.v[br] + kvo, 131072);
  const rsrc_t rx = RSRC(p.x[br] + b * 65536, 65536), rq = RSRC(p.wq[br], 4 << 20), ro = RSRC(p.wo[br], 4 << 20);
  unsigned sw[4];
#pragma unroll
  for (int v = 0; v < 4; ++v) sw[v] = (unsigned)((c ^ (4 * v + g)) * 16);
  const unsigned swv = (unsigned)((c ^ (4 * g)) * 16);
  const unsigned vV = g * 256 + swv, vW = g * 8192 + swv, vO = g * 1024 + swv;
  LAS char* ka[4]; LAS char* va[4];
#pragma unroll
  for (int s = 0; s < 4; ++s) {
    ka[s] = lw + (c >> 2) * 2048 + (c & 3) * 256 + (((4 * s + g) ^ c) * 16);
    va[s] = lw + (8 * g + (c >> 2)) * 256 + (((4 * s + (c & 3)) ^ (4 * (c >> 2))) * 16);
  }
#define ISSUE_X(kb2, rh, SL) dma_rows(rx, g * 1024 + sw[0], g * 1024 + sw[1], g * 1024 + sw[2], g * 1024 + sw[3], ((rh) ^ kh) * 32768 + kh * 512 + (kb2) * 256, ldsw + (SL) * SLOT, 4096)
#define ISSUE_W(st_, SL) dma_rows(rq, vW, vW, vW, vW, (256 * kh + 32 * (st_)) * 8192 + h * 256, ldsw + (SL) * SLOT, 32768)
#define ISSUE_K(i_, SL) dma_kv(rk, g * 256 + sw[0], g * 256 + sw[1], g * 256 + sw[2], g * 256 + sw[3], (i_) * 8192, ldsw + (SL) * SLOT)
#define ISSUE_V(i_, SL) dma_kv(rv, vV, vV, vV, vV, (i_) * 8192, ldsw + (SL) * SLOT)
#define ISSUE_O(t_, SL) dma_rows(ro, vO, vO, vO, vO, ((h - pr) * 128 + 32 * (t_)) * 1024 + w * 256, ldsw + (SL) * SLOT, 4096)

  St st;
  ISSUE_X(0, 0, 0); ISSUE_X(0, 1, 1); ISSUE_W(0, 2); ISSUE_W(1, 3);
  {
#pragma unroll
    for (int s = 0; s < 4; ++s)
#pragma unroll
      for (int u = 0; u < 2; ++u)
#pragma unroll
        for (int qb = 0; qb < 4; ++qb) st.o[s][u][qb] = f32x4{0.f, 0.f, 0.f, 0.f};
    LAS char* xa[4];
#pragma unroll
    for (int s = 0; s < 4; ++s) xa[s] = lw + c * 256 + (((4 * s + g) ^ c) * 16);
    bf16x8 xf[4][4];
#define READ_X(rh, SL) _Pragma("unroll") for (int s = 0; s < 4; ++s) { xf[s][2 * (rh)] = rd128(xa[s] + (SL) * SLOT); xf[s][2 * (rh) + 1] = rd128(xa[s] + (SL) * SLOT + 4096); }
    wait_vm<24>(); READ_X(0, 0); ISSUE_W(2, 0);
    wait_vm<24>(); READ_X(1, 1); ISSUE_W(3, 1);
    wait_vm<24>(); gemm_slot<2>(st.o, va, xf[0]); ISSUE_X(1, 0, 2);
    wait_vm<24>(); gemm_slot<3>(st.o, va, xf[1]); ISSUE_X(1, 1, 3);
    wait_vm<24>(); gemm_slot<0>(st.o, va, xf[2]); ISSUE_W(4, 0);
    wait_vm<24>(); gemm_slot<1>(st.o, va, xf[3]); ISSUE_W(5, 1);
    wait_vm<24>(); READ_X(0, 2); ISSUE_W(6, 2);
    wait_vm<24>(); READ_X(1, 3); ISSUE_W(7, 3);
    wait_vm<24>(); gemm_slot<0>(st.o, va, xf[0]); ISSUE_K(0, 0);
    wait_vm<24>(); gemm_slot<1>(st.o, va, xf[1]); ISSUE_K(1, 1);
    wait_vm<24>(); gemm_slot<2>(st.o, va, xf[2]); ISSUE_V(0, 2);
    wait_vm<24>(); gemm_slot<3>(st.o, va, xf[3]); ISSUE_K(2, 3);
  }
  STAMP(1);
  LAS char* const scr_own = (LAS char*)smem + SCR0 + w * SLOT + lane * 16;
  LAS char* const scr_par = (LAS char*)smem + SCR0 + (w ^ 1) * SLOT + lane * 16;
  {
#pragma unroll
    for (int j = 0; j < 2; ++j)
#pragma unroll
      for (int s = 0; s < 4; ++s) {
        const f32x4 a = st.o[s][0][2 + j], bq = st.o[s][1][2 + j];
        *(LAS bf16x8*)(scr_own + (j * 4 + s) * 1024) = pack8(a, bq);
      }
    wg_barrier();
    bf16x8 mine[2][4];
#pragma unroll
    for (int j = 0; j < 2; ++j)
#pragma unroll
      for (int s = 0; s < 4; ++s) {
        const u32x4 in = *(LAS u32x4*)(scr_par + (j * 4 + s) * 1024);
        f32x4 a = st.o[s][0][j], bq = st.o[s][1][j];
        a[0] = (a[0] + bflo(in[0])) * C1; a[1] = (a[1] + bfhi(in[0])) * C1; a[2] = (a[2] + bflo(in[1])) * C1; a[3] = (a[3] + bfhi(in[1])) * C1;
        bq[0] = (bq[0] + bflo(in[2])) * C1; bq[1] = (bq[1] + bfhi(in[2])) * C1; bq[2] = (bq[2] + bflo(in[3])) * C1; bq[3] = (bq[3] + bfhi(in[3])) * C1;
        mine[j][s] = pack8(a, bq);
        *(LAS bf16x8*)(scr_par + (j * 4 + s) * 1024) = mine[j][s];
      }
    wg_barrier();
#pragma unroll
    for (int j = 0; j < 2; ++j)
#pragma unroll
      for (int s = 0; s < 4; ++s) {
        const bf16x8 oth = *(LAS bf16x8*)(scr_own + (j * 4 + s) * 1024);
        st.q[j][s] = mine[j][s];
        st.q[2 + j][s] = oth;
      }
  }
  STAMP(2);
#pragma unroll
  for (int s = 0; s < 4; ++s)
#pragma unroll
    for (int u = 0; u < 2; ++u)
#pragma unroll
      for (int qb = 0; qb < 4; ++qb) st.o[s][u][qb] = f32x4{0.f, 0.f, 0.f, 0.f};
#pragma unroll
  for (int qb = 0; qb < 4; ++qb) { st.l[qb] = f32x4{0.f, 0.f, 0.f, 0.f}; st.negm[qb] = f32x4{0.f, 0.f, 0.f, 0.f}; }
  const u32x4 onesu = {0x3f803f80u, 0x3f803f80u, 0x3f803f80u, 0x3f803f80u};
  const bf16x8 ones = __builtin_bit_cast(bf16x8, onesu);
  f32x4 SA[2][4], SB[2][4];
  bf16x8 kf[2][4], P[4];
  wait_vm<24>(); qk_read<0>(kf, ka); ISSUE_V(1, 0);
  qk_mma(SA, kf, st); rescale<true>(SA, st);
#define STEP(Scur, Snxt, KSL, VSL, ISS_K, ISS_V) \
  wait_vm<24>(); qk_read<KSL>(kf, ka); ISS_K; \
  qk_mma(Snxt, kf, st); softmax_p(P, Scur); \
  wait_vm<24>(); pv<VSL>(st, va, P, ones); ISS_V; \
  check(Snxt, st);
  for (int i = 0; i < 12; i += 2) {
    STEP(SA, SB, 1, 2, ISSUE_K(i + 3, 1), ISSUE_V(i + 2, 2));
    STEP(SB, SA, 3, 0, ISSUE_K(i + 4, 3), ISSUE_V(i + 3, 0));
  }
  STEP(SA, SB, 1, 2, ISSUE_K(15, 1), ISSUE_V(14, 2));
  STEP(SB, SA, 3, 0, ISSUE_V(15, 3), ISSUE_O(0, 0));
  STEP(SA, SB, 1, 2, ISSUE_O(1, 1), ISSUE_O(2, 2));
  softmax_p(P, SB);
  wait_vm<24>(); pv<3>(st, va, P, ones);
  STAMP(3);
  bf16x8 of[4][4];
  {
    LAS f32x2* const stat_own = (LAS f32x2*)(lw + 3 * SLOT);
    LAS f32x2* const stat_par = (LAS f32x2*)((LAS char*)smem + (w ^ 1) * RING + 3 * SLOT);
    asm volatile("s_waitcnt lgkmcnt(0)" ::: "memory");
#pragma unroll
    for (int j = 0; j < 2; ++j) {
      const float nm = st.negm[2 + j][0], ll = st.l[2 + j][0];
      stat_own[j * 64 + lane] = f32x2{nm, ll};
#pragma unroll
      for (int s = 0; s < 4; ++s) {
        const f32x4 a = st.o[s][0][2 + j], bq = st.o[s][1][2 + j];
        *(LAS bf16x8*)(scr_own + (j * 4 + s) * 1024) = pack8(a, bq);
      }
    }
    wg_barrier();
#pragma unroll
    for (int j = 0; j < 2; ++j) {
      const f32x2 sp = stat_par[j * 64 + lane];
      const float nm = st.negm[j][0], ll = st.l[j][0];
      const float nM = fminf(nm, sp[0]);
      const float fa = __builtin_amdgcn_exp2f(nM - nm), fb = __builtin_amdgcn_exp2f(nM - sp[0]);
      const float inv = 1.0f / (fa * ll + fb * sp[1]);
      const float ga = fa * inv, gb = fb * inv;
#pragma unroll
      for (int s = 0; s < 4; ++s) {
        const u32x4 in = *(LAS u32x4*)(scr_par + (j * 4 + s) * 1024);
        f32x4 a = st.o[s][0][j], bq = st.o[s][1][j];
        a[0] = a[0] * ga + bflo(in[0]) * gb; a[1] = a[1] * ga + bfhi(in[0]) * gb; a[2] = a[2] * ga + bflo(in[1]) * gb; a[3] = a[3] * ga + bfhi(in[1]) * gb;
        bq[0] = bq[0] * ga + bflo(in[2]) * gb; bq[1] = bq[1] * ga + bfhi(in[2]) * gb; bq[2] = bq[2] * ga + bflo(in[3]) * gb; bq[3] = bq[3] * ga + bfhi(in[3]) * gb;
        *(LAS bf16x8*)(scr_par + (j * 4 + s) * 1024) = pack8(a, bq);
      }
    }
    wg_barrier();
  }
  ISSUE_O(3, 3);
  STAMP(4);
#define READ_O(PP) _Pragma("unroll") for (int s = 0; s < 4; ++s) _Pragma("unroll") for (int qb = 0; qb < 4; ++qb) \
    of[s][qb] = *(LAS bf16x8*)((LAS char*)smem + SCR0 + ((2 * (PP) + (qb >> 1)) ^ 1) * SLOT + ((qb & 1) * 4 + s) * 1024 + lane * 16);
  f32x4 acc[4][2][4];
#define ZERO_ACC() _Pragma("unroll") for (int s = 0; s < 4; ++s) _Pragma("unroll") for (int u = 0; u < 2; ++u) _Pragma("unroll") for (int qb = 0; qb < 4; ++qb) acc[s][u][qb] = f32x4{0.f, 0.f, 0.f, 0.f};
  ZERO_ACC();
  READ_O(0);
  wait_vm<24>(); gemm_slot<0>(acc, va, of[0]); ISSUE_O(4, 0);
  wait_vm<24>(); gemm_slot<1>(acc, va, of[1]); ISSUE_O(5, 1);
  wait_vm<24>(); gemm_slot<2>(acc, va, of[2]); ISSUE_O(6, 2);
  wait_vm<24>(); gemm_slot<3>(acc, va, of[3]); ISSUE_O(7, 3);
  READ_O(1);
  constexpr int NMEM = 16;
  wait_vm<24>(); gemm_slot<0>(acc, va, of[0]);
  wait_vm<16>(); gemm_slot<1>(acc, va, of[1]);
  wait_vm<8>(); gemm_slot<2>(acc, va, of[2]);
  wait_vm<0>(); gemm_slot<3>(acc, va, of[3]);
  STAMP(5);
  const rsrc_t rp = RSRC(p.ws + WS_PART + (size_t)grp * NMEM * 65536, NMEM * 65536);
  const int nblk = w;
#pragma unroll
  for (int qb = 0; qb < 4; ++qb)
#pragma unroll
    for (int s = 0; s < 4; ++s)
      __builtin_amdgcn_raw_buffer_store_b128(__builtin_bit_cast(u32x4, pack8(acc[s][0][qb], acc[s][1][qb])), rp, lane * 16 + (qb * 4 + s) * 1024, mem * 65536 + nblk * 16384, 16);
  asm volatile("s_waitcnt vmcnt(0)" ::: "memory");
  wg_barrier();
  GAS u64* const flags = (GAS u64*)(p.ws + WS_FLAG);
  const int fgrp = egrp * NMEM;
  if (tid == 0) __hip_atomic_store(flags + (fgrp + mem) * 16, tag, __ATOMIC_RELAXED, __HIP_MEMORY_SCOPE_AGENT);
  STAMP(6);
  if (w == 0) {
    for (int spin = 0; spin < (1 << 18); ++spin) {
      u64 v = tag;
      if (lane < NMEM) v = __hip_atomic_load(flags + (fgrp + lane) * 16, __ATOMIC_RELAXED, __HIP_MEMORY_SCOPE_AGENT);
      if (__all(v == tag)) break;
      __builtin_amdgcn_s_sleep(1);
    }
    __builtin_amdgcn_fence(__ATOMIC_ACQUIRE, "wavefront");
    if (mem == 0 && lane == 0) __hip_atomic_store((GAS u64*)(p.ws + WS_EPOCH + egrp * 128), tag, __ATOMIC_RELAXED, __HIP_MEMORY_SCOPE_AGENT);
  }
  wg_barrier();
  STAMP(7);
  {
    const int blk = mem * 4 + w;
    u32x4 v[NMEM];
#pragma unroll
    for (int q = 0; q < NMEM; ++q) v[q] = __builtin_amdgcn_raw_buffer_load_b128(rp, lane * 16, q * 65536 + blk * 1024, 16);
    f32x4 a = {0.f, 0.f, 0.f, 0.f}, bq = {0.f, 0.f, 0.f, 0.f};
#pragma unroll
    for (int q = 0; q < NMEM; ++q) {
      a[0] += bflo(v[q][0]); a[1] += bfhi(v[q][0]); a[2] += bflo(v[q][1]); a[3] += bfhi(v[q][1]);
      bq[0] += bflo(v[q][2]); bq[1] += bfhi(v[q][2]); bq[2] += bflo(v[q][3]); bq[3] += bfhi(v[q][3]);
    }
    const int n = blk >> 4, qb = (blk >> 2) & 3, s = blk & 3;
    *(bf16x8*)(p.out + ((size_t)(b * 64 + 16 * qb + c) * 1024 + br * 512 + 128 * n + 32 * s + 8 * g) * 2) = pack8(a, bq);
  }
}

extern "C" void kernel_launch(void* const* d_in, const int* in_sizes, int n_in, void* d_out, int out_size, void* d_ws, size_t ws_size, hipStream_t stream) {
  Params p;
  for (int br = 0; br < 2; ++br) {
    p.x[br] = (const char*)d_in[5 * br + 0]; p.k[br] = (const char*)d_in[5 * br + 1]; p.v[br] = (const char*)d_in[5 * br + 2];
    p.wq[br] = (const char*)d_in[5 * br + 3]; p.wo[br] = (const char*)d_in[5 * br + 4];
  }
  p.out = (char*)d_out; p.ws = (char*)d_ws;
  (void)hipFuncSetAttribute((const void*)fused_attn, hipFuncAttributeMaxDynamicSharedMemorySize, LDS_BYTES);
  hipLaunchKernelGGL(fused_attn, dim3(256), dim3(256), LDS_BYTES, stream, p);
}
```

```cpp
#include <hip/hip_runtime.h>
#include <hip/hip_bf16.h>
#include <stdint.h>

typedef __bf16 bf16x2 __attribute__((ext_vector_type(2)));
typedef float f32x4 __attribute__((ext_vector_type(4)));
typedef float f32x2 __attribute__((ext_vector_type(2)));
typedef unsigned u32x4 __attribute__((ext_vector_type(4)));
typedef unsigned long long u64;
#define LAS __attribute__((address_space(3)))
#define GAS __attribute__((address_space(1)))
#define DI __device__ __forceinline__
#define RSRC(p, n) __builtin_amdgcn_make_buffer_rsrc((void*)(p), 0, (n), 0x00020000)
typedef __amdgpu_buffer_rsrc_t rsrc_t;

constexpr int SLOT = 8192, RINGB = 65536, SPARE = 131072, LDS_BYTES = 163840;
constexpr size_t WS_FLAG = 0, WS_SIG = 32768, WS_EPOCH = 65536, WS_PART = 1u << 20;

struct Params {
  const char* x[2]; const char* k[2]; const char* v[2]; const char* wq[2]; const char* wo[2];
  char* out; char* ws;
};

DI unsigned cvtpk(float lo, float hi) { f32x2 v = {lo, hi}; return __builtin_bit_cast(unsigned, __builtin_convertvector(v, bf16x2)); }
DI float bflo(unsigned u) { return __builtin_bit_cast(float, u << 16); }
DI float bfhi(unsigned u) { return __builtin_bit_cast(float, u & 0xffff0000u); }
DI int lane_id() { int l; asm volatile("v_mbcnt_lo_u32_b32 %0, -1, 0\n\tv_mbcnt_hi_u32_b32 %0, -1, %0" : "=v"(l)); return l; }
#define HEAD_ASM \
  "s_add_u32 m0, %[mz], 0\n\t" \
  "s_add_u32 %[st], %[sx0], 0\n\t" \
  "buffer_load_dwordx4 %[xv0], %[rx], %[st] offen offset:0 lds\n\t" \
  "buffer_load_dwordx4 %[xv1], %[rx], %[st] offen offset:1024 lds\n\t" \
  "buffer_load_dwordx4 %[xv2], %[rx], %[st] offen offset:2048 lds\n\t" \
  "buffer_load_dwordx4 %[xv3], %[rx], %[st] offen offset:3072 lds\n\t" \
  "s_add_u32 m0, %[mz], 4096\n\t" \
  "s_add_u32 %[st], %[sx0], 16384\n\t" \
  "buffer_load_dwordx4 %[xv0], %[rx], %[st] offen offset:0 lds\n\t" \
  "buffer_load_dwordx4 %[xv1], %[rx], %[st] offen offset:1024 lds\n\t" \
  "buffer_load_dwordx4 %[xv2], %[rx], %[st] offen offset:2048 lds\n\t" \
  "buffer_load_dwordx4 %[xv3], %[rx], %[st] offen offset:3072 lds\n\t" \
  "s_add_u32 m0, %[mw], 0\n\t" \
  "s_add_u32 %[st], %[wa], %[wb]\n\t" \
  "buffer_load_dwordx4 %[wv0], %[rq], %[st] offen offset:0 lds\n\t" \
  "buffer_load_dwordx4 %[wv1], %[rq], %[st] offen offset:1024 lds\n\t" \
  "buffer_load_dwordx4 %[wv2], %[rq], %[st] offen offset:2048 lds\n\t" \
  "buffer_load_dwordx4 %[wv3], %[rq], %[st] offen offset:3072 lds\n\t"
#define BODY_A \
  "s_add_u32 %[st], %[wa], %[wb]\n\t" \
  "s_add_u32 m0, %[mw], 4096\n\t" \
  "s_add_u32 %[st], %[st], 0x20000\n\t" \
  "buffer_load_dwordx4 %[wv0], %[rq], %[st] offen offset:0 lds\n\t" \
  "buffer_load_dwordx4 %[wv1], %[rq], %[st] offen offset:1024 lds\n\t" \
  "buffer_load_dwordx4 %[wv2], %[rq], %[st] offen offset:2048 lds\n\t" \
  "buffer_load_dwordx4 %[wv3], %[rq], %[st] offen offset:3072 lds\n\t" \
  "s_add_u32 %[wb], %[wb], 0x40000\n\t" \
  "s_and_b32 %[wb], %[wb], 0xfffff\n\t" \
  "s_add_u32 m0, %[mw], 8192\n\t" \
  "s_add_u32 %[st], %[wa], %[wb]\n\t" \
  "buffer_load_dwordx4 %[wv0], %[rq], %[st] offen offset:0 lds\n\t" \
  "buffer_load_dwordx4 %[wv1], %[rq], %[st] offen offset:1024 lds\n\t" \
  "buffer_load_dwordx4 %[wv2], %[rq], %[st] offen offset:2048 lds\n\t" \
  "buffer_load_dwordx4 %[wv3], %[rq], %[st] offen offset:3072 lds\n\t" \
  "s_add_u32 m0, %[mw], 12288\n\t" \
  "s_add_u32 %[st], %[st], 0x20000\n\t" \
  "buffer_load_dwordx4 %[wv0], %[rq], %[st] offen offset:0 lds\n\t" \
  "buffer_load_dwordx4 %[wv1], %[rq], %[st] offen offset:1024 lds\n\t" \
  "buffer_load_dwordx4 %[wv2], %[rq], %[st] offen offset:2048 lds\n\t" \
  "buffer_load_dwordx4 %[wv3], %[rq], %[st] offen offset:3072 lds\n\t" \
  "s_add_u32 m0, %[mw], 16384\n\t" \
  "s_add_u32 %[st], %[sz], 0x1000\n\t" \
  "buffer_load_dwordx4 %[zv], %[ro], %[sz] offen offset:0 lds\n\t" \
  "buffer_load_dwordx4 %[zv], %[ro], %[sz] offen offset:1024 lds\n\t" \
  "buffer_load_dwordx4 %[zv], %[ro], %[sz] offen offset:2048 lds\n\t" \
  "buffer_load_dwordx4 %[zv], %[ro], %[sz] offen offset:3072 lds\n\t" \
  "buffer_load_dwordx4 %[zv], %[ro], %[st] offen offset:0 lds\n\t" \
  "buffer_load_dwordx4 %[zv], %[ro], %[st] offen offset:1024 lds\n\t" \
  "buffer_load_dwordx4 %[zv], %[ro], %[st] offen offset:2048 lds\n\t" \
  "buffer_load_dwordx4 %[zv], %[ro], %[st] offen offset:3072 lds\n\t" \
  "buffer_load_dwordx4 %[zv], %[rz], %[sz] offen offset:0 lds\n\t" \
  "buffer_load_dwordx4 %[zv], %[rz], %[sz] offen offset:1024 lds\n\t" \
  "buffer_load_dwordx4 %[zv], %[rz], %[sz] offen offset:2048 lds\n\t" \
  "buffer_load_dwordx4 %[zv], %[rz], %[sz] offen offset:3072 lds\n\t" \
  "buffer_load_dwordx4 %[zv], %[rz], %[st] offen offset:0 lds\n\t" \
  "buffer_load_dwordx4 %[zv], %[rz], %[st] offen offset:1024 lds\n\t" \
  "buffer_load_dwordx4 %[zv], %[rz], %[st] offen offset:2048 lds\n\t" \
  "buffer_load_dwordx4 %[zv], %[rz], %[st] offen offset:3072 lds\n\t" \
  "s_add_u32 %[wb], %[wb], 0x40000\n\t" \
  "s_and_b32 %[wb], %[wb], 0xfffff\n\t" \
  "s_add_u32 m0, %[mw], 16384\n\t" \
  "s_add_u32 %[st], %[wa], %[wb]\n\t" \
  "buffer_load_dwordx4 %[wv0], %[rq], %[st] offen offset:0 lds\n\t" \
  "buffer_load_dwordx4 %[wv1], %[rq], %[st] offen offset:1024 lds\n\t" \
  "buffer_load_dwordx4 %[wv2], %[rq], %[st] offen offset:2048 lds\n\t" \
  "buffer_load_dwordx4 %[wv3], %[rq], %[st] offen offset:3072 lds\n\t" \
  "s_add_u32 m0, %[mw], 20480\n\t" \
  "s_add_u32 %[st], %[st], 0x20000\n\t" \
  "buffer_load_dwordx4 %[wv0], %[rq], %[st] offen offset:0 lds\n\t" \
  "buffer_load_dwordx4 %[wv1], %[rq], %[st] offen offset:1024 lds\n\t" \
  "buffer_load_dwordx4 %[wv2], %[rq], %[st] offen offset:2048 lds\n\t" \
  "buffer_load_dwordx4 %[wv3], %[rq], %[st] offen offset:3072 lds\n\t" \
  "buffer_load_dwordx2 v[184:185], off, %[re], 0 sc1\n\t" \
  "v_mov_b32 v64, 0\n\t" \
  "v_mov_b32 v65, 0\n\t" \
  "v_mov_b32 v66, 0\n\t" \
  "v_mov_b32 v67, 0\n\t" \
  "v_mov_b32 v68, 0\n\t" \
  "v_mov_b32 v69, 0\n\t" \
  "v_mov_b32 v70, 0\n\t" \
  "v_mov_b32 v71, 0\n\t" \
  "v_mov_b32 v72, 0\n\t" \
  "v_mov_b32 v73, 0\n\t" \
  "v_mov_b32 v74, 0\n\t" \
  "v_mov_b32 v75, 0\n\t" \
  "v_mov_b32 v76, 0\n\t" \
  "v_mov_b32 v77, 0\n\t" \
  "v_mov_b32 v78, 0\n\t" \
  "v_mov_b32 v79, 0\n\t" \
  "v_mov_b32 v80, 0\n\t" \
  "v_mov_b32 v81, 0\n\t" \
  "v_mov_b32 v82, 0\n\t" \
  "v_mov_b32 v83, 0\n\t" \
  "v_mov_b32 v84, 0\n\t" \
  "v_mov_b32 v85, 0\n\t" \
  "v_mov_b32 v86, 0\n\t" \
  "v_mov_b32 v87, 0\n\t" \
  "v_mov_b32 v88, 0\n\t" \
  "v_mov_b32 v89, 0\n\t" \
  "v_mov_b32 v90, 0\n\t" \
  "v_mov_b32 v91, 0\n\t" \
  "v_mov_b32 v92, 0\n\t" \
  "v_mov_b32 v93, 0\n\t" \
  "v_mov_b32 v94, 0\n\t" \
  "v_mov_b32 v95, 0\n\t" \
  "v_mov_b32 v96, 0\n\t" \
  "v_mov_b32 v97, 0\n\t" \
  "v_mov_b32 v98, 0\n\t" \
  "v_mov_b32 v99, 0\n\t" \
  "v_mov_b32 v100, 0\n\t" \
  "v_mov_b32 v101, 0\n\t" \
  "v_mov_b32 v102, 0\n\t" \
  "v_mov_b32 v103, 0\n\t" \
  "v_mov_b32 v104, 0\n\t" \
  "v_mov_b32 v105, 0\n\t" \
  "v_mov_b32 v106, 0\n\t" \
  "v_mov_b32 v107, 0\n\t" \
  "v_mov_b32 v108, 0\n\t" \
  "v_mov_b32 v109, 0\n\t" \
  "v_mov_b32 v110, 0\n\t" \
  "v_mov_b32 v111, 0\n\t" \
  "v_mov_b32 v112, 0\n\t" \
  "v_mov_b32 v113, 0\n\t" \
  "v_mov_b32 v114, 0\n\t" \
  "v_mov_b32 v115, 0\n\t" \
  "v_mov_b32 v116, 0\n\t" \
  "v_mov_b32 v117, 0\n\t" \
  "v_mov_b32 v118, 0\n\t" \
  "v_mov_b32 v119, 0\n\t" \
  "v_mov_b32 v120, 0\n\t" \
  "v_mov_b32 v121, 0\n\t" \
  "v_mov_b32 v122, 0\n\t" \
  "v_mov_b32 v123, 0\n\t" \
  "v_mov_b32 v124, 0\n\t" \
  "v_mov_b32 v125, 0\n\t" \
  "v_mov_b32 v126, 0\n\t" \
  "v_mov_b32 v127, 0\n\t" \
  "v_mov_b32 v128, 0\n\t" \
  "v_mov_b32 v129, 0\n\t" \
  "v_mov_b32 v130, 0\n\t" \
  "v_mov_b32 v131, 0\n\t" \
  "v_mov_b32 v132, 0\n\t" \
  "v_mov_b32 v133, 0\n\t" \
  "v_mov_b32 v134, 0\n\t" \
  "v_mov_b32 v135, 0\n\t" \
  "s_waitcnt vmcnt(16)\n\t" \
  "s_barrier\n\t" \
  "ds_read_b64_tr_b16 a[0:1], %[va0] offset:0\n\t" \
  "ds_read_b64_tr_b16 a[2:3], %[va0] offset:1024\n\t" \
  "ds_read_b64_tr_b16 a[4:5], %[va1] offset:0\n\t" \
  "ds_read_b64_tr_b16 a[6:7], %[va1] offset:1024\n\t" \
  "ds_read_b64_tr_b16 a[8:9], %[va2] offset:0\n\t" \
  "ds_read_b64_tr_b16 a[10:11], %[va2] offset:1024\n\t" \
  "ds_read_b64_tr_b16 a[12:13], %[va3] offset:0\n\t" \
  "ds_read_b64_tr_b16 a[14:15], %[va3] offset:1024\n\t" \
  "ds_read_b64_tr_b16 a[16:17], %[va4] offset:0\n\t" \
  "ds_read_b64_tr_b16 a[18:19], %[va4] offset:1024\n\t" \
  "ds_read_b64_tr_b16 a[20:21], %[va5] offset:0\n\t" \
  "ds_read_b64_tr_b16 a[22:23], %[va5] offset:1024\n\t" \
  "ds_read_b64_tr_b16 a[24:25], %[va6] offset:0\n\t" \
  "ds_read_b64_tr_b16 a[26:27], %[va6] offset:1024\n\t" \
  "ds_read_b64_tr_b16 a[28:29], %[va7] offset:0\n\t" \
  "ds_read_b64_tr_b16 a[30:31], %[va7] offset:1024\n\t" \
  "ds_read_b128 a[128:131], %[xa0] offset:0\n\t" \
  "ds_read_b128 a[132:135], %[xa0] offset:4096\n\t" \
  "ds_read_b128 a[136:139], %[xa1] offset:0\n\t" \
  "ds_read_b128 a[140:143], %[xa1] offset:4096\n\t" \
  "ds_read_b128 a[144:147], %[xa2] offset:0\n\t" \
  "ds_read_b128 a[148:151], %[xa2] offset:4096\n\t" \
  "ds_read_b128 a[152:155], %[xa3] offset:0\n\t" \
  "ds_read_b128 a[156:159], %[xa3] offset:4096\n\t" \
  "ds_read_b128 a[160:163], %[xa0] offset:8192\n\t" \
  "ds_read_b128 a[164:167], %[xa0] offset:12288\n\t" \
  "ds_read_b128 a[168:171], %[xa1] offset:8192\n\t" \
  "ds_read_b128 a[172:175], %[xa1] offset:12288\n\t" \
  "ds_read_b128 a[176:179], %[xa2] offset:8192\n\t" \
  "ds_read_b128 a[180:183], %[xa2] offset:12288\n\t" \
  "ds_read_b128 a[184:187], %[xa3] offset:8192\n\t" \
  "ds_read_b128 a[188:191], %[xa3] offset:12288\n\t" \
  "ds_read_b128 a[192:195], %[xa0] offset:16384\n\t" \
  "ds_read_b128 a[196:199], %[xa0] offset:20480\n\t" \
  "ds_read_b128 a[200:203], %[xa1] offset:16384\n\t" \
  "ds_read_b128 a[204:207], %[xa1] offset:20480\n\t" \
  "ds_read_b128 a[208:211], %[xa2] offset:16384\n\t" \
  "ds_read_b128 a[212:215], %[xa2] offset:20480\n\t" \
  "ds_read_b128 a[216:219], %[xa3] offset:16384\n\t" \
  "ds_read_b128 a[220:223], %[xa3] offset:20480\n\t" \
  "ds_read_b128 a[224:227], %[xa0] offset:24576\n\t" \
  "ds_read_b128 a[228:231], %[xa0] offset:28672\n\t" \
  "ds_read_b128 a[232:235], %[xa1] offset:24576\n\t" \
  "ds_read_b128 a[236:239], %[xa1] offset:28672\n\t" \
  "ds_read_b128 a[240:243], %[xa2] offset:24576\n\t" \
  "ds_read_b128 a[244:247], %[xa2] offset:28672\n\t" \
  "ds_read_b128 a[248:251], %[xa3] offset:24576\n\t" \
  "ds_read_b128 a[252:255], %[xa3] offset:28672\n\t" \
  "s_waitcnt lgkmcnt(0)\n\t" \
  "s_barrier\n\t" \
  "s_add_u32 %[wb], %[wb], 0x40000\n\t" \
  "s_and_b32 %[wb], %[wb], 0xfffff\n\t" \
  "s_add_u32 m0, %[mw], 0\n\t" \
  "s_add_u32 %[st], %[wa], %[wb]\n\t" \
  "buffer_load_dwordx4 %[wv0], %[rq], %[st] offen offset:0 lds\n\t" \
  "buffer_load_dwordx4 %[wv1], %[rq], %[st] offen offset:1024 lds\n\t" \
  "buffer_load_dwordx4 %[wv2], %[rq], %[st] offen offset:2048 lds\n\t" \
  "buffer_load_dwordx4 %[wv3], %[rq], %[st] offen offset:3072 lds\n\t" \
  "s_add_u32 m0, %[mw], 4096\n\t" \
  "s_add_u32 %[st], %[st], 0x20000\n\t" \
  "buffer_load_dwordx4 %[wv0], %[rq], %[st] offen offset:0 lds\n\t" \
  "buffer_load_dwordx4 %[wv1], %[rq], %[st] offen offset:1024 lds\n\t" \
  "buffer_load_dwordx4 %[wv2], %[rq], %[st] offen offset:2048 lds\n\t" \
  "buffer_load_dwordx4 %[wv3], %[rq], %[st] offen offset:3072 lds\n\t" \
  "s_waitcnt vmcnt(16)\n\t" \
  "v_mfma_f32_16x16x32_bf16 v[192:195], a[0:3], a[128:131], 0\n\t" \
  "ds_read_b64_tr_b16 a[32:33], %[va0] offset:8192\n\t" \
  "ds_read_b64_tr_b16 a[34:35], %[va0] offset:9216\n\t" \
  "v_mfma_f32_16x16x32_bf16 v[196:199], a[0:3], a[132:135], 0\n\t" \
  "ds_read_b64_tr_b16 a[36:37], %[va1] offset:8192\n\t" \
  "v_mfma_f32_16x16x32_bf16 v[200:203], a[4:7], a[128:131], 0\n\t" \
  "ds_read_b64_tr_b16 a[38:39], %[va1] offset:9216\n\t" \
  "ds_read_b64_tr_b16 a[40:41], %[va2] offset:8192\n\t" \
  "v_mfma_f32_16x16x32_bf16 v[204:207], a[4:7], a[132:135], 0\n\t" \
  "ds_read_b64_tr_b16 a[42:43], %[va2] offset:9216\n\t" \
  "v_mfma_f32_16x16x32_bf16 v[208:211], a[8:11], a[128:131], 0\n\t" \
  "ds_read_b64_tr_b16 a[44:45], %[va3] offset:8192\n\t" \
  "ds_read_b64_tr_b16 a[46:47], %[va3] offset:9216\n\t" \
  "v_mfma_f32_16x16x32_bf16 v[212:215], a[8:11], a[132:135], 0\n\t" \
  "ds_read_b64_tr_b16 a[48:49], %[va4] offset:8192\n\t" \
  "v_mfma_f32_16x16x32_bf16 v[216:219], a[12:15], a[128:131], 0\n\t" \
  "ds_read_b64_tr_b16 a[50:51], %[va4] offset:9216\n\t" \
  "ds_read_b64_tr_b16 a[52:53], %[va5] offset:8192\n\t" \
  "v_mfma_f32_16x16x32_bf16 v[220:223], a[12:15], a[132:135], 0\n\t" \
  "ds_read_b64_tr_b16 a[54:55], %[va5] offset:9216\n\t" \
  "v_mfma_f32_16x16x32_bf16 v[224:227], a[16:19], a[128:131], 0\n\t" \
  "ds_read_b64_tr_b16 a[56:57], %[va6] offset:8192\n\t" \
  "ds_read_b64_tr_b16 a[58:59], %[va6] offset:9216\n\t" \
  "v_mfma_f32_16x16x32_bf16 v[228:231], a[16:19], a[132:135], 0\n\t" \
  "ds_read_b64_tr_b16 a[60:61], %[va7] offset:8192\n\t" \
  "ds_read_b64_tr_b16 a[62:63], %[va7] offset:9216\n\t" \
  "v_mfma_f32_16x16x32_bf16 v[232:235], a[20:23], a[128:131], 0\n\t" \
  "v_mfma_f32_16x16x32_bf16 v[236:239], a[20:23], a[132:135], 0\n\t" \
  "v_mfma_f32_16x16x32_bf16 v[240:243], a[24:27], a[128:131], 0\n\t" \
  "s_waitcnt lgkmcnt(0)\n\t" \
  "s_add_u32 %[wb], %[wb], 0x40000\n\t" \
  "s_and_b32 %[wb], %[wb], 0xfffff\n\t" \
  "s_add_u32 %[wa], %[wa], 0x100000\n\t" \
  "s_and_b32 %[wa], %[wa], 0x3fffff\n\t" \
  "s_add_u32 m0, %[mw], 8192\n\t" \
  "s_add_u32 %[st], %[wa], %[wb]\n\t" \
  "buffer_load_dwordx4 %[wv0], %[rq], %[st] offen offset:0 lds\n\t" \
  "buffer_load_dwordx4 %[wv1], %[rq], %[st] offen offset:1024 lds\n\t" \
  "buffer_load_dwordx4 %[wv2], %[rq], %[st] offen offset:2048 lds\n\t" \
  "buffer_load_dwordx4 %[wv3], %[rq], %[st] offen offset:3072 lds\n\t" \
  "v_mfma_f32_16x16x32_bf16 v[244:247], a[24:27], a[132:135], 0\n\t" \
  "s_add_u32 m0, %[mw], 12288\n\t" \
  "s_add_u32 %[st], %[st], 0x20000\n\t" \
  "buffer_load_dwordx4 %[wv0], %[rq], %[st] offen offset:0 lds\n\t" \
  "buffer_load_dwordx4 %[wv1], %[rq], %[st] offen offset:1024 lds\n\t" \
  "buffer_load_dwordx4 %[wv2], %[rq], %[st] offen offset:2048 lds\n\t" \
  "buffer_load_dwordx4 %[wv3], %[rq], %[st] offen offset:3072 lds\n\t" \
  "s_add_u32 m0, %[mkv], 0\n\t" \
  "s_add_u32 %[sk], %[sk], 0x2000\n\t" \
  "buffer_load_dwordx4 %[kv0], %[rk], %[sk] offen offset:0 nt lds\n\t" \
  "buffer_load_dwordx4 %[kv0], %[rk], %[sk] offen offset:1024 nt lds\n\t" \
  "v_mfma_f32_16x16x32_bf16 v[248:251], a[28:31], a[128:131], 0\n\t" \
  "s_add_u32 m0, %[mkv], 16384\n\t" \
  "s_add_u32 %[sk], %[sk], 0x2000\n\t" \
  "buffer_load_dwordx4 %[kv0], %[rk], %[sk] offen offset:0 nt lds\n\t" \
  "buffer_load_dwordx4 %[kv0], %[rk], %[sk] offen offset:1024 nt lds\n\t" \
  "s_add_u32 m0, %[mkv], 24576\n\t" \
  "s_add_u32 %[sv], %[sv], 0x2000\n\t" \
  "buffer_load_dwordx4 %[vv0], %[rv], %[sv] offen offset:0 nt lds\n\t" \
  "buffer_load_dwordx4 %[vv0], %[rv], %[sv] offen offset:1024 nt lds\n\t" \
  "v_mfma_f32_16x16x32_bf16 v[252:255], a[28:31], a[132:135], 0\n\t" \
  "s_waitcnt vmcnt(22)\n\t" \
  "v_mfma_f32_16x16x32_bf16 v[192:195], a[32:35], a[136:139], v[192:195]\n\t" \
  "ds_read_b64_tr_b16 a[0:1], %[va0] offset:16384\n\t" \
  "ds_read_b64_tr_b16 a[2:3], %[va0] offset:17408\n\t" \
  "v_mfma_f32_16x16x32_bf16 v[196:199], a[32:35], a[140:143], v[196:199]\n\t" \
  "ds_read_b64_tr_b16 a[4:5], %[va1] offset:16384\n\t" \
  "v_mfma_f32_16x16x32_bf16 v[200:203], a[36:39], a[136:139], v[200:203]\n\t" \
  "ds_read_b64_tr_b16 a[6:7], %[va1] offset:17408\n\t" \
  "ds_read_b64_tr_b16 a[8:9], %[va2] offset:16384\n\t" \
  "v_mfma_f32_16x16x32_bf16 v[204:207], a[36:39], a[140:143], v[204:207]\n\t" \
  "ds_read_b64_tr_b16 a[10:11], %[va2] offset:17408\n\t" \
  "v_mfma_f32_16x16x32_bf16 v[208:211], a[40:43], a[136:139], v[208:211]\n\t" \
  "ds_read_b64_tr_b16 a[12:13], %[va3] offset:16384\n\t" \
  "ds_read_b64_tr_b16 a[14:15], %[va3] offset:17408\n\t" \
  "v_mfma_f32_16x16x32_bf16 v[212:215], a[40:43], a[140:143], v[212:215]\n\t" \
  "ds_read_b64_tr_b16 a[16:17], %[va4] offset:16384\n\t" \
  "v_mfma_f32_16x16x32_bf16 v[216:219], a[44:47], a[136:139], v[216:219]\n\t" \
  "ds_read_b64_tr_b16 a[18:19], %[va4] offset:17408\n\t" \
  "ds_read_b64_tr_b16 a[20:21], %[va5] offset:16384\n\t" \
  "v_mfma_f32_16x16x32_bf16 v[220:223], a[44:47], a[140:143], v[220:223]\n\t" \
  "ds_read_b64_tr_b16 a[22:23], %[va5] offset:17408\n\t" \
  "v_mfma_f32_16x16x32_bf16 v[224:227], a[48:51], a[136:139], v[224:227]\n\t" \
  "ds_read_b64_tr_b16 a[24:25], %[va6] offset:16384\n\t" \
  "ds_read_b64_tr_b16 a[26:27], %[va6] offset:17408\n\t" \
  "v_mfma_f32_16x16x32_bf16 v[228:231], a[48:51], a[140:143], v[228:231]\n\t" \
  "ds_read_b64_tr_b16 a[28:29], %[va7] offset:16384\n\t" \
  "ds_read_b64_tr_b16 a[30:31], %[va7] offset:17408\n\t" \
  "v_mfma_f32_16x16x32_bf16 v[232:235], a[52:55], a[136:139], v[232:235]\n\t" \
  "v_mfma_f32_16x16x32_bf16 v[236:239], a[52:55], a[140:143], v[236:239]\n\t" \
  "v_mfma_f32_16x16x32_bf16 v[240:243], a[56:59], a[136:139], v[240:243]\n\t" \
  "s_waitcnt lgkmcnt(0)\n\t" \
  "s_add_u32 %[wb], %[wb], 0x40000\n\t" \
  "s_and_b32 %[wb], %[wb], 0xfffff\n\t" \
  "s_add_u32 m0, %[mw], 16384\n\t" \
  "s_add_u32 %[st], %[wa], %[wb]\n\t" \
  "buffer_load_dwordx4 %[wv0], %[rq], %[st] offen offset:0 lds\n\t" \
  "buffer_load_dwordx4 %[wv1], %[rq], %[st] offen offset:1024 lds\n\t" \
  "buffer_load_dwordx4 %[wv2], %[rq], %[st] offen offset:2048 lds\n\t" \
  "buffer_load_dwordx4 %[wv3], %[rq], %[st] offen offset:3072 lds\n\t" \
  "v_mfma_f32_16x16x32_bf16 v[244:247], a[56:59], a[140:143], v[244:247]\n\t" \
  "s_add_u32 m0, %[mw], 20480\n\t" \
  "s_add_u32 %[st], %[st], 0x20000\n\t" \
  "buffer_load_dwordx4 %[wv0], %[rq], %[st] offen offset:0 lds\n\t" \
  "buffer_load_dwordx4 %[wv1], %[rq], %[st] offen offset:1024 lds\n\t" \
  "buffer_load_dwordx4 %[wv2], %[rq], %[st] offen offset:2048 lds\n\t" \
  "buffer_load_dwordx4 %[wv3], %[rq], %[st] offen offset:3072 lds\n\t" \
  "s_add_u32 m0, %[mkv], 32768\n\t" \
  "s_add_u32 %[sk], %[sk], 0x2000\n\t" \
  "v_mfma_f32_16x16x32_bf16 v[248:251], a[60:63], a[136:139], v[248:251]\n\t" \
  "buffer_load_dwordx4 %[kv0], %[rk], %[sk] offen offset:0 nt lds\n\t" \
  "buffer_load_dwordx4 %[kv0], %[rk], %[sk] offen offset:1024 nt lds\n\t" \
  "s_add_u32 m0, %[mkv], 40960\n\t" \
  "s_add_u32 %[sv], %[sv], 0x2000\n\t" \
  "buffer_load_dwordx4 %[vv0], %[rv], %[sv] offen offset:0 nt lds\n\t" \
  "buffer_load_dwordx4 %[vv0], %[rv], %[sv] offen offset:1024 nt lds\n\t" \
  "v_mfma_f32_16x16x32_bf16 v[252:255], a[60:63], a[140:143], v[252:255]\n\t" \
  "s_waitcnt vmcnt(26)\n\t" \
  "v_mfma_f32_16x16x32_bf16 v[192:195], a[0:3], a[144:147], v[192:195]\n\t" \
  "ds_read_b64_tr_b16 a[32:33], %[va0] offset:0\n\t" \
  "ds_read_b64_tr_b16 a[34:35], %[va0] offset:1024\n\t" \
  "v_mfma_f32_16x16x32_bf16 v[196:199], a[0:3], a[148:151], v[196:199]\n\t" \
  "ds_read_b64_tr_b16 a[36:37], %[va1] offset:0\n\t" \
  "v_mfma_f32_16x16x32_bf16 v[200:203], a[4:7], a[144:147], v[200:203]\n\t" \
  "ds_read_b64_tr_b16 a[38:39], %[va1] offset:1024\n\t" \
  "ds_read_b64_tr_b16 a[40:41], %[va2] offset:0\n\t" \
  "v_mfma_f32_16x16x32_bf16 v[204:207], a[4:7], a[148:151], v[204:207]\n\t" \
  "ds_read_b64_tr_b16 a[42:43], %[va2] offset:1024\n\t" \
  "v_mfma_f32_16x16x32_bf16 v[208:211], a[8:11], a[144:147], v[208:211]\n\t" \
  "ds_read_b64_tr_b16 a[44:45], %[va3] offset:0\n\t" \
  "ds_read_b64_tr_b16 a[46:47], %[va3] offset:1024\n\t" \
  "v_mfma_f32_16x16x32_bf16 v[212:215], a[8:11], a[148:151], v[212:215]\n\t" \
  "ds_read_b64_tr_b16 a[48:49], %[va4] offset:0\n\t" \
  "v_mfma_f32_16x16x32_bf16 v[216:219], a[12:15], a[144:147], v[216:219]\n\t" \
  "ds_read_b64_tr_b16 a[50:51], %[va4] offset:1024\n\t" \
  "ds_read_b64_tr_b16 a[52:53], %[va5] offset:0\n\t" \
  "v_mfma_f32_16x16x32_bf16 v[220:223], a[12:15], a[148:151], v[220:223]\n\t" \
  "ds_read_b64_tr_b16 a[54:55], %[va5] offset:1024\n\t" \
  "v_mfma_f32_16x16x32_bf16 v[224:227], a[16:19], a[144:147], v[224:227]\n\t" \
  "ds_read_b64_tr_b16 a[56:57], %[va6] offset:0\n\t" \
  "ds_read_b64_tr_b16 a[58:59], %[va6] offset:1024\n\t" \
  "v_mfma_f32_16x16x32_bf16 v[228:231], a[16:19], a[148:151], v[228:231]\n\t" \
  "ds_read_b64_tr_b16 a[60:61], %[va7] offset:0\n\t" \
  "ds_read_b64_tr_b16 a[62:63], %[va7] offset:1024\n\t" \
  "v_mfma_f32_16x16x32_bf16 v[232:235], a[20:23], a[144:147], v[232:235]\n\t" \
  "v_mfma_f32_16x16x32_bf16 v[236:239], a[20:23], a[148:151], v[236:239]\n\t" \
  "v_mfma_f32_16x16x32_bf16 v[240:243], a[24:27], a[144:147], v[240:243]\n\t" \
  "s_waitcnt lgkmcnt(0)\n\t" \
  "s_add_u32 %[wb], %[wb], 0x40000\n\t" \
  "s_and_b32 %[wb], %[wb], 0xfffff\n\t" \
  "s_add_u32 m0, %[mw], 0\n\t" \
  "s_add_u32 %[st], %[wa], %[wb]\n\t" \
  "buffer_load_dwordx4 %[wv0], %[rq], %[st] offen offset:0 lds\n\t" \
  "buffer_load_dwordx4 %[wv1], %[rq], %[st] offen offset:1024 lds\n\t" \
  "buffer_load_dwordx4 %[wv2], %[rq], %[st] offen offset:2048 lds\n\t" \
  "buffer_load_dwordx4 %[wv3], %[rq], %[st] offen offset:3072 lds\n\t" \
  "v_mfma_f32_16x16x32_bf16 v[244:247], a[24:27], a[148:151], v[244:247]\n\t" \
  "s_add_u32 m0, %[mw], 4096\n\t" \
  "s_add_u32 %[st], %[st], 0x20000\n\t" \
  "buffer_load_dwordx4 %[wv0], %[rq], %[st] offen offset:0 lds\n\t" \
  "buffer_load_dwordx4 %[wv1], %[rq], %[st] offen offset:1024 lds\n\t" \
  "buffer_load_dwordx4 %[wv2], %[rq], %[st] offen offset:2048 lds\n\t" \
  "buffer_load_dwordx4 %[wv3], %[rq], %[st] offen offset:3072 lds\n\t" \
  "s_add_u32 m0, %[mkv], 49152\n\t" \
  "s_add_u32 %[sk], %[sk], 0x2000\n\t" \
  "v_mfma_f32_16x16x32_bf16 v[248:251], a[28:31], a[144:147], v[248:251]\n\t" \
  "buffer_load_dwordx4 %[kv0], %[rk], %[sk] offen offset:0 nt lds\n\t" \
  "buffer_load_dwordx4 %[kv0], %[rk], %[sk] offen offset:1024 nt lds\n\t" \
  "s_add_u32 m0, %[mkv], 57344\n\t" \
  "s_add_u32 %[sv], %[sv], 0x2000\n\t" \
  "buffer_load_dwordx4 %[vv0], %[rv], %[sv] offen offset:0 nt lds\n\t" \
  "buffer_load_dwordx4 %[vv0], %[rv], %[sv] offen offset:1024 nt lds\n\t" \
  "v_mfma_f32_16x16x32_bf16 v[252:255], a[28:31], a[148:151], v[252:255]\n\t" \
  "s_waitcnt vmcnt(30)\n\t" \
  "v_mfma_f32_16x16x32_bf16 v[192:195], a[32:35], a[152:155], v[192:195]\n\t" \
  "ds_read_b64_tr_b16 a[0:1], %[va0] offset:8192\n\t" \
  "ds_read_b64_tr_b16 a[2:3], %[va0] offset:9216\n\t" \
  "v_mfma_f32_16x16x32_bf16 v[196:199], a[32:35], a[156:159], v[196:199]\n\t" \
  "ds_read_b64_tr_b16 a[4:5], %[va1] offset:8192\n\t" \
  "v_mfma_f32_16x16x32_bf16 v[200:203], a[36:39], a[152:155], v[200:203]\n\t" \
  "ds_read_b64_tr_b16 a[6:7], %[va1] offset:9216\n\t" \
  "ds_read_b64_tr_b16 a[8:9], %[va2] offset:8192\n\t" \
  "v_mfma_f32_16x16x32_bf16 v[204:207], a[36:39], a[156:159], v[204:207]\n\t" \
  "ds_read_b64_tr_b16 a[10:11], %[va2] offset:9216\n\t" \
  "v_mfma_f32_16x16x32_bf16 v[208:211], a[40:43], a[152:155], v[208:211]\n\t" \
  "ds_read_b64_tr_b16 a[12:13], %[va3] offset:8192\n\t" \
  "ds_read_b64_tr_b16 a[14:15], %[va3] offset:9216\n\t" \
  "v_mfma_f32_16x16x32_bf16 v[212:215], a[40:43], a[156:159], v[212:215]\n\t" \
  "ds_read_b64_tr_b16 a[16:17], %[va4] offset:8192\n\t" \
  "v_mfma_f32_16x16x32_bf16 v[216:219], a[44:47], a[152:155], v[216:219]\n\t" \
  "ds_read_b64_tr_b16 a[18:19], %[va4] offset:9216\n\t" \
  "ds_read_b64_tr_b16 a[20:21], %[va5] offset:8192\n\t" \
  "v_mfma_f32_16x16x32_bf16 v[220:223], a[44:47], a[156:159], v[220:223]\n\t" \
  "ds_read_b64_tr_b16 a[22:23], %[va5] offset:9216\n\t" \
  "v_mfma_f32_16x16x32_bf16 v[224:227], a[48:51], a[152:155], v[224:227]\n\t" \
  "ds_read_b64_tr_b16 a[24:25], %[va6] offset:8192\n\t" \
  "ds_read_b64_tr_b16 a[26:27], %[va6] offset:9216\n\t" \
  "v_mfma_f32_16x16x32_bf16 v[228:231], a[48:51], a[156:159], v[228:231]\n\t" \
  "ds_read_b64_tr_b16 a[28:29], %[va7] offset:8192\n\t" \
  "ds_read_b64_tr_b16 a[30:31], %[va7] offset:9216\n\t" \
  "v_mfma_f32_16x16x32_bf16 v[232:235], a[52:55], a[152:155], v[232:235]\n\t" \
  "v_mfma_f32_16x16x32_bf16 v[236:239], a[52:55], a[156:159], v[236:239]\n\t" \
  "v_mfma_f32_16x16x32_bf16 v[240:243], a[56:59], a[152:155], v[240:243]\n\t" \
  "s_waitcnt lgkmcnt(0)\n\t" \
  "s_add_u32 %[wb], %[wb], 0x40000\n\t" \
  "s_and_b32 %[wb], %[wb], 0xfffff\n\t" \
  "s_add_u32 m0, %[mw], 8192\n\t" \
  "s_add_u32 %[st], %[wa], %[wb]\n\t" \
  "buffer_load_dwordx4 %[wv0], %[rq], %[st] offen offset:0 lds\n\t" \
  "v_mfma_f32_16x16x32_bf16 v[244:247], a[56:59], a[156:159], v[244:247]\n\t" \
  "buffer_load_dwordx4 %[wv1], %[rq], %[st] offen offset:1024 lds\n\t" \
  "buffer_load_dwordx4 %[wv2], %[rq], %[st] offen offset:2048 lds\n\t" \
  "buffer_load_dwordx4 %[wv3], %[rq], %[st] offen offset:3072 lds\n\t" \
  "s_add_u32 m0, %[mw], 12288\n\t" \
  "s_add_u32 %[st], %[st], 0x20000\n\t" \
  "v_mfma_f32_16x16x32_bf16 v[248:251], a[60:63], a[152:155], v[248:251]\n\t" \
  "buffer_load_dwordx4 %[wv0], %[rq], %[st] offen offset:0 lds\n\t" \
  "buffer_load_dwordx4 %[wv1], %[rq], %[st] offen offset:1024 lds\n\t" \
  "buffer_load_dwordx4 %[wv2], %[rq], %[st] offen offset:2048 lds\n\t" \
  "buffer_load_dwordx4 %[wv3], %[rq], %[st] offen offset:3072 lds\n\t" \
  "v_mfma_f32_16x16x32_bf16 v[252:255], a[60:63], a[156:159], v[252:255]\n\t" \
  "s_waitcnt vmcnt(24)\n\t" \
  "v_mfma_f32_16x16x32_bf16 v[192:195], a[0:3], a[160:163], v[192:195]\n\t" \
  "ds_read_b64_tr_b16 a[32:33], %[va0] offset:16384\n\t" \
  "ds_read_b64_tr_b16 a[34:35], %[va0] offset:17408\n\t" \
  "v_mfma_f32_16x16x32_bf16 v[196:199], a[0:3], a[164:167], v[196:199]\n\t" \
  "ds_read_b64_tr_b16 a[36:37], %[va1] offset:16384\n\t" \
  "v_mfma_f32_16x16x32_bf16 v[200:203], a[4:7], a[160:163], v[200:203]\n\t" \
  "ds_read_b64_tr_b16 a[38:39], %[va1] offset:17408\n\t" \
  "ds_read_b64_tr_b16 a[40:41], %[va2] offset:16384\n\t" \
  "v_mfma_f32_16x16x32_bf16 v[204:207], a[4:7], a[164:167], v[204:207]\n\t" \
  "ds_read_b64_tr_b16 a[42:43], %[va2] offset:17408\n\t" \
  "v_mfma_f32_16x16x32_bf16 v[208:211], a[8:11], a[160:163], v[208:211]\n\t" \
  "ds_read_b64_tr_b16 a[44:45], %[va3] offset:16384\n\t" \
  "ds_read_b64_tr_b16 a[46:47], %[va3] offset:17408\n\t" \
  "v_mfma_f32_16x16x32_bf16 v[212:215], a[8:11], a[164:167], v[212:215]\n\t" \
  "ds_read_b64_tr_b16 a[48:49], %[va4] offset:16384\n\t" \
  "v_mfma_f32_16x16x32_bf16 v[216:219], a[12:15], a[160:163], v[216:219]\n\t" \
  "ds_read_b64_tr_b16 a[50:51], %[va4] offset:17408\n\t" \
  "ds_read_b64_tr_b16 a[52:53], %[va5] offset:16384\n\t" \
  "v_mfma_f32_16x16x32_bf16 v[220:223], a[12:15], a[164:167], v[220:223]\n\t" \
  "ds_read_b64_tr_b16 a[54:55], %[va5] offset:17408\n\t" \
  "v_mfma_f32_16x16x32_bf16 v[224:227], a[16:19], a[160:163], v[224:227]\n\t" \
  "ds_read_b64_tr_b16 a[56:57], %[va6] offset:16384\n\t" \
  "ds_read_b64_tr_b16 a[58:59], %[va6] offset:17408\n\t" \
  "v_mfma_f32_16x16x32_bf16 v[228:231], a[16:19], a[164:167], v[228:231]\n\t" \
  "ds_read_b64_tr_b16 a[60:61], %[va7] offset:16384\n\t" \
  "ds_read_b64_tr_b16 a[62:63], %[va7] offset:17408\n\t" \
  "v_mfma_f32_16x16x32_bf16 v[232:235], a[20:23], a[160:163], v[232:235]\n\t" \
  "v_mfma_f32_16x16x32_bf16 v[236:239], a[20:23], a[164:167], v[236:239]\n\t" \
  "v_mfma_f32_16x16x32_bf16 v[240:243], a[24:27], a[160:163], v[240:243]\n\t" \
  "s_waitcnt lgkmcnt(0)\n\t" \
  "s_add_u32 %[wb], %[wb], 0x40000\n\t" \
  "s_and_b32 %[wb], %[wb], 0xfffff\n\t" \
  "s_add_u32 %[wa], %[wa], 0x100000\n\t" \
  "s_and_b32 %[wa], %[wa], 0x3fffff\n\t" \
  "s_add_u32 m0, %[mw], 16384\n\t" \
  "s_add_u32 %[st], %[wa], %[wb]\n\t" \
  "v_mfma_f32_16x16x32_bf16 v[244:247], a[24:27], a[164:167], v[244:247]\n\t" \
  "buffer_load_dwordx4 %[wv0], %[rq], %[st] offen offset:0 lds\n\t" \
  "buffer_load_dwordx4 %[wv1], %[rq], %[st] offen offset:1024 lds\n\t" \
  "buffer_load_dwordx4 %[wv2], %[rq], %[st] offen offset:2048 lds\n\t" \
  "buffer_load_dwordx4 %[wv3], %[rq], %[st] offen offset:3072 lds\n\t" \
  "s_add_u32 m0, %[mw], 20480\n\t" \
  "s_add_u32 %[st], %[st], 0x20000\n\t" \
  "v_mfma_f32_16x16x32_bf16 v[248:251], a[28:31], a[160:163], v[248:251]\n\t" \
  "buffer_load_dwordx4 %[wv0], %[rq], %[st] offen offset:0 lds\n\t" \
  "buffer_load_dwordx4 %[wv1], %[rq], %[st] offen offset:1024 lds\n\t" \
  "buffer_load_dwordx4 %[wv2], %[rq], %[st] offen offset:2048 lds\n\t" \
  "buffer_load_dwordx4 %[wv3], %[rq], %[st] offen offset:3072 lds\n\t" \
  "v_mfma_f32_16x16x32_bf16 v[252:255], a[28:31], a[164:167], v[252:255]\n\t" \
  "s_waitcnt vmcnt(20)\n\t" \
  "v_mfma_f32_16x16x32_bf16 v[192:195], a[32:35], a[168:171], v[192:195]\n\t" \
  "ds_read_b64_tr_b16 a[0:1], %[va0] offset:0\n\t" \
  "ds_read_b64_tr_b16 a[2:3], %[va0] offset:1024\n\t" \
  "v_mfma_f32_16x16x32_bf16 v[196:199], a[32:35], a[172:175], v[196:199]\n\t" \
  "ds_read_b64_tr_b16 a[4:5], %[va1] offset:0\n\t" \
  "v_mfma_f32_16x16x32_bf16 v[200:203], a[36:39], a[168:171], v[200:203]\n\t" \
  "ds_read_b64_tr_b16 a[6:7], %[va1] offset:1024\n\t" \
  "ds_read_b64_tr_b16 a[8:9], %[va2] offset:0\n\t" \
  "v_mfma_f32_16x16x32_bf16 v[204:207], a[36:39], a[172:175], v[204:207]\n\t" \
  "ds_read_b64_tr_b16 a[10:11], %[va2] offset:1024\n\t" \
  "v_mfma_f32_16x16x32_bf16 v[208:211], a[40:43], a[168:171], v[208:211]\n\t" \
  "ds_read_b64_tr_b16 a[12:13], %[va3] offset:0\n\t" \
  "ds_read_b64_tr_b16 a[14:15], %[va3] offset:1024\n\t" \
  "v_mfma_f32_16x16x32_bf16 v[212:215], a[40:43], a[172:175], v[212:215]\n\t" \
  "ds_read_b64_tr_b16 a[16:17], %[va4] offset:0\n\t" \
  "v_mfma_f32_16x16x32_bf16 v[216:219], a[44:47], a[168:171], v[216:219]\n\t" \
  "ds_read_b64_tr_b16 a[18:19], %[va4] offset:1024\n\t" \
  "ds_read_b64_tr_b16 a[20:21], %[va5] offset:0\n\t" \
  "v_mfma_f32_16x16x32_bf16 v[220:223], a[44:47], a[172:175], v[220:223]\n\t" \
  "ds_read_b64_tr_b16 a[22:23], %[va5] offset:1024\n\t" \
  "v_mfma_f32_16x16x32_bf16 v[224:227], a[48:51], a[168:171], v[224:227]\n\t" \
  "ds_read_b64_tr_b16 a[24:25], %[va6] offset:0\n\t" \
  "ds_read_b64_tr_b16 a[26:27], %[va6] offset:1024\n\t" \
  "v_mfma_f32_16x16x32_bf16 v[228:231], a[48:51], a[172:175], v[228:231]\n\t" \
  "ds_read_b64_tr_b16 a[28:29], %[va7] offset:0\n\t" \
  "ds_read_b64_tr_b16 a[30:31], %[va7] offset:1024\n\t" \
  "v_mfma_f32_16x16x32_bf16 v[232:235], a[52:55], a[168:171], v[232:235]\n\t" \
  "v_mfma_f32_16x16x32_bf16 v[236:239], a[52:55], a[172:175], v[236:239]\n\t" \
  "v_mfma_f32_16x16x32_bf16 v[240:243], a[56:59], a[168:171], v[240:243]\n\t" \
  "s_waitcnt lgkmcnt(0)\n\t" \
  "s_add_u32 %[wb], %[wb], 0x40000\n\t" \
  "s_and_b32 %[wb], %[wb], 0xfffff\n\t" \
  "s_add_u32 m0, %[mw], 0\n\t" \
  "s_add_u32 %[st], %[wa], %[wb]\n\t" \
  "buffer_load_dwordx4 %[wv0], %[rq], %[st] offen offset:0 lds\n\t" \
  "v_mfma_f32_16x16x32_bf16 v[244:247], a[56:59], a[172:175], v[244:247]\n\t" \
  "buffer_load_dwordx4 %[wv1], %[rq], %[st] offen offset:1024 lds\n\t" \
  "buffer_load_dwordx4 %[wv2], %[rq], %[st] offen offset:2048 lds\n\t" \
  "buffer_load_dwordx4 %[wv3], %[rq], %[st] offen offset:3072 lds\n\t" \
  "s_add_u32 m0, %[mw], 4096\n\t" \
  "s_add_u32 %[st], %[st], 0x20000\n\t" \
  "v_mfma_f32_16x16x32_bf16 v[248:251], a[60:63], a[168:171], v[248:251]\n\t" \
  "buffer_load_dwordx4 %[wv0], %[rq], %[st] offen offset:0 lds\n\t" \
  "buffer_load_dwordx4 %[wv1], %[rq], %[st] offen offset:1024 lds\n\t" \
  "buffer_load_dwordx4 %[wv2], %[rq], %[st] offen offset:2048 lds\n\t" \
  "buffer_load_dwordx4 %[wv3], %[rq], %[st] offen offset:3072 lds\n\t" \
  "v_mfma_f32_16x16x32_bf16 v[252:255], a[60:63], a[172:175], v[252:255]\n\t" \
  "s_waitcnt vmcnt(16)\n\t" \
  "v_mfma_f32_16x16x32_bf16 v[192:195], a[0:3], a[176:179], v[192:195]\n\t" \
  "ds_read_b64_tr_b16 a[32:33], %[va0] offset:8192\n\t" \
  "ds_read_b64_tr_b16 a[34:35], %[va0] offset:9216\n\t" \
  "v_mfma_f32_16x16x32_bf16 v[196:199], a[0:3], a[180:183], v[196:199]\n\t" \
  "ds_read_b64_tr_b16 a[36:37], %[va1] offset:8192\n\t" \
  "v_mfma_f32_16x16x32_bf16 v[200:203], a[4:7], a[176:179], v[200:203]\n\t" \
  "ds_read_b64_tr_b16 a[38:39], %[va1] offset:9216\n\t" \
  "ds_read_b64_tr_b16 a[40:41], %[va2] offset:8192\n\t" \
  "v_mfma_f32_16x16x32_bf16 v[204:207], a[4:7], a[180:183], v[204:207]\n\t" \
  "ds_read_b64_tr_b16 a[42:43], %[va2] offset:9216\n\t" \
  "v_mfma_f32_16x16x32_bf16 v[208:211], a[8:11], a[176:179], v[208:211]\n\t" \
  "ds_read_b64_tr_b16 a[44:45], %[va3] offset:8192\n\t" \
  "ds_read_b64_tr_b16 a[46:47], %[va3] offset:9216\n\t" \
  "v_mfma_f32_16x16x32_bf16 v[212:215], a[8:11], a[180:183], v[212:215]\n\t" \
  "ds_read_b64_tr_b16 a[48:49], %[va4] offset:8192\n\t" \
  "v_mfma_f32_16x16x32_bf16 v[216:219], a[12:15], a[176:179], v[216:219]\n\t" \
  "ds_read_b64_tr_b16 a[50:51], %[va4] offset:9216\n\t" \
  "ds_read_b64_tr_b16 a[52:53], %[va5] offset:8192\n\t" \
  "v_mfma_f32_16x16x32_bf16 v[220:223], a[12:15], a[180:183], v[220:223]\n\t" \
  "ds_read_b64_tr_b16 a[54:55], %[va5] offset:9216\n\t" \
  "v_mfma_f32_16x16x32_bf16 v[224:227], a[16:19], a[176:179], v[224:227]\n\t" \
  "ds_read_b64_tr_b16 a[56:57], %[va6] offset:8192\n\t" \
  "ds_read_b64_tr_b16 a[58:59], %[va6] offset:9216\n\t" \
  "v_mfma_f32_16x16x32_bf16 v[228:231], a[16:19], a[180:183], v[228:231]\n\t" \
  "ds_read_b64_tr_b16 a[60:61], %[va7] offset:8192\n\t" \
  "ds_read_b64_tr_b16 a[62:63], %[va7] offset:9216\n\t" \
  "v_mfma_f32_16x16x32_bf16 v[232:235], a[20:23], a[176:179], v[232:235]\n\t" \
  "v_mfma_f32_16x16x32_bf16 v[236:239], a[20:23], a[180:183], v[236:239]\n\t" \
  "v_mfma_f32_16x16x32_bf16 v[240:243], a[24:27], a[176:179], v[240:243]\n\t" \
  "s_waitcnt lgkmcnt(0)\n\t" \
  "s_add_u32 %[wb], %[wb], 0x40000\n\t" \
  "s_and_b32 %[wb], %[wb], 0xfffff\n\t" \
  "s_add_u32 m0, %[mw], 8192\n\t" \
  "s_add_u32 %[st], %[wa], %[wb]\n\t" \
  "buffer_load_dwordx4 %[wv0], %[rq], %[st] offen offset:0 lds\n\t" \
  "v_mfma_f32_16x16x32_bf16 v[244:247], a[24:27], a[180:183], v[244:247]\n\t" \
  "buffer_load_dwordx4 %[wv1], %[rq], %[st] offen offset:1024 lds\n\t" \
  "buffer_load_dwordx4 %[wv2], %[rq], %[st] offen offset:2048 lds\n\t" \
  "buffer_load_dwordx4 %[wv3], %[rq], %[st] offen offset:3072 lds\n\t" \
  "s_add_u32 m0, %[mw], 12288\n\t" \
  "s_add_u32 %[st], %[st], 0x20000\n\t" \
  "v_mfma_f32_16x16x32_bf16 v[248:251], a[28:31], a[176:179], v[248:251]\n\t" \
  "buffer_load_dwordx4 %[wv0], %[rq], %[st] offen offset:0 lds\n\t" \
  "buffer_load_dwordx4 %[wv1], %[rq], %[st] offen offset:1024 lds\n\t" \
  "buffer_load_dwordx4 %[wv2], %[rq], %[st] offen offset:2048 lds\n\t" \
  "buffer_load_dwordx4 %[wv3], %[rq], %[st] offen offset:3072 lds\n\t" \
  "v_mfma_f32_16x16x32_bf16 v[252:255], a[28:31], a[180:183], v[252:255]\n\t" \
  "s_waitcnt vmcnt(16)\n\t" \
  "v_mfma_f32_16x16x32_bf16 v[192:195], a[32:35], a[184:187], v[192:195]\n\t" \
  "ds_read_b64_tr_b16 a[0:1], %[va0] offset:16384\n\t" \
  "ds_read_b64_tr_b16 a[2:3], %[va0] offset:17408\n\t" \
  "v_mfma_f32_16x16x32_bf16 v[196:199], a[32:35], a[188:191], v[196:199]\n\t" \
  "ds_read_b64_tr_b16 a[4:5], %[va1] offset:16384\n\t" \
  "v_mfma_f32_16x16x32_bf16 v[200:203], a[36:39], a[184:187], v[200:203]\n\t" \
  "ds_read_b64_tr_b16 a[6:7], %[va1] offset:17408\n\t" \
  "ds_read_b64_tr_b16 a[8:9], %[va2] offset:16384\n\t" \
  "v_mfma_f32_16x16x32_bf16 v[204:207], a[36:39], a[188:191], v[204:207]\n\t" \
  "ds_read_b64_tr_b16 a[10:11], %[va2] offset:17408\n\t" \
  "v_mfma_f32_16x16x32_bf16 v[208:211], a[40:43], a[184:187], v[208:211]\n\t" \
  "ds_read_b64_tr_b16 a[12:13], %[va3] offset:16384\n\t" \
  "ds_read_b64_tr_b16 a[14:15], %[va3] offset:17408\n\t" \
  "v_mfma_f32_16x16x32_bf16 v[212:215], a[40:43], a[188:191], v[212:215]\n\t" \
  "ds_read_b64_tr_b16 a[16:17], %[va4] offset:16384\n\t" \
  "v_mfma_f32_16x16x32_bf16 v[216:219], a[44:47], a[184:187], v[216:219]\n\t" \
  "ds_read_b64_tr_b16 a[18:19], %[va4] offset:17408\n\t" \
  "ds_read_b64_tr_b16 a[20:21], %[va5] offset:16384\n\t" \
  "v_mfma_f32_16x16x32_bf16 v[220:223], a[44:47], a[188:191], v[220:223]\n\t" \
  "ds_read_b64_tr_b16 a[22:23], %[va5] offset:17408\n\t" \
  "v_mfma_f32_16x16x32_bf16 v[224:227], a[48:51], a[184:187], v[224:227]\n\t" \
  "ds_read_b64_tr_b16 a[24:25], %[va6] offset:16384\n\t" \
  "ds_read_b64_tr_b16 a[26:27], %[va6] offset:17408\n\t" \
  "v_mfma_f32_16x16x32_bf16 v[228:231], a[48:51], a[188:191], v[228:231]\n\t" \
  "ds_read_b64_tr_b16 a[28:29], %[va7] offset:16384\n\t" \
  "ds_read_b64_tr_b16 a[30:31], %[va7] offset:17408\n\t" \
  "v_mfma_f32_16x16x32_bf16 v[232:235], a[52:55], a[184:187], v[232:235]\n\t" \
  "v_mfma_f32_16x16x32_bf16 v[236:239], a[52:55], a[188:191], v[236:239]\n\t" \
  "v_mfma_f32_16x16x32_bf16 v[240:243], a[56:59], a[184:187], v[240:243]\n\t" \
  "s_waitcnt lgkmcnt(0)\n\t" \
  "s_add_u32 %[wb], %[wb], 0x40000\n\t" \
  "s_and_b32 %[wb], %[wb], 0xfffff\n\t" \
  "s_add_u32 m0, %[mw], 16384\n\t" \
  "s_add_u32 %[st], %[wa], %[wb]\n\t" \
  "buffer_load_dwordx4 %[wv0], %[rq], %[st] offen offset:0 lds\n\t" \
  "v_mfma_f32_16x16x32_bf16 v[244:247], a[56:59], a[188:191], v[244:247]\n\t" \
  "buffer_load_dwordx4 %[wv1], %[rq], %[st] offen offset:1024 lds\n\t" \
  "buffer_load_dwordx4 %[wv2], %[rq], %[st] offen offset:2048 lds\n\t" \
  "buffer_load_dwordx4 %[wv3], %[rq], %[st] offen offset:3072 lds\n\t" \
  "s_add_u32 m0, %[mw], 20480\n\t" \
  "s_add_u32 %[st], %[st], 0x20000\n\t" \
  "v_mfma_f32_16x16x32_bf16 v[248:251], a[60:63], a[184:187], v[248:251]\n\t" \
  "buffer_load_dwordx4 %[wv0], %[rq], %[st] offen offset:0 lds\n\t" \
  "buffer_load_dwordx4 %[wv1], %[rq], %[st] offen offset:1024 lds\n\t" \
  "buffer_load_dwordx4 %[wv2], %[rq], %[st] offen offset:2048 lds\n\t" \
  "buffer_load_dwordx4 %[wv3], %[rq], %[st] offen offset:3072 lds\n\t" \
  "v_mfma_f32_16x16x32_bf16 v[252:255], a[60:63], a[188:191], v[252:255]\n\t" \
  "s_waitcnt vmcnt(16)\n\t" \
  "v_mfma_f32_16x16x32_bf16 v[192:195], a[0:3], a[192:195], v[192:195]\n\t" \
  "ds_read_b64_tr_b16 a[32:33], %[va0] offset:0\n\t" \
  "ds_read_b64_tr_b16 a[34:35], %[va0] offset:1024\n\t" \
  "v_mfma_f32_16x16x32_bf16 v[196:199], a[0:3], a[196:199], v[196:199]\n\t" \
  "ds_read_b64_tr_b16 a[36:37], %[va1] offset:0\n\t" \
  "v_mfma_f32_16x16x32_bf16 v[200:203], a[4:7], a[192:195], v[200:203]\n\t" \
  "ds_read_b64_tr_b16 a[38:39], %[va1] offset:1024\n\t" \
  "ds_read_b64_tr_b16 a[40:41], %[va2] offset:0\n\t" \
  "v_mfma_f32_16x16x32_bf16 v[204:207], a[4:7], a[196:199], v[204:207]\n\t" \
  "ds_read_b64_tr_b16 a[42:43], %[va2] offset:1024\n\t" \
  "v_mfma_f32_16x16x32_bf16 v[208:211], a[8:11], a[192:195], v[208:211]\n\t" \
  "ds_read_b64_tr_b16 a[44:45], %[va3] offset:0\n\t" \
  "ds_read_b64_tr_b16 a[46:47], %[va3] offset:1024\n\t" \
  "v_mfma_f32_16x16x32_bf16 v[212:215], a[8:11], a[196:199], v[212:215]\n\t" \
  "ds_read_b64_tr_b16 a[48:49], %[va4] offset:0\n\t" \
  "v_mfma_f32_16x16x32_bf16 v[216:219], a[12:15], a[192:195], v[216:219]\n\t" \
  "ds_read_b64_tr_b16 a[50:51], %[va4] offset:1024\n\t" \
  "ds_read_b64_tr_b16 a[52:53], %[va5] offset:0\n\t" \
  "v_mfma_f32_16x16x32_bf16 v[220:223], a[12:15], a[196:199], v[220:223]\n\t" \
  "ds_read_b64_tr_b16 a[54:55], %[va5] offset:1024\n\t" \
  "v_mfma_f32_16x16x32_bf16 v[224:227], a[16:19], a[192:195], v[224:227]\n\t" \
  "ds_read_b64_tr_b16 a[56:57], %[va6] offset:0\n\t" \
  "ds_read_b64_tr_b16 a[58:59], %[va6] offset:1024\n\t" \
  "v_mfma_f32_16x16x32_bf16 v[228:231], a[16:19], a[196:199], v[228:231]\n\t" \
  "ds_read_b64_tr_b16 a[60:61], %[va7] offset:0\n\t" \
  "ds_read_b64_tr_b16 a[62:63], %[va7] offset:1024\n\t" \
  "v_mfma_f32_16x16x32_bf16 v[232:235], a[20:23], a[192:195], v[232:235]\n\t" \
  "v_mfma_f32_16x16x32_bf16 v[236:239], a[20:23], a[196:199], v[236:239]\n\t" \
  "v_mfma_f32_16x16x32_bf16 v[240:243], a[24:27], a[192:195], v[240:243]\n\t" \
  "s_waitcnt lgkmcnt(0)\n\t" \
  "s_add_u32 %[wb], %[wb], 0x40000\n\t" \
  "s_and_b32 %[wb], %[wb], 0xfffff\n\t" \
  "s_add_u32 %[wa], %[wa], 0x100000\n\t" \
  "s_and_b32 %[wa], %[wa], 0x3fffff\n\t" \
  "s_add_u32 m0, %[mw], 0\n\t" \
  "s_add_u32 %[st], %[wa], %[wb]\n\t" \
  "v_mfma_f32_16x16x32_bf16 v[244:247], a[24:27], a[196:199], v[244:247]\n\t" \
  "buffer_load_dwordx4 %[wv0], %[rq], %[st] offen offset:0 lds\n\t" \
  "buffer_load_dwordx4 %[wv1], %[rq], %[st] offen offset:1024 lds\n\t" \
  "buffer_load_dwordx4 %[wv2], %[rq], %[st] offen offset:2048 lds\n\t" \
  "buffer_load_dwordx4 %[wv3], %[rq], %[st] offen offset:3072 lds\n\t" \
  "s_add_u32 m0, %[mw], 4096\n\t" \
  "s_add_u32 %[st], %[st], 0x20000\n\t" \
  "v_mfma_f32_16x16x32_bf16 v[248:251], a[28:31], a[192:195], v[248:251]\n\t" \
  "buffer_load_dwordx4 %[wv0], %[rq], %[st] offen offset:0 lds\n\t" \
  "buffer_load_dwordx4 %[wv1], %[rq], %[st] offen offset:1024 lds\n\t" \
  "buffer_load_dwordx4 %[wv2], %[rq], %[st] offen offset:2048 lds\n\t" \
  "buffer_load_dwordx4 %[wv3], %[rq], %[st] offen offset:3072 lds\n\t" \
  "v_mfma_f32_16x16x32_bf16 v[252:255], a[28:31], a[196:199], v[252:255]\n\t" \
  "s_waitcnt vmcnt(16)\n\t" \
  "v_mfma_f32_16x16x32_bf16 v[192:195], a[32:35], a[200:203], v[192:195]\n\t" \
  "ds_read_b64_tr_b16 a[0:1], %[va0] offset:8192\n\t" \
  "ds_read_b64_tr_b16 a[2:3], %[va0] offset:9216\n\t" \
  "v_mfma_f32_16x16x32_bf16 v[196:199], a[32:35], a[204:207], v[196:199]\n\t" \
  "ds_read_b64_tr_b16 a[4:5], %[va1] offset:8192\n\t" \
  "v_mfma_f32_16x16x32_bf16 v[200:203], a[36:39], a[200:203], v[200:203]\n\t" \
  "ds_read_b64_tr_b16 a[6:7], %[va1] offset:9216\n\t" \
  "ds_read_b64_tr_b16 a[8:9], %[va2] offset:8192\n\t" \
  "v_mfma_f32_16x16x32_bf16 v[204:207], a[36:39], a[204:207], v[204:207]\n\t" \
  "ds_read_b64_tr_b16 a[10:11], %[va2] offset:9216\n\t" \
  "v_mfma_f32_16x16x32_bf16 v[208:211], a[40:43], a[200:203], v[208:211]\n\t" \
  "ds_read_b64_tr_b16 a[12:13], %[va3] offset:8192\n\t" \
  "ds_read_b64_tr_b16 a[14:15], %[va3] offset:9216\n\t" \
  "v_mfma_f32_16x16x32_bf16 v[212:215], a[40:43], a[204:207], v[212:215]\n\t" \
  "ds_read_b64_tr_b16 a[16:17], %[va4] offset:8192\n\t" \
  "v_mfma_f32_16x16x32_bf16 v[216:219], a[44:47], a[200:203], v[216:219]\n\t" \
  "ds_read_b64_tr_b16 a[18:19], %[va4] offset:9216\n\t" \
  "ds_read_b64_tr_b16 a[20:21], %[va5] offset:8192\n\t" \
  "v_mfma_f32_16x16x32_bf16 v[220:223], a[44:47], a[204:207], v[220:223]\n\t" \
  "ds_read_b64_tr_b16 a[22:23], %[va5] offset:9216\n\t" \
  "v_mfma_f32_16x16x32_bf16 v[224:227], a[48:51], a[200:203], v[224:227]\n\t" \
  "ds_read_b64_tr_b16 a[24:25], %[va6] offset:8192\n\t" \
  "ds_read_b64_tr_b16 a[26:27], %[va6] offset:9216\n\t" \
  "v_mfma_f32_16x16x32_bf16 v[228:231], a[48:51], a[204:207], v[228:231]\n\t" \
  "ds_read_b64_tr_b16 a[28:29], %[va7] offset:8192\n\t" \
  "ds_read_b64_tr_b16 a[30:31], %[va7] offset:9216\n\t" \
  "v_mfma_f32_16x16x32_bf16 v[232:235], a[52:55], a[200:203], v[232:235]\n\t" \
  "v_mfma_f32_16x16x32_bf16 v[236:239], a[52:55], a[204:207], v[236:239]\n\t" \
  "v_mfma_f32_16x16x32_bf16 v[240:243], a[56:59], a[200:203], v[240:243]\n\t" \
  "s_waitcnt lgkmcnt(0)\n\t" \
  "s_add_u32 %[wb], %[wb], 0x40000\n\t" \
  "s_and_b32 %[wb], %[wb], 0xfffff\n\t" \
  "s_add_u32 m0, %[mw], 8192\n\t" \
  "s_add_u32 %[st], %[wa], %[wb]\n\t" \
  "buffer_load_dwordx4 %[wv0], %[rq], %[st] offen offset:0 lds\n\t" \
  "v_mfma_f32_16x16x32_bf16 v[244:247], a[56:59], a[204:207], v[244:247]\n\t" \
  "buffer_load_dwordx4 %[wv1], %[rq], %[st] offen offset:1024 lds\n\t" \
  "buffer_load_dwordx4 %[wv2], %[rq], %[st] offen offset:2048 lds\n\t" \
  "buffer_load_dwordx4 %[wv3], %[rq], %[st] offen offset:3072 lds\n\t" \
  "s_add_u32 m0, %[mw], 12288\n\t" \
  "s_add_u32 %[st], %[st], 0x20000\n\t" \
  "v_mfma_f32_16x16x32_bf16 v[248:251], a[60:63], a[200:203], v[248:251]\n\t" \
  "buffer_load_dwordx4 %[wv0], %[rq], %[st] offen offset:0 lds\n\t" \
  "buffer_load_dwordx4 %[wv1], %[rq], %[st] offen offset:1024 lds\n\t" \
  "buffer_load_dwordx4 %[wv2], %[rq], %[st] offen offset:2048 lds\n\t" \
  "buffer_load_dwordx4 %[wv3], %[rq], %[st] offen offset:3072 lds\n\t" \
  "v_mfma_f32_16x16x32_bf16 v[252:255], a[60:63], a[204:207], v[252:255]\n\t" \
  "s_waitcnt vmcnt(16)\n\t" \
  "v_mfma_f32_16x16x32_bf16 v[192:195], a[0:3], a[208:211], v[192:195]\n\t" \
  "ds_read_b64_tr_b16 a[32:33], %[va0] offset:16384\n\t" \
  "ds_read_b64_tr_b16 a[34:35], %[va0] offset:17408\n\t" \
  "v_mfma_f32_16x16x32_bf16 v[196:199], a[0:3], a[212:215], v[196:199]\n\t" \
  "ds_read_b64_tr_b16 a[36:37], %[va1] offset:16384\n\t" \
  "v_mfma_f32_16x16x32_bf16 v[200:203], a[4:7], a[208:211], v[200:203]\n\t" \
  "ds_read_b64_tr_b16 a[38:39], %[va1] offset:17408\n\t" \
  "ds_read_b64_tr_b16 a[40:41], %[va2] offset:16384\n\t" \
  "v_mfma_f32_16x16x32_bf16 v[204:207], a[4:7], a[212:215], v[204:207]\n\t" \
  "ds_read_b64_tr_b16 a[42:43], %[va2] offset:17408\n\t" \
  "v_mfma_f32_16x16x32_bf16 v[208:211], a[8:11], a[208:211], v[208:211]\n\t" \
  "ds_read_b64_tr_b16 a[44:45], %[va3] offset:16384\n\t" \
  "ds_read_b64_tr_b16 a[46:47], %[va3] offset:17408\n\t" \
  "v_mfma_f32_16x16x32_bf16 v[212:215], a[8:11], a[212:215], v[212:215]\n\t" \
  "ds_read_b64_tr_b16 a[48:49], %[va4] offset:16384\n\t" \
  "v_mfma_f32_16x16x32_bf16 v[216:219], a[12:15], a[208:211], v[216:219]\n\t" \
  "ds_read_b64_tr_b16 a[50:51], %[va4] offset:17408\n\t" \
  "ds_read_b64_tr_b16 a[52:53], %[va5] offset:16384\n\t" \
  "v_mfma_f32_16x16x32_bf16 v[220:223], a[12:15], a[212:215], v[220:223]\n\t" \
  "ds_read_b64_tr_b16 a[54:55], %[va5] offset:17408\n\t" \
  "v_mfma_f32_16x16x32_bf16 v[224:227], a[16:19], a[208:211], v[224:227]\n\t" \
  "ds_read_b64_tr_b16 a[56:57], %[va6] offset:16384\n\t" \
  "ds_read_b64_tr_b16 a[58:59], %[va6] offset:17408\n\t" \
  "v_mfma_f32_16x16x32_bf16 v[228:231], a[16:19], a[212:215], v[228:231]\n\t" \
  "ds_read_b64_tr_b16 a[60:61], %[va7] offset:16384\n\t" \
  "ds_read_b64_tr_b16 a[62:63], %[va7] offset:17408\n\t" \
  "v_mfma_f32_16x16x32_bf16 v[232:235], a[20:23], a[208:211], v[232:235]\n\t" \
  "v_mfma_f32_16x16x32_bf16 v[236:239], a[20:23], a[212:215], v[236:239]\n\t" \
  "v_mfma_f32_16x16x32_bf16 v[240:243], a[24:27], a[208:211], v[240:243]\n\t" \
  "s_waitcnt lgkmcnt(0)\n\t" \
  "s_add_u32 %[wb], %[wb], 0x40000\n\t" \
  "s_and_b32 %[wb], %[wb], 0xfffff\n\t" \
  "s_add_u32 m0, %[mw], 16384\n\t" \
  "s_add_u32 %[st], %[wa], %[wb]\n\t" \
  "buffer_load_dwordx4 %[wv0], %[rq], %[st] offen offset:0 lds\n\t" \
  "v_mfma_f32_16x16x32_bf16 v[244:247], a[24:27], a[212:215], v[244:247]\n\t" \
  "buffer_load_dwordx4 %[wv1], %[rq], %[st] offen offset:1024 lds\n\t" \
  "buffer_load_dwordx4 %[wv2], %[rq], %[st] offen offset:2048 lds\n\t" \
  "buffer_load_dwordx4 %[wv3], %[rq], %[st] offen offset:3072 lds\n\t" \
  "s_add_u32 m0, %[mw], 20480\n\t" \
  "s_add_u32 %[st], %[st], 0x20000\n\t" \
  "v_mfma_f32_16x16x32_bf16 v[248:251], a[28:31], a[208:211], v[248:251]\n\t" \
  "buffer_load_dwordx4 %[wv0], %[rq], %[st] offen offset:0 lds\n\t" \
  "buffer_load_dwordx4 %[wv1], %[rq], %[st] offen offset:1024 lds\n\t" \
  "buffer_load_dwordx4 %[wv2], %[rq], %[st] offen offset:2048 lds\n\t" \
  "buffer_load_dwordx4 %[wv3], %[rq], %[st] offen offset:3072 lds\n\t" \
  "v_mfma_f32_16x16x32_bf16 v[252:255], a[28:31], a[212:215], v[252:255]\n\t" \
  "s_waitcnt vmcnt(16)\n\t" \
  "v_mfma_f32_16x16x32_bf16 v[192:195], a[32:35], a[216:219], v[192:195]\n\t" \
  "ds_read_b64_tr_b16 a[0:1], %[va0] offset:0\n\t" \
  "ds_read_b64_tr_b16 a[2:3], %[va0] offset:1024\n\t" \
  "v_mfma_f32_16x16x32_bf16 v[196:199], a[32:35], a[220:223], v[196:199]\n\t" \
  "ds_read_b64_tr_b16 a[4:5], %[va1] offset:0\n\t" \
  "v_mfma_f32_16x16x32_bf16 v[200:203], a[36:39], a[216:219], v[200:203]\n\t" \
  "ds_read_b64_tr_b16 a[6:7], %[va1] offset:1024\n\t" \
  "ds_read_b64_tr_b16 a[8:9], %[va2] offset:0\n\t" \
  "v_mfma_f32_16x16x32_bf16 v[204:207], a[36:39], a[220:223], v[204:207]\n\t" \
  "ds_read_b64_tr_b16 a[10:11], %[va2] offset:1024\n\t" \
  "v_mfma_f32_16x16x32_bf16 v[208:211], a[40:43], a[216:219], v[208:211]\n\t" \
  "ds_read_b64_tr_b16 a[12:13], %[va3] offset:0\n\t" \
  "ds_read_b64_tr_b16 a[14:15], %[va3] offset:1024\n\t" \
  "v_mfma_f32_16x16x32_bf16 v[212:215], a[40:43], a[220:223], v[212:215]\n\t" \
  "ds_read_b64_tr_b16 a[16:17], %[va4] offset:0\n\t" \
  "v_mfma_f32_16x16x32_bf16 v[216:219], a[44:47], a[216:219], v[216:219]\n\t" \
  "ds_read_b64_tr_b16 a[18:19], %[va4] offset:1024\n\t" \
  "ds_read_b64_tr_b16 a[20:21], %[va5] offset:0\n\t" \
  "v_mfma_f32_16x16x32_bf16 v[220:223], a[44:47], a[220:223], v[220:223]\n\t" \
  "ds_read_b64_tr_b16 a[22:23], %[va5] offset:1024\n\t" \
  "v_mfma_f32_16x16x32_bf16 v[224:227], a[48:51], a[216:219], v[224:227]\n\t" \
  "ds_read_b64_tr_b16 a[24:25], %[va6] offset:0\n\t" \
  "ds_read_b64_tr_b16 a[26:27], %[va6] offset:1024\n\t" \
  "v_mfma_f32_16x16x32_bf16 v[228:231], a[48:51], a[220:223], v[228:231]\n\t" \
  "ds_read_b64_tr_b16 a[28:29], %[va7] offset:0\n\t" \
  "ds_read_b64_tr_b16 a[30:31], %[va7] offset:1024\n\t" \
  "v_mfma_f32_16x16x32_bf16 v[232:235], a[52:55], a[216:219], v[232:235]\n\t" \
  "v_mfma_f32_16x16x32_bf16 v[236:239], a[52:55], a[220:223], v[236:239]\n\t" \
  "v_mfma_f32_16x16x32_bf16 v[240:243], a[56:59], a[216:219], v[240:243]\n\t" \
  "s_waitcnt lgkmcnt(0)\n\t" \
  "s_add_u32 %[wb], %[wb], 0x40000\n\t" \
  "s_and_b32 %[wb], %[wb], 0xfffff\n\t" \
  "s_add_u32 m0, %[mw], 0\n\t" \
  "s_add_u32 %[st], %[wa], %[wb]\n\t" \
  "buffer_load_dwordx4 %[wv0], %[rq], %[st] offen offset:0 lds\n\t" \
  "v_mfma_f32_16x16x32_bf16 v[244:247], a[56:59], a[220:223], v[244:247]\n\t" \
  "buffer_load_dwordx4 %[wv1], %[rq], %[st] offen offset:1024 lds\n\t" \
  "buffer_load_dwordx4 %[wv2], %[rq], %[st] offen offset:2048 lds\n\t" \
  "buffer_load_dwordx4 %[wv3], %[rq], %[st] offen offset:3072 lds\n\t" \
  "s_add_u32 m0, %[mw], 4096\n\t" \
  "s_add_u32 %[st], %[st], 0x20000\n\t" \
  "v_mfma_f32_16x16x32_bf16 v[248:251], a[60:63], a[216:219], v[248:251]\n\t" \
  "buffer_load_dwordx4 %[wv0], %[rq], %[st] offen offset:0 lds\n\t" \
  "buffer_load_dwordx4 %[wv1], %[rq], %[st] offen offset:1024 lds\n\t" \
  "buffer_load_dwordx4 %[wv2], %[rq], %[st] offen offset:2048 lds\n\t" \
  "buffer_load_dwordx4 %[wv3], %[rq], %[st] offen offset:3072 lds\n\t" \
  "v_mfma_f32_16x16x32_bf16 v[252:255], a[60:63], a[220:223], v[252:255]\n\t" \
  "s_waitcnt vmcnt(16)\n\t" \
  "v_mfma_f32_16x16x32_bf16 v[192:195], a[0:3], a[224:227], v[192:195]\n\t" \
  "ds_read_b64_tr_b16 a[32:33], %[va0] offset:8192\n\t" \
  "ds_read_b64_tr_b16 a[34:35], %[va0] offset:9216\n\t" \
  "v_mfma_f32_16x16x32_bf16 v[196:199], a[0:3], a[228:231], v[196:199]\n\t" \
  "ds_read_b64_tr_b16 a[36:37], %[va1] offset:8192\n\t" \
  "v_mfma_f32_16x16x32_bf16 v[200:203], a[4:7], a[224:227], v[200:203]\n\t" \
  "ds_read_b64_tr_b16 a[38:39], %[va1] offset:9216\n\t" \
  "ds_read_b64_tr_b16 a[40:41], %[va2] offset:8192\n\t" \
  "v_mfma_f32_16x16x32_bf16 v[204:207], a[4:7], a[228:231], v[204:207]\n\t" \
  "ds_read_b64_tr_b16 a[42:43], %[va2] offset:9216\n\t" \
  "v_mfma_f32_16x16x32_bf16 v[208:211], a[8:11], a[224:227], v[208:211]\n\t" \
  "ds_read_b64_tr_b16 a[44:45], %[va3] offset:8192\n\t" \
  "ds_read_b64_tr_b16 a[46:47], %[va3] offset:9216\n\t" \
  "v_mfma_f32_16x16x32_bf16 v[212:215], a[8:11], a[228:231], v[212:215]\n\t" \
  "ds_read_b64_tr_b16 a[48:49], %[va4] offset:8192\n\t" \
  "v_mfma_f32_16x16x32_bf16 v[216:219], a[12:15], a[224:227], v[216:219]\n\t" \
  "ds_read_b64_tr_b16 a[50:51], %[va4] offset:9216\n\t" \
  "ds_read_b64_tr_b16 a[52:53], %[va5] offset:8192\n\t" \
  "v_mfma_f32_16x16x32_bf16 v[220:223], a[12:15], a[228:231], v[220:223]\n\t" \
  "ds_read_b64_tr_b16 a[54:55], %[va5] offset:9216\n\t" \
  "v_mfma_f32_16x16x32_bf16 v[224:227], a[16:19], a[224:227], v[224:227]\n\t" \
  "ds_read_b64_tr_b16 a[56:57], %[va6] offset:8192\n\t" \
  "ds_read_b64_tr_b16 a[58:59], %[va6] offset:9216\n\t" \
  "v_mfma_f32_16x16x32_bf16 v[228:231], a[16:19], a[228:231], v[228:231]\n\t" \
  "ds_read_b64_tr_b16 a[60:61], %[va7] offset:8192\n\t" \
  "ds_read_b64_tr_b16 a[62:63], %[va7] offset:9216\n\t" \
  "v_mfma_f32_16x16x32_bf16 v[232:235], a[20:23], a[224:227], v[232:235]\n\t" \
  "v_mfma_f32_16x16x32_bf16 v[236:239], a[20:23], a[228:231], v[236:239]\n\t" \
  "v_mfma_f32_16x16x32_bf16 v[240:243], a[24:27], a[224:227], v[240:243]\n\t" \
  "s_waitcnt lgkmcnt(0)\n\t" \
  "v_mfma_f32_16x16x32_bf16 v[244:247], a[24:27], a[228:231], v[244:247]\n\t" \
  "v_mfma_f32_16x16x32_bf16 v[248:251], a[28:31], a[224:227], v[248:251]\n\t" \
  "v_mfma_f32_16x16x32_bf16 v[252:255], a[28:31], a[228:231], v[252:255]\n\t" \
  "s_waitcnt vmcnt(8)\n\t" \
  "v_mfma_f32_16x16x32_bf16 v[192:195], a[32:35], a[232:235], v[192:195]\n\t" \
  "ds_read_b64_tr_b16 a[0:1], %[va0] offset:16384\n\t" \
  "ds_read_b64_tr_b16 a[2:3], %[va0] offset:17408\n\t" \
  "v_mfma_f32_16x16x32_bf16 v[196:199], a[32:35], a[236:239], v[196:199]\n\t" \
  "ds_read_b64_tr_b16 a[4:5], %[va1] offset:16384\n\t" \
  "v_mfma_f32_16x16x32_bf16 v[200:203], a[36:39], a[232:235], v[200:203]\n\t" \
  "ds_read_b64_tr_b16 a[6:7], %[va1] offset:17408\n\t" \
  "ds_read_b64_tr_b16 a[8:9], %[va2] offset:16384\n\t" \
  "v_mfma_f32_16x16x32_bf16 v[204:207], a[36:39], a[236:239], v[204:207]\n\t" \
  "ds_read_b64_tr_b16 a[10:11], %[va2] offset:17408\n\t" \
  "v_mfma_f32_16x16x32_bf16 v[208:211], a[40:43], a[232:235], v[208:211]\n\t" \
  "ds_read_b64_tr_b16 a[12:13], %[va3] offset:16384\n\t" \
  "ds_read_b64_tr_b16 a[14:15], %[va3] offset:17408\n\t" \
  "v_mfma_f32_16x16x32_bf16 v[212:215], a[40:43], a[236:239], v[212:215]\n\t" \
  "ds_read_b64_tr_b16 a[16:17], %[va4] offset:16384\n\t" \
  "v_mfma_f32_16x16x32_bf16 v[216:219], a[44:47], a[232:235], v[216:219]\n\t" \
  "ds_read_b64_tr_b16 a[18:19], %[va4] offset:17408\n\t" \
  "ds_read_b64_tr_b16 a[20:21], %[va5] offset:16384\n\t" \
  "v_mfma_f32_16x16x32_bf16 v[220:223], a[44:47], a[236:239], v[220:223]\n\t" \
  "ds_read_b64_tr_b16 a[22:23], %[va5] offset:17408\n\t" \
  "v_mfma_f32_16x16x32_bf16 v[224:227], a[48:51], a[232:235], v[224:227]\n\t" \
  "ds_read_b64_tr_b16 a[24:25], %[va6] offset:16384\n\t" \
  "ds_read_b64_tr_b16 a[26:27], %[va6] offset:17408\n\t" \
  "v_mfma_f32_16x16x32_bf16 v[228:231], a[48:51], a[236:239], v[228:231]\n\t" \
  "ds_read_b64_tr_b16 a[28:29], %[va7] offset:16384\n\t" \
  "ds_read_b64_tr_b16 a[30:31], %[va7] offset:17408\n\t" \
  "v_mfma_f32_16x16x32_bf16 v[232:235], a[52:55], a[232:235], v[232:235]\n\t" \
  "v_mfma_f32_16x16x32_bf16 v[236:239], a[52:55], a[236:239], v[236:239]\n\t" \
  "v_mfma_f32_16x16x32_bf16 v[240:243], a[56:59], a[232:235], v[240:243]\n\t" \
  "s_waitcnt lgkmcnt(0)\n\t" \
  "v_mfma_f32_16x16x32_bf16 v[244:247], a[56:59], a[236:239], v[244:247]\n\t" \
  "v_mfma_f32_16x16x32_bf16 v[248:251], a[60:63], a[232:235], v[248:251]\n\t" \
  "v_mfma_f32_16x16x32_bf16 v[252:255], a[60:63], a[236:239], v[252:255]\n\t" \
  "s_waitcnt vmcnt(0)\n\t" \
  "v_mfma_f32_16x16x32_bf16 v[192:195], a[0:3], a[240:243], v[192:195]\n\t" \
  "ds_read_b64_tr_b16 a[32:33], %[va0] offset:0\n\t" \
  "ds_read_b64_tr_b16 a[34:35], %[va0] offset:1024\n\t" \
  "v_mfma_f32_16x16x32_bf16 v[196:199], a[0:3], a[244:247], v[196:199]\n\t" \
  "ds_read_b64_tr_b16 a[36:37], %[va1] offset:0\n\t" \
  "v_mfma_f32_16x16x32_bf16 v[200:203], a[4:7], a[240:243], v[200:203]\n\t" \
  "ds_read_b64_tr_b16 a[38:39], %[va1] offset:1024\n\t" \
  "ds_read_b64_tr_b16 a[40:41], %[va2] offset:0\n\t" \
  "v_mfma_f32_16x16x32_bf16 v[204:207], a[4:7], a[244:247], v[204:207]\n\t" \
  "ds_read_b64_tr_b16 a[42:43], %[va2] offset:1024\n\t" \
  "v_mfma_f32_16x16x32_bf16 v[208:211], a[8:11], a[240:243], v[208:211]\n\t" \
  "ds_read_b64_tr_b16 a[44:45], %[va3] offset:0\n\t" \
  "ds_read_b64_tr_b16 a[46:47], %[va3] offset:1024\n\t" \
  "v_mfma_f32_16x16x32_bf16 v[212:215], a[8:11], a[244:247], v[212:215]\n\t" \
  "ds_read_b64_tr_b16 a[48:49], %[va4] offset:0\n\t" \
  "v_mfma_f32_16x16x32_bf16 v[216:219], a[12:15], a[240:243], v[216:219]\n\t" \
  "ds_read_b64_tr_b16 a[50:51], %[va4] offset:1024\n\t" \
  "ds_read_b64_tr_b16 a[52:53], %[va5] offset:0\n\t" \
  "v_mfma_f32_16x16x32_bf16 v[220:223], a[12:15], a[244:247], v[220:223]\n\t" \
  "ds_read_b64_tr_b16 a[54:55], %[va5] offset:1024\n\t" \
  "v_mfma_f32_16x16x32_bf16 v[224:227], a[16:19], a[240:243], v[224:227]\n\t" \
  "ds_read_b64_tr_b16 a[56:57], %[va6] offset:0\n\t" \
  "ds_read_b64_tr_b16 a[58:59], %[va6] offset:1024\n\t" \
  "v_mfma_f32_16x16x32_bf16 v[228:231], a[16:19], a[244:247], v[228:231]\n\t" \
  "ds_read_b64_tr_b16 a[60:61], %[va7] offset:0\n\t" \
  "ds_read_b64_tr_b16 a[62:63], %[va7] offset:1024\n\t" \
  "v_mfma_f32_16x16x32_bf16 v[232:235], a[20:23], a[240:243], v[232:235]\n\t" \
  "v_mfma_f32_16x16x32_bf16 v[236:239], a[20:23], a[244:247], v[236:239]\n\t" \
  "v_mfma_f32_16x16x32_bf16 v[240:243], a[24:27], a[240:243], v[240:243]\n\t" \
  "s_waitcnt lgkmcnt(0)\n\t" \
  "v_mfma_f32_16x16x32_bf16 v[244:247], a[24:27], a[244:247], v[244:247]\n\t" \
  "v_mfma_f32_16x16x32_bf16 v[248:251], a[28:31], a[240:243], v[248:251]\n\t" \
  "v_mfma_f32_16x16x32_bf16 v[252:255], a[28:31], a[244:247], v[252:255]\n\t" \
  "v_mfma_f32_16x16x32_bf16 v[192:195], a[32:35], a[248:251], v[192:195]\n\t" \
  "v_mfma_f32_16x16x32_bf16 v[196:199], a[32:35], a[252:255], v[196:199]\n\t" \
  "v_mfma_f32_16x16x32_bf16 v[200:203], a[36:39], a[248:251], v[200:203]\n\t" \
  "v_mfma_f32_16x16x32_bf16 v[204:207], a[36:39], a[252:255], v[204:207]\n\t" \
  "v_mfma_f32_16x16x32_bf16 v[208:211], a[40:43], a[248:251], v[208:211]\n\t" \
  "v_mfma_f32_16x16x32_bf16 v[212:215], a[40:43], a[252:255], v[212:215]\n\t" \
  "v_mfma_f32_16x16x32_bf16 v[216:219], a[44:47], a[248:251], v[216:219]\n\t" \
  "v_mfma_f32_16x16x32_bf16 v[220:223], a[44:47], a[252:255], v[220:223]\n\t" \
  "v_mfma_f32_16x16x32_bf16 v[224:227], a[48:51], a[248:251], v[224:227]\n\t" \
  "v_mfma_f32_16x16x32_bf16 v[228:231], a[48:51], a[252:255], v[228:231]\n\t" \
  "v_mfma_f32_16x16x32_bf16 v[232:235], a[52:55], a[248:251], v[232:235]\n\t" \
  "v_mfma_f32_16x16x32_bf16 v[236:239], a[52:55], a[252:255], v[236:239]\n\t" \
  "v_mfma_f32_16x16x32_bf16 v[240:243], a[56:59], a[248:251], v[240:243]\n\t" \
  "v_mfma_f32_16x16x32_bf16 v[244:247], a[56:59], a[252:255], v[244:247]\n\t" \
  "v_mfma_f32_16x16x32_bf16 v[248:251], a[60:63], a[248:251], v[248:251]\n\t" \
  "v_mfma_f32_16x16x32_bf16 v[252:255], a[60:63], a[252:255], v[252:255]\n\t" \
  "v_subrev_u32 %[va0], %[vdel], %[va0]\n\t" \
  "v_subrev_u32 %[va1], %[vdel], %[va1]\n\t" \
  "v_subrev_u32 %[va2], %[vdel], %[va2]\n\t" \
  "v_subrev_u32 %[va3], %[vdel], %[va3]\n\t" \
  "v_subrev_u32 %[va4], %[vdel], %[va4]\n\t" \
  "v_subrev_u32 %[va5], %[vdel], %[va5]\n\t" \
  "v_subrev_u32 %[va6], %[vdel], %[va6]\n\t" \
  "v_subrev_u32 %[va7], %[vdel], %[va7]\n\t" \
  "s_waitcnt vmcnt(12)\n\t" \
  "s_barrier\n\t" \
  "ds_read_b128 a[64:67], %[ka0] offset:0\n\t" \
  "ds_read_b128 a[80:83], %[ka0] offset:1024\n\t" \
  "ds_read_b128 a[68:71], %[ka1] offset:0\n\t" \
  "ds_read_b128 a[84:87], %[ka1] offset:1024\n\t" \
  "ds_read_b128 a[72:75], %[ka2] offset:0\n\t" \
  "ds_read_b128 a[88:91], %[ka2] offset:1024\n\t" \
  "ds_read_b128 a[76:79], %[ka3] offset:0\n\t" \
  "ds_read_b128 a[92:95], %[ka3] offset:1024\n\t" \
  "s_mov_b32 %[st], 0x3e0293ee\n\t" \
  "v_mul_f32 v192, %[st], v192\n\t" \
  "v_mul_f32 v193, %[st], v193\n\t" \
  "v_mul_f32 v194, %[st], v194\n\t" \
  "v_mul_f32 v195, %[st], v195\n\t" \
  "v_cvt_pk_bf16_f32 v136, v192, v193\n\t" \
  "v_cvt_pk_bf16_f32 v137, v194, v195\n\t" \
  "v_mul_f32 v200, %[st], v200\n\t" \
  "v_mul_f32 v201, %[st], v201\n\t" \
  "v_mul_f32 v202, %[st], v202\n\t" \
  "v_mul_f32 v203, %[st], v203\n\t" \
  "v_cvt_pk_bf16_f32 v138, v200, v201\n\t" \
  "v_cvt_pk_bf16_f32 v139, v202, v203\n\t" \
  "v_mul_f32 v208, %[st], v208\n\t" \
  "v_mul_f32 v209, %[st], v209\n\t" \
  "v_mul_f32 v210, %[st], v210\n\t" \
  "v_mul_f32 v211, %[st], v211\n\t" \
  "v_cvt_pk_bf16_f32 v140, v208, v209\n\t" \
  "v_cvt_pk_bf16_f32 v141, v210, v211\n\t" \
  "v_mul_f32 v216, %[st], v216\n\t" \
  "v_mul_f32 v217, %[st], v217\n\t" \
  "v_mul_f32 v218, %[st], v218\n\t" \
  "v_mul_f32 v219, %[st], v219\n\t" \
  "v_cvt_pk_bf16_f32 v142, v216, v217\n\t" \
  "v_cvt_pk_bf16_f32 v143, v218, v219\n\t" \
  "v_mul_f32 v224, %[st], v224\n\t" \
  "v_mul_f32 v225, %[st], v225\n\t" \
  "v_mul_f32 v226, %[st], v226\n\t" \
  "v_mul_f32 v227, %[st], v227\n\t" \
  "v_cvt_pk_bf16_f32 v144, v224, v225\n\t" \
  "v_cvt_pk_bf16_f32 v145, v226, v227\n\t" \
  "v_mul_f32 v232, %[st], v232\n\t" \
  "v_mul_f32 v233, %[st], v233\n\t" \
  "v_mul_f32 v234, %[st], v234\n\t" \
  "v_mul_f32 v235, %[st], v235\n\t" \
  "v_cvt_pk_bf16_f32 v146, v232, v233\n\t" \
  "v_cvt_pk_bf16_f32 v147, v234, v235\n\t" \
  "v_mul_f32 v240, %[st], v240\n\t" \
  "v_mul_f32 v241, %[st], v241\n\t" \
  "v_mul_f32 v242, %[st], v242\n\t" \
  "v_mul_f32 v243, %[st], v243\n\t" \
  "v_cvt_pk_bf16_f32 v148, v240, v241\n\t" \
  "v_cvt_pk_bf16_f32 v149, v242, v243\n\t" \
  "v_mul_f32 v248, %[st], v248\n\t" \
  "v_mul_f32 v249, %[st], v249\n\t" \
  "v_mul_f32 v250, %[st], v250\n\t" \
  "v_mul_f32 v251, %[st], v251\n\t" \
  "v_cvt_pk_bf16_f32 v150, v248, v249\n\t" \
  "v_cvt_pk_bf16_f32 v151, v250, v251\n\t" \
  "v_mul_f32 v196, %[st], v196\n\t" \
  "v_mul_f32 v197, %[st], v197\n\t" \
  "v_mul_f32 v198, %[st], v198\n\t" \
  "v_mul_f32 v199, %[st], v199\n\t" \
  "v_cvt_pk_bf16_f32 v152, v196, v197\n\t" \
  "v_cvt_pk_bf16_f32 v153, v198, v199\n\t" \
  "v_mul_f32 v204, %[st], v204\n\t" \
  "v_mul_f32 v205, %[st], v205\n\t" \
  "v_mul_f32 v206, %[st], v206\n\t" \
  "v_mul_f32 v207, %[st], v207\n\t" \
  "v_cvt_pk_bf16_f32 v154, v204, v205\n\t" \
  "v_cvt_pk_bf16_f32 v155, v206, v207\n\t" \
  "v_mul_f32 v212, %[st], v212\n\t" \
  "v_mul_f32 v213, %[st], v213\n\t" \
  "v_mul_f32 v214, %[st], v214\n\t" \
  "v_mul_f32 v215, %[st], v215\n\t" \
  "v_cvt_pk_bf16_f32 v156, v212, v213\n\t" \
  "v_cvt_pk_bf16_f32 v157, v214, v215\n\t" \
  "v_mul_f32 v220, %[st], v220\n\t" \
  "v_mul_f32 v221, %[st], v221\n\t" \
  "v_mul_f32 v222, %[st], v222\n\t" \
  "v_mul_f32 v223, %[st], v223\n\t" \
  "v_cvt_pk_bf16_f32 v158, v220, v221\n\t" \
  "v_cvt_pk_bf16_f32 v159, v222, v223\n\t" \
  "v_mul_f32 v228, %[st], v228\n\t" \
  "v_mul_f32 v229, %[st], v229\n\t" \
  "v_mul_f32 v230, %[st], v230\n\t" \
  "v_mul_f32 v231, %[st], v231\n\t" \
  "v_cvt_pk_bf16_f32 v160, v228, v229\n\t" \
  "v_cvt_pk_bf16_f32 v161, v230, v231\n\t" \
  "v_mul_f32 v236, %[st], v236\n\t" \
  "v_mul_f32 v237, %[st], v237\n\t" \
  "v_mul_f32 v238, %[st], v238\n\t" \
  "v_mul_f32 v239, %[st], v239\n\t" \
  "v_cvt_pk_bf16_f32 v162, v236, v237\n\t" \
  "v_cvt_pk_bf16_f32 v163, v238, v239\n\t" \
  "v_mul_f32 v244, %[st], v244\n\t" \
  "v_mul_f32 v245, %[st], v245\n\t" \
  "v_mul_f32 v246, %[st], v246\n\t" \
  "v_mul_f32 v247, %[st], v247\n\t" \
  "v_cvt_pk_bf16_f32 v164, v244, v245\n\t" \
  "v_cvt_pk_bf16_f32 v165, v246, v247\n\t" \
  "v_mul_f32 v252, %[st], v252\n\t" \
  "v_mul_f32 v253, %[st], v253\n\t" \
  "v_mul_f32 v254, %[st], v254\n\t" \
  "v_mul_f32 v255, %[st], v255\n\t" \
  "v_cvt_pk_bf16_f32 v166, v252, v253\n\t" \
  "v_cvt_pk_bf16_f32 v167, v254, v255\n\t" \
  "v_lshl_add_u64 v[226:227], v[184:185], 0, 1\n\t" \
  "v_mov_b32 v216, 0x3f803f80\n\t" \
  "v_readfirstlane_b32 vcc_lo, v226\n\t" \
  "v_readfirstlane_b32 vcc_hi, v227\n\t" \
  "v_mov_b32 v200, 0\n\t" \
  "v_mov_b32 v201, 0\n\t" \
  "v_mov_b32 v202, 0\n\t" \
  "v_mov_b32 v203, 0\n\t" \
  "v_mov_b32 v204, 0\n\t" \
  "v_mov_b32 v205, 0\n\t" \
  "v_mov_b32 v206, 0\n\t" \
  "v_mov_b32 v207, 0\n\t" \
  "v_mov_b32 v224, 0\n\t" \
  "s_mov_b64 %[tag], vcc\n\t" \
  "v_accvgpr_write_b32 a128, v216\n\t" \
  "v_accvgpr_write_b32 a129, v216\n\t" \
  "v_accvgpr_write_b32 a130, v216\n\t" \
  "v_accvgpr_write_b32 a131, v216\n\t" \
  "v_permlane16_swap_b32 v136, v138\n\t" \
  "v_permlane16_swap_b32 v137, v139\n\t" \
  "v_permlane16_swap_b32 v140, v142\n\t" \
  "v_permlane16_swap_b32 v141, v143\n\t" \
  "v_permlane16_swap_b32 v144, v146\n\t" \
  "v_permlane16_swap_b32 v145, v147\n\t" \
  "v_permlane16_swap_b32 v148, v150\n\t" \
  "v_permlane16_swap_b32 v149, v151\n\t" \
  "v_permlane16_swap_b32 v152, v154\n\t" \
  "v_permlane16_swap_b32 v153, v155\n\t" \
  "v_permlane16_swap_b32 v156, v158\n\t" \
  "v_permlane16_swap_b32 v157, v159\n\t" \
  "v_permlane16_swap_b32 v160, v162\n\t" \
  "v_permlane16_swap_b32 v161, v163\n\t" \
  "v_permlane16_swap_b32 v164, v166\n\t" \
  "v_permlane16_swap_b32 v165, v167\n\t" \
  "s_waitcnt vmcnt(8) lgkmcnt(0)\n\t" \
  "s_barrier\n\t" \
  "v_mfma_f32_16x16x32_bf16 v[168:171], a[64:67], v[136:139], v[200:203]\n\t" \
  "s_add_u32 m0, %[mkv], 0\n\t" \
  "ds_read_b128 a[96:99], %[ka0] offset:16384\n\t" \
  "v_mfma_f32_16x16x32_bf16 v[172:175], a[80:83], v[136:139], v[200:203]\n\t" \
  "ds_read_b128 a[112:115], %[ka0] offset:17408\n\t" \
  "s_add_u32 %[sk], %[sk], 0x2000\n\t" \
  "v_mfma_f32_16x16x32_bf16 v[168:171], a[68:71], v[140:143], v[168:171]\n\t" \
  "ds_read_b128 a[100:103], %[ka1] offset:16384\n\t" \
  "ds_read_b128 a[116:119], %[ka1] offset:17408\n\t" \
  "v_mfma_f32_16x16x32_bf16 v[172:175], a[84:87], v[140:143], v[172:175]\n\t" \
  "buffer_load_dwordx4 %[kv0], %[rk], %[sk] offen offset:0 nt lds\n\t" \
  "ds_read_b128 a[104:107], %[ka2] offset:16384\n\t" \
  "v_mfma_f32_16x16x32_bf16 v[168:171], a[72:75], v[144:147], v[168:171]\n\t" \
  "ds_read_b128 a[120:123], %[ka2] offset:17408\n\t" \
  "buffer_load_dwordx4 %[kv0], %[rk], %[sk] offen offset:1024 nt lds\n\t" \
  "v_mfma_f32_16x16x32_bf16 v[172:175], a[88:91], v[144:147], v[172:175]\n\t" \
  "ds_read_b128 a[108:111], %[ka3] offset:16384\n\t" \
  "ds_read_b128 a[124:127], %[ka3] offset:17408\n\t" \
  "v_mfma_f32_16x16x32_bf16 v[168:171], a[76:79], v[148:151], v[168:171]\n\t" \
  "s_add_u32 m0, %[mkv], 8192\n\t" \
  "ds_read_b64_tr_b16 a[0:1], %[va0] offset:24576\n\t" \
  "v_mfma_f32_16x16x32_bf16 v[172:175], a[92:95], v[148:151], v[172:175]\n\t" \
  "ds_read_b64_tr_b16 a[2:3], %[va0] offset:25600\n\t" \
  "s_add_u32 %[sv], %[sv], 0x2000\n\t" \
  "v_mfma_f32_16x16x32_bf16 v[184:187], a[64:67], v[152:155], v[204:207]\n\t" \
  "ds_read_b64_tr_b16 a[4:5], %[va1] offset:24576\n\t" \
  "ds_read_b64_tr_b16 a[6:7], %[va1] offset:25600\n\t" \
  "v_mfma_f32_16x16x32_bf16 v[188:191], a[80:83], v[152:155], v[204:207]\n\t" \
  "buffer_load_dwordx4 %[vv0], %[rv], %[sv] offen offset:0 nt lds\n\t" \
  "ds_read_b64_tr_b16 a[8:9], %[va2] offset:24576\n\t" \
  "v_mfma_f32_16x16x32_bf16 v[184:187], a[68:71], v[156:159], v[184:187]\n\t" \
  "ds_read_b64_tr_b16 a[10:11], %[va2] offset:25600\n\t" \
  "buffer_load_dwordx4 %[vv0], %[rv], %[sv] offen offset:1024 nt lds\n\t" \
  "v_mfma_f32_16x16x32_bf16 v[188:191], a[84:87], v[156:159], v[188:191]\n\t" \
  "ds_read_b64_tr_b16 a[12:13], %[va3] offset:24576\n\t" \
  "ds_read_b64_tr_b16 a[14:15], %[va3] offset:25600\n\t" \
  "v_mfma_f32_16x16x32_bf16 v[184:187], a[72:75], v[160:163], v[184:187]\n\t" \
  "ds_read_b64_tr_b16 a[16:17], %[va4] offset:24576\n\t" \
  "ds_read_b64_tr_b16 a[18:19], %[va4] offset:25600\n\t" \
  "v_mfma_f32_16x16x32_bf16 v[188:191], a[88:91], v[160:163], v[188:191]\n\t" \
  "ds_read_b64_tr_b16 a[20:21], %[va5] offset:24576\n\t" \
  "ds_read_b64_tr_b16 a[22:23], %[va5] offset:25600\n\t" \
  "v_mfma_f32_16x16x32_bf16 v[184:187], a[76:79], v[164:167], v[184:187]\n\t" \
  "ds_read_b64_tr_b16 a[24:25], %[va6] offset:24576\n\t" \
  "ds_read_b64_tr_b16 a[26:27], %[va6] offset:25600\n\t" \
  "v_mfma_f32_16x16x32_bf16 v[188:191], a[92:95], v[164:167], v[188:191]\n\t" \
  "ds_read_b64_tr_b16 a[28:29], %[va7] offset:24576\n\t" \
  "ds_read_b64_tr_b16 a[30:31], %[va7] offset:25600\n\t" \
  "s_nop 7\n\t" \
  "s_nop 7\n\t" \
  "v_max3_f32 v216, v168, v169, v170\n\t" \
  "v_max3_f32 v217, v171, v172, v173\n\t" \
  "v_max3_f32 v216, v216, v174, v175\n\t" \
  "v_max_f32 v216, v216, v217\n\t" \
  "v_mov_b32 v217, v216\n\t" \
  "s_nop 1\n\t" \
  "v_permlane16_swap_b32 v216, v217\n\t" \
  "s_nop 1\n\t" \
  "v_max_f32 v216, v216, v217\n\t" \
  "v_mov_b32 v217, v216\n\t" \
  "s_nop 1\n\t" \
  "v_permlane32_swap_b32 v216, v217\n\t" \
  "s_nop 1\n\t" \
  "v_max_f32 v216, v216, v217\n\t" \
  "v_add_f32 v218, 0x40c00000, v216\n\t" \
  "v_sub_f32 v168, v168, v218\n\t" \
  "v_sub_f32 v169, v169, v218\n\t" \
  "v_sub_f32 v170, v170, v218\n\t" \
  "v_sub_f32 v171, v171, v218\n\t" \
  "v_sub_f32 v172, v172, v218\n\t" \
  "v_sub_f32 v173, v173, v218\n\t" \
  "v_sub_f32 v174, v174, v218\n\t" \
  "v_sub_f32 v175, v175, v218\n\t" \
  "v_sub_f32 v200, v200, v218\n\t" \
  "v_sub_f32 v201, v201, v218\n\t" \
  "v_sub_f32 v202, v202, v218\n\t" \
  "v_sub_f32 v203, v203, v218\n\t" \
  "v_exp_f32 v208, v168\n\t" \
  "v_exp_f32 v209, v169\n\t" \
  "v_exp_f32 v210, v170\n\t" \
  "v_exp_f32 v211, v171\n\t" \
  "v_exp_f32 v212, v172\n\t" \
  "v_exp_f32 v213, v173\n\t" \
  "v_exp_f32 v214, v174\n\t" \
  "v_exp_f32 v215, v175\n\t" \
  "s_nop 0\n\t" \
  "v_cvt_pk_bf16_f32 v192, v208, v209\n\t" \
  "v_cvt_pk_bf16_f32 v193, v210, v211\n\t" \
  "v_cvt_pk_bf16_f32 v194, v212, v213\n\t" \
  "v_cvt_pk_bf16_f32 v195, v214, v215\n\t" \
  "v_max3_f32 v216, v184, v185, v186\n\t" \
  "v_max3_f32 v217, v187, v188, v189\n\t" \
  "v_max3_f32 v216, v216, v190, v191\n\t" \
  "v_max_f32 v216, v216, v217\n\t" \
  "v_mov_b32 v217, v216\n\t" \
  "s_nop 1\n\t" \
  "v_permlane16_swap_b32 v216, v217\n\t" \
  "s_nop 1\n\t" \
  "v_max_f32 v216, v216, v217\n\t" \
  "v_mov_b32 v217, v216\n\t" \
  "s_nop 1\n\t" \
  "v_permlane32_swap_b32 v216, v217\n\t" \
  "s_nop 1\n\t" \
  "v_max_f32 v216, v216, v217\n\t" \
  "v_add_f32 v218, 0x40c00000, v216\n\t" \
  "v_sub_f32 v184, v184, v218\n\t" \
  "v_sub_f32 v185, v185, v218\n\t" \
  "v_sub_f32 v186, v186, v218\n\t" \
  "v_sub_f32 v187, v187, v218\n\t" \
  "v_sub_f32 v188, v188, v218\n\t" \
  "v_sub_f32 v189, v189, v218\n\t" \
  "v_sub_f32 v190, v190, v218\n\t" \
  "v_sub_f32 v191, v191, v218\n\t" \
  "v_sub_f32 v204, v204, v218\n\t" \
  "v_sub_f32 v205, v205, v218\n\t" \
  "v_sub_f32 v206, v206, v218\n\t" \
  "v_sub_f32 v207, v207, v218\n\t" \
  "v_exp_f32 v208, v184\n\t" \
  "v_exp_f32 v209, v185\n\t" \
  "v_exp_f32 v210, v186\n\t" \
  "v_exp_f32 v211, v187\n\t" \
  "v_exp_f32 v212, v188\n\t" \
  "v_exp_f32 v213, v189\n\t" \
  "v_exp_f32 v214, v190\n\t" \
  "v_exp_f32 v215, v191\n\t" \
  "s_nop 0\n\t" \
  "v_cvt_pk_bf16_f32 v196, v208, v209\n\t" \
  "v_cvt_pk_bf16_f32 v197, v210, v211\n\t" \
  "v_cvt_pk_bf16_f32 v198, v212, v213\n\t" \
  "v_cvt_pk_bf16_f32 v199, v214, v215\n\t" \
  "s_nop 3\n\t" \
  "s_mov_b32 %[cnt], 7\n\t" \
  ".Lloop_%=:\n\t" \
  "s_waitcnt vmcnt(8) lgkmcnt(0)\n\t" \
  "s_add_u32 m0, %[mkv], 16384\n\t" \
  "s_add_u32 %[sk], %[sk], 0x2000\n\t" \
  "s_barrier\n\t" \
  "v_mfma_f32_16x16x32_bf16 v[176:179], a[96:99], v[136:139], v[200:203]\n\t" \
  "buffer_load_dwordx4 %[kv0], %[rk], %[sk] offen offset:0 nt lds\n\t" \
  "v_cvt_pk_bf16_f32 v196, v208, v209\n\t" \
  "v_mfma_f32_16x16x32_bf16 v[180:183], a[112:115], v[136:139], v[200:203]\n\t" \
  "ds_read_b128 a[64:67], %[ka0] offset:32768\n\t" \
  "v_cvt_pk_bf16_f32 v197, v210, v211\n\t" \
  "v_mfma_f32_16x16x32_bf16 v[176:179], a[100:103], v[140:143], v[176:179]\n\t" \
  "ds_read_b128 a[80:83], %[ka0] offset:33792\n\t" \
  "v_cvt_pk_bf16_f32 v198, v212, v213\n\t" \
  "v_mfma_f32_16x16x32_bf16 v[180:183], a[116:119], v[140:143], v[180:183]\n\t" \
  "ds_read_b128 a[68:71], %[ka1] offset:32768\n\t" \
  "v_cvt_pk_bf16_f32 v199, v214, v215\n\t" \
  "v_mfma_f32_16x16x32_bf16 v[176:179], a[104:107], v[144:147], v[176:179]\n\t" \
  "ds_read_b128 a[84:87], %[ka1] offset:33792\n\t" \
  "v_or3_b32 v224, v196, v197, v224\n\t" \
  "v_mfma_f32_16x16x32_bf16 v[180:183], a[120:123], v[144:147], v[180:183]\n\t" \
  "ds_read_b128 a[72:75], %[ka2] offset:32768\n\t" \
  "v_or3_b32 v224, v198, v199, v224\n\t" \
  "v_mfma_f32_16x16x32_bf16 v[176:179], a[108:111], v[148:151], v[176:179]\n\t" \
  "ds_read_b128 a[88:91], %[ka2] offset:33792\n\t" \
  "v_and_b32 v216, 0x40004000, v224\n\t" \
  "v_mfma_f32_16x16x32_bf16 v[180:183], a[124:127], v[148:151], v[180:183]\n\t" \
  "v_cmp_ne_u32 vcc, 0, v216\n\t" \
  "s_cbranch_vccnz .Lrare0_%=\n\t" \
  ".Lback0_%=:\n\t" \
  "v_mfma_f32_16x16x32_bf16 v[64:67], a[0:3], v[192:195], v[64:67]\n\t" \
  "ds_read_b128 a[76:79], %[ka3] offset:32768\n\t" \
  "buffer_load_dwordx4 %[kv0], %[rk], %[sk] offen offset:1024 nt lds\n\t" \
  "v_mfma_f32_16x16x32_bf16 v[72:75], a[4:7], v[192:195], v[72:75]\n\t" \
  "ds_read_b128 a[92:95], %[ka3] offset:33792\n\t" \
  "v_exp_f32 v208, v176\n\t" \
  "v_mfma_f32_16x16x32_bf16 v[80:83], a[8:11], v[192:195], v[80:83]\n\t" \
  "ds_read_b64_tr_b16 a[32:33], %[va0] offset:40960\n\t" \
  "v_exp_f32 v209, v177\n\t" \
  "v_mfma_f32_16x16x32_bf16 v[88:91], a[12:15], v[192:195], v[88:91]\n\t" \
  "ds_read_b64_tr_b16 a[34:35], %[va0] offset:41984\n\t" \
  "v_exp_f32 v210, v178\n\t" \
  "v_mfma_f32_16x16x32_bf16 v[96:99], a[16:19], v[192:195], v[96:99]\n\t" \
  "ds_read_b64_tr_b16 a[36:37], %[va1] offset:40960\n\t" \
  "v_exp_f32 v211, v179\n\t" \
  "v_mfma_f32_16x16x32_bf16 v[104:107], a[20:23], v[192:195], v[104:107]\n\t" \
  "ds_read_b64_tr_b16 a[38:39], %[va1] offset:41984\n\t" \
  "v_exp_f32 v212, v180\n\t" \
  "v_mfma_f32_16x16x32_bf16 v[112:115], a[24:27], v[192:195], v[112:115]\n\t" \
  "ds_read_b64_tr_b16 a[40:41], %[va2] offset:40960\n\t" \
  "v_exp_f32 v213, v181\n\t" \
  "v_mfma_f32_16x16x32_bf16 v[120:123], a[28:31], v[192:195], v[120:123]\n\t" \
  "s_add_u32 m0, %[mkv], 24576\n\t" \
  "v_exp_f32 v214, v182\n\t" \
  "v_mfma_f32_16x16x32_bf16 v[128:131], a[128:131], v[192:195], v[128:131]\n\t" \
  "s_add_u32 %[sv], %[sv], 0x2000\n\t" \
  "v_exp_f32 v215, v183\n\t" \
  "v_mfma_f32_16x16x32_bf16 v[184:187], a[96:99], v[152:155], v[204:207]\n\t" \
  "buffer_load_dwordx4 %[vv0], %[rv], %[sv] offen offset:0 nt lds\n\t" \
  "v_cvt_pk_bf16_f32 v192, v208, v209\n\t" \
  "v_mfma_f32_16x16x32_bf16 v[188:191], a[112:115], v[152:155], v[204:207]\n\t" \
  "ds_read_b64_tr_b16 a[42:43], %[va2] offset:41984\n\t" \
  "v_cvt_pk_bf16_f32 v193, v210, v211\n\t" \
  "v_mfma_f32_16x16x32_bf16 v[184:187], a[100:103], v[156:159], v[184:187]\n\t" \
  "ds_read_b64_tr_b16 a[44:45], %[va3] offset:40960\n\t" \
  "v_cvt_pk_bf16_f32 v194, v212, v213\n\t" \
  "v_mfma_f32_16x16x32_bf16 v[188:191], a[116:119], v[156:159], v[188:191]\n\t" \
  "ds_read_b64_tr_b16 a[46:47], %[va3] offset:41984\n\t" \
  "v_cvt_pk_bf16_f32 v195, v214, v215\n\t" \
  "v_mfma_f32_16x16x32_bf16 v[184:187], a[104:107], v[160:163], v[184:187]\n\t" \
  "ds_read_b64_tr_b16 a[48:49], %[va4] offset:40960\n\t" \
  "v_or_b32 v224, v192, v193\n\t" \
  "v_mfma_f32_16x16x32_bf16 v[188:191], a[120:123], v[160:163], v[188:191]\n\t" \
  "ds_read_b64_tr_b16 a[50:51], %[va4] offset:41984\n\t" \
  "v_or3_b32 v224, v194, v195, v224\n\t" \
  "v_mfma_f32_16x16x32_bf16 v[184:187], a[108:111], v[164:167], v[184:187]\n\t" \
  "ds_read_b64_tr_b16 a[52:53], %[va5] offset:40960\n\t" \
  "ds_read_b64_tr_b16 a[54:55], %[va5] offset:41984\n\t" \
  "v_mfma_f32_16x16x32_bf16 v[188:191], a[124:127], v[164:167], v[188:191]\n\t" \
  "ds_read_b64_tr_b16 a[56:57], %[va6] offset:40960\n\t" \
  "ds_read_b64_tr_b16 a[58:59], %[va6] offset:41984\n\t" \
  "v_mfma_f32_16x16x32_bf16 v[68:71], a[0:3], v[196:199], v[68:71]\n\t" \
  "ds_read_b64_tr_b16 a[60:61], %[va7] offset:40960\n\t" \
  "buffer_load_dwordx4 %[vv0], %[rv], %[sv] offen offset:1024 nt lds\n\t" \
  "v_mfma_f32_16x16x32_bf16 v[76:79], a[4:7], v[196:199], v[76:79]\n\t" \
  "ds_read_b64_tr_b16 a[62:63], %[va7] offset:41984\n\t" \
  "v_exp_f32 v208, v184\n\t" \
  "v_mfma_f32_16x16x32_bf16 v[84:87], a[8:11], v[196:199], v[84:87]\n\t" \
  "v_exp_f32 v209, v185\n\t" \
  "v_mfma_f32_16x16x32_bf16 v[92:95], a[12:15], v[196:199], v[92:95]\n\t" \
  "v_exp_f32 v210, v186\n\t" \
  "v_mfma_f32_16x16x32_bf16 v[100:103], a[16:19], v[196:199], v[100:103]\n\t" \
  "v_exp_f32 v211, v187\n\t" \
  "v_mfma_f32_16x16x32_bf16 v[108:111], a[20:23], v[196:199], v[108:111]\n\t" \
  "v_exp_f32 v212, v188\n\t" \
  "v_mfma_f32_16x16x32_bf16 v[116:119], a[24:27], v[196:199], v[116:119]\n\t" \
  "v_exp_f32 v213, v189\n\t" \
  "v_mfma_f32_16x16x32_bf16 v[124:127], a[28:31], v[196:199], v[124:127]\n\t" \
  "v_exp_f32 v214, v190\n\t" \
  "v_mfma_f32_16x16x32_bf16 v[132:135], a[128:131], v[196:199], v[132:135]\n\t" \
  "v_exp_f32 v215, v191\n\t" \
  "s_waitcnt vmcnt(8) lgkmcnt(0)\n\t" \
  "s_add_u32 m0, %[mkv], 32768\n\t" \
  "s_add_u32 %[sk], %[sk], 0x2000\n\t" \
  "s_barrier\n\t" \
  "v_mfma_f32_16x16x32_bf16 v[168:171], a[64:67], v[136:139], v[200:203]\n\t" \
  "buffer_load_dwordx4 %[kv0], %[rk], %[sk] offen offset:0 nt lds\n\t" \
  "v_cvt_pk_bf16_f32 v196, v208, v209\n\t" \
  "v_mfma_f32_16x16x32_bf16 v[172:175], a[80:83], v[136:139], v[200:203]\n\t" \
  "ds_read_b128 a[96:99], %[ka0] offset:49152\n\t" \
  "v_cvt_pk_bf16_f32 v197, v210, v211\n\t" \
  "v_mfma_f32_16x16x32_bf16 v[168:171], a[68:71], v[140:143], v[168:171]\n\t" \
  "ds_read_b128 a[112:115], %[ka0] offset:50176\n\t" \
  "v_cvt_pk_bf16_f32 v198, v212, v213\n\t" \
  "v_mfma_f32_16x16x32_bf16 v[172:175], a[84:87], v[140:143], v[172:175]\n\t" \
  "ds_read_b128 a[100:103], %[ka1] offset:49152\n\t" \
  "v_cvt_pk_bf16_f32 v199, v214, v215\n\t" \
  "v_mfma_f32_16x16x32_bf16 v[168:171], a[72:75], v[144:147], v[168:171]\n\t" \
  "ds_read_b128 a[116:119], %[ka1] offset:50176\n\t" \
  "v_or3_b32 v224, v196, v197, v224\n\t" \
  "v_mfma_f32_16x16x32_bf16 v[172:175], a[88:91], v[144:147], v[172:175]\n\t" \
  "ds_read_b128 a[104:107], %[ka2] offset:49152\n\t" \
  "v_or3_b32 v224, v198, v199, v224\n\t" \
  "v_mfma_f32_16x16x32_bf16 v[168:171], a[76:79], v[148:151], v[168:171]\n\t" \
  "ds_read_b128 a[120:123], %[ka2] offset:50176\n\t" \
  "v_and_b32 v216, 0x40004000, v224\n\t" \
  "v_mfma_f32_16x16x32_bf16 v[172:175], a[92:95], v[148:151], v[172:175]\n\t" \
  "v_cmp_ne_u32 vcc, 0, v216\n\t" \
  "s_cbranch_vccnz .Lrare1_%=\n\t" \
  ".Lback1_%=:\n\t" \
  "v_mfma_f32_16x16x32_bf16 v[64:67], a[32:35], v[192:195], v[64:67]\n\t" \
  "ds_read_b128 a[108:111], %[ka3] offset:49152\n\t" \
  "buffer_load_dwordx4 %[kv0], %[rk], %[sk] offen offset:1024 nt lds\n\t" \
  "v_mfma_f32_16x16x32_bf16 v[72:75], a[36:39], v[192:195], v[72:75]\n\t" \
  "ds_read_b128 a[124:127], %[ka3] offset:50176\n\t" \
  "v_exp_f32 v208, v168\n\t" \
  "v_mfma_f32_16x16x32_bf16 v[80:83], a[40:43], v[192:195], v[80:83]\n\t" \
  "ds_read_b64_tr_b16 a[0:1], %[va0] offset:57344\n\t" \
  "v_exp_f32 v209, v169\n\t" \
  "v_mfma_f32_16x16x32_bf16 v[88:91], a[44:47], v[192:195], v[88:91]\n\t" \
  "ds_read_b64_tr_b16 a[2:3], %[va0] offset:58368\n\t" \
  "v_exp_f32 v210, v170\n\t" \
  "v_mfma_f32_16x16x32_bf16 v[96:99], a[48:51], v[192:195], v[96:99]\n\t" \
  "ds_read_b64_tr_b16 a[4:5], %[va1] offset:57344\n\t" \
  "v_exp_f32 v211, v171\n\t" \
  "v_mfma_f32_16x16x32_bf16 v[104:107], a[52:55], v[192:195], v[104:107]\n\t" \
  "ds_read_b64_tr_b16 a[6:7], %[va1] offset:58368\n\t" \
  "v_exp_f32 v212, v172\n\t" \
  "v_mfma_f32_16x16x32_bf16 v[112:115], a[56:59], v[192:195], v[112:115]\n\t" \
  "ds_read_b64_tr_b16 a[8:9], %[va2] offset:57344\n\t" \
  "v_exp_f32 v213, v173\n\t" \
  "v_mfma_f32_16x16x32_bf16 v[120:123], a[60:63], v[192:195], v[120:123]\n\t" \
  "s_add_u32 m0, %[mkv], 40960\n\t" \
  "v_exp_f32 v214, v174\n\t" \
  "v_mfma_f32_16x16x32_bf16 v[128:131], a[128:131], v[192:195], v[128:131]\n\t" \
  "s_add_u32 %[sv], %[sv], 0x2000\n\t" \
  "v_exp_f32 v215, v175\n\t" \
  "v_mfma_f32_16x16x32_bf16 v[184:187], a[64:67], v[152:155], v[204:207]\n\t" \
  "buffer_load_dwordx4 %[vv0], %[rv], %[sv] offen offset:0 nt lds\n\t" \
  "v_cvt_pk_bf16_f32 v192, v208, v209\n\t" \
  "v_mfma_f32_16x16x32_bf16 v[188:191], a[80:83], v[152:155], v[204:207]\n\t" \
  "ds_read_b64_tr_b16 a[10:11], %[va2] offset:58368\n\t" \
  "v_cvt_pk_bf16_f32 v193, v210, v211\n\t" \
  "v_mfma_f32_16x16x32_bf16 v[184:187], a[68:71], v[156:159], v[184:187]\n\t" \
  "ds_read_b64_tr_b16 a[12:13], %[va3] offset:57344\n\t" \
  "v_cvt_pk_bf16_f32 v194, v212, v213\n\t" \
  "v_mfma_f32_16x16x32_bf16 v[188:191], a[84:87], v[156:159], v[188:191]\n\t" \
  "ds_read_b64_tr_b16 a[14:15], %[va3] offset:58368\n\t" \
  "v_cvt_pk_bf16_f32 v195, v214, v215\n\t" \
  "v_mfma_f32_16x16x32_bf16 v[184:187], a[72:75], v[160:163], v[184:187]\n\t" \
  "ds_read_b64_tr_b16 a[16:17], %[va4] offset:57344\n\t" \
  "v_or_b32 v224, v192, v193\n\t" \
  "v_mfma_f32_16x16x32_bf16 v[188:191], a[88:91], v[160:163], v[188:191]\n\t" \
  "ds_read_b64_tr_b16 a[18:19], %[va4] offset:58368\n\t" \
  "v_or3_b32 v224, v194, v195, v224\n\t" \
  "v_mfma_f32_16x16x32_bf16 v[184:187], a[76:79], v[164:167], v[184:187]\n\t" \
  "ds_read_b64_tr_b16 a[20:21], %[va5] offset:57344\n\t" \
  "ds_read_b64_tr_b16 a[22:23], %[va5] offset:58368\n\t" \
  "v_mfma_f32_16x16x32_bf16 v[188:191], a[92:95], v[164:167], v[188:191]\n\t" \
  "ds_read_b64_tr_b16 a[24:25], %[va6] offset:57344\n\t" \
  "ds_read_b64_tr_b16 a[26:27], %[va6] offset:58368\n\t" \
  "v_mfma_f32_16x16x32_bf16 v[68:71], a[32:35], v[196:199], v[68:71]\n\t" \
  "ds_read_b64_tr_b16 a[28:29], %[va7] offset:57344\n\t" \
  "buffer_load_dwordx4 %[vv0], %[rv], %[sv] offen offset:1024 nt lds\n\t" \
  "v_mfma_f32_16x16x32_bf16 v[76:79], a[36:39], v[196:199], v[76:79]\n\t" \
  "ds_read_b64_tr_b16 a[30:31], %[va7] offset:58368\n\t" \
  "v_exp_f32 v208, v184\n\t" \
  "v_mfma_f32_16x16x32_bf16 v[84:87], a[40:43], v[196:199], v[84:87]\n\t" \
  "v_exp_f32 v209, v185\n\t" \
  "v_mfma_f32_16x16x32_bf16 v[92:95], a[44:47], v[196:199], v[92:95]\n\t" \
  "v_exp_f32 v210, v186\n\t" \
  "v_mfma_f32_16x16x32_bf16 v[100:103], a[48:51], v[196:199], v[100:103]\n\t" \
  "v_exp_f32 v211, v187\n\t" \
  "v_mfma_f32_16x16x32_bf16 v[108:111], a[52:55], v[196:199], v[108:111]\n\t" \
  "v_exp_f32 v212, v188\n\t" \
  "v_mfma_f32_16x16x32_bf16 v[116:119], a[56:59], v[196:199], v[116:119]\n\t" \
  "v_exp_f32 v213, v189\n\t" \
  "v_mfma_f32_16x16x32_bf16 v[124:127], a[60:63], v[196:199], v[124:127]\n\t" \
  "v_exp_f32 v214, v190\n\t" \
  "v_mfma_f32_16x16x32_bf16 v[132:135], a[128:131], v[196:199], v[132:135]\n\t" \
  "v_exp_f32 v215, v191\n\t" \
  "s_waitcnt vmcnt(8) lgkmcnt(0)\n\t" \
  "s_add_u32 m0, %[mkv], 49152\n\t" \
  "s_add_u32 %[sk], %[sk], 0x2000\n\t" \
  "s_barrier\n\t" \
  "v_mfma_f32_16x16x32_bf16 v[176:179], a[96:99], v[136:139], v[200:203]\n\t" \
  "buffer_load_dwordx4 %[kv0], %[rk], %[sk] offen offset:0 nt lds\n\t" \
  "v_cvt_pk_bf16_f32 v196, v208, v209\n\t" \
  "v_mfma_f32_16x16x32_bf16 v[180:183], a[112:115], v[136:139], v[200:203]\n\t" \
  "ds_read_b128 a[64:67], %[ka0] offset:0\n\t" \
  "v_cvt_pk_bf16_f32 v197, v210, v211\n\t" \
  "v_mfma_f32_16x16x32_bf16 v[176:179], a[100:103], v[140:143], v[176:179]\n\t" \
  "ds_read_b128 a[80:83], %[ka0] offset:1024\n\t" \
  "v_cvt_pk_bf16_f32 v198, v212, v213\n\t" \
  "v_mfma_f32_16x16x32_bf16 v[180:183], a[116:119], v[140:143], v[180:183]\n\t" \
  "ds_read_b128 a[68:71], %[ka1] offset:0\n\t" \
  "v_cvt_pk_bf16_f32 v199, v214, v215\n\t" \
  "v_mfma_f32_16x16x32_bf16 v[176:179], a[104:107], v[144:147], v[176:179]\n\t" \
  "ds_read_b128 a[84:87], %[ka1] offset:1024\n\t" \
  "v_or3_b32 v224, v196, v197, v224\n\t" \
  "v_mfma_f32_16x16x32_bf16 v[180:183], a[120:123], v[144:147], v[180:183]\n\t" \
  "ds_read_b128 a[72:75], %[ka2] offset:0\n\t" \
  "v_or3_b32 v224, v198, v199, v224\n\t" \
  "v_mfma_f32_16x16x32_bf16 v[176:179], a[108:111], v[148:151], v[176:179]\n\t" \
  "ds_read_b128 a[88:91], %[ka2] offset:1024\n\t" \
  "v_and_b32 v216, 0x40004000, v224\n\t" \
  "v_mfma_f32_16x16x32_bf16 v[180:183], a[124:127], v[148:151], v[180:183]\n\t" \
  "v_cmp_ne_u32 vcc, 0, v216\n\t" \
  "s_cbranch_vccnz .Lrare2_%=\n\t" \
  ".Lback2_%=:\n\t" \
  "v_mfma_f32_16x16x32_bf16 v[64:67], a[0:3], v[192:195], v[64:67]\n\t" \
  "ds_read_b128 a[76:79], %[ka3] offset:0\n\t" \
  "buffer_load_dwordx4 %[kv0], %[rk], %[sk] offen offset:1024 nt lds\n\t" \
  "v_mfma_f32_16x16x32_bf16 v[72:75], a[4:7], v[192:195], v[72:75]\n\t" \
  "ds_read_b128 a[92:95], %[ka3] offset:1024\n\t" \
  "v_exp_f32 v208, v176\n\t" \
  "v_mfma_f32_16x16x32_bf16 v[80:83], a[8:11], v[192:195], v[80:83]\n\t" \
  "ds_read_b64_tr_b16 a[32:33], %[va0] offset:8192\n\t" \
  "v_exp_f32 v209, v177\n\t" \
  "v_mfma_f32_16x16x32_bf16 v[88:91], a[12:15], v[192:195], v[88:91]\n\t" \
  "ds_read_b64_tr_b16 a[34:35], %[va0] offset:9216\n\t" \
  "v_exp_f32 v210, v178\n\t" \
  "v_mfma_f32_16x16x32_bf16 v[96:99], a[16:19], v[192:195], v[96:99]\n\t" \
  "ds_read_b64_tr_b16 a[36:37], %[va1] offset:8192\n\t" \
  "v_exp_f32 v211, v179\n\t" \
  "v_mfma_f32_16x16x32_bf16 v[104:107], a[20:23], v[192:195], v[104:107]\n\t" \
  "ds_read_b64_tr_b16 a[38:39], %[va1] offset:9216\n\t" \
  "v_exp_f32 v212, v180\n\t" \
  "v_mfma_f32_16x16x32_bf16 v[112:115], a[24:27], v[192:195], v[112:115]\n\t" \
  "ds_read_b64_tr_b16 a[40:41], %[va2] offset:8192\n\t" \
  "v_exp_f32 v213, v181\n\t" \
  "v_mfma_f32_16x16x32_bf16 v[120:123], a[28:31], v[192:195], v[120:123]\n\t" \
  "s_add_u32 m0, %[mkv], 57344\n\t" \
  "v_exp_f32 v214, v182\n\t" \
  "v_mfma_f32_16x16x32_bf16 v[128:131], a[128:131], v[192:195], v[128:131]\n\t" \
  "s_add_u32 %[sv], %[sv], 0x2000\n\t" \
  "v_exp_f32 v215, v183\n\t" \
  "v_mfma_f32_16x16x32_bf16 v[184:187], a[96:99], v[152:155], v[204:207]\n\t" \
  "buffer_load_dwordx4 %[vv0], %[rv], %[sv] offen offset:0 nt lds\n\t" \
  "v_cvt_pk_bf16_f32 v192, v208, v209\n\t" \
  "v_mfma_f32_16x16x32_bf16 v[188:191], a[112:115], v[152:155], v[204:207]\n\t" \
  "ds_read_b64_tr_b16 a[42:43], %[va2] offset:9216\n\t" \
  "v_cvt_pk_bf16_f32 v193, v210, v211\n\t" \
  "v_mfma_f32_16x16x32_bf16 v[184:187], a[100:103], v[156:159], v[184:187]\n\t" \
  "ds_read_b64_tr_b16 a[44:45], %[va3] offset:8192\n\t" \
  "v_cvt_pk_bf16_f32 v194, v212, v213\n\t" \
  "v_mfma_f32_16x16x32_bf16 v[188:191], a[116:119], v[156:159], v[188:191]\n\t" \
  "ds_read_b64_tr_b16 a[46:47], %[va3] offset:9216\n\t" \
  "v_cvt_pk_bf16_f32 v195, v214, v215\n\t" \
  "v_mfma_f32_16x16x32_bf16 v[184:187], a[104:107], v[160:163], v[184:187]\n\t" \
  "ds_read_b64_tr_b16 a[48:49], %[va4] offset:8192\n\t" \
  "v_or_b32 v224, v192, v193\n\t" \
  "v_mfma_f32_16x16x32_bf16 v[188:191], a[120:123], v[160:163], v[188:191]\n\t" \
  "ds_read_b64_tr_b16 a[50:51], %[va4] offset:9216\n\t" \
  "v_or3_b32 v224, v194, v195, v224\n\t" \
  "v_mfma_f32_16x16x32_bf16 v[184:187], a[108:111], v[164:167], v[184:187]\n\t" \
  "ds_read_b64_tr_b16 a[52:53], %[va5] offset:8192\n\t" \
  "ds_read_b64_tr_b16 a[54:55], %[va5] offset:9216\n\t" \
  "v_mfma_f32_16x16x32_bf16 v[188:191], a[124:127], v[164:167], v[188:191]\n\t" \
  "ds_read_b64_tr_b16 a[56:57], %[va6] offset:8192\n\t" \
  "ds_read_b64_tr_b16 a[58:59], %[va6] offset:9216\n\t" \
  "v_mfma_f32_16x16x32_bf16 v[68:71], a[0:3], v[196:199], v[68:71]\n\t" \
  "ds_read_b64_tr_b16 a[60:61], %[va7] offset:8192\n\t" \
  "buffer_load_dwordx4 %[vv0], %[rv], %[sv] offen offset:1024 nt lds\n\t" \
  "v_mfma_f32_16x16x32_bf16 v[76:79], a[4:7], v[196:199], v[76:79]\n\t" \
  "ds_read_b64_tr_b16 a[62:63], %[va7] offset:9216\n\t" \
  "v_exp_f32 v208, v184\n\t" \
  "v_mfma_f32_16x16x32_bf16 v[84:87], a[8:11], v[196:199], v[84:87]\n\t" \
  "v_exp_f32 v209, v185\n\t" \
  "v_mfma_f32_16x16x32_bf16 v[92:95], a[12:15], v[196:199], v[92:95]\n\t" \
  "v_exp_f32 v210, v186\n\t" \
  "v_mfma_f32_16x16x32_bf16 v[100:103], a[16:19], v[196:199], v[100:103]\n\t" \
  "v_exp_f32 v211, v187\n\t" \
  "v_mfma_f32_16x16x32_bf16 v[108:111], a[20:23], v[196:199], v[108:111]\n\t" \
  "v_exp_f32 v212, v188\n\t" \
  "v_mfma_f32_16x16x32_bf16 v[116:119], a[24:27], v[196:199], v[116:119]\n\t" \
  "v_exp_f32 v213, v189\n\t" \
  "v_mfma_f32_16x16x32_bf16 v[124:127], a[28:31], v[196:199], v[124:127]\n\t" \
  "v_exp_f32 v214, v190\n\t" \
  "v_mfma_f32_16x16x32_bf16 v[132:135], a[128:131], v[196:199], v[132:135]\n\t" \
  "v_exp_f32 v215, v191\n\t" \
  "s_waitcnt vmcnt(8) lgkmcnt(0)\n\t" \
  "s_add_u32 m0, %[mkv], 0\n\t" \
  "s_add_u32 %[sk], %[sk], 0x2000\n\t" \
  "s_barrier\n\t" \
  "v_mfma_f32_16x16x32_bf16 v[168:171], a[64:67], v[136:139], v[200:203]\n\t" \
  "buffer_load_dwordx4 %[kv0], %[rk], %[sk] offen offset:0 nt lds\n\t" \
  "v_cvt_pk_bf16_f32 v196, v208, v209\n\t" \
  "v_mfma_f32_16x16x32_bf16 v[172:175], a[80:83], v[136:139], v[200:203]\n\t" \
  "ds_read_b128 a[96:99], %[ka0] offset:16384\n\t" \
  "v_cvt_pk_bf16_f32 v197, v210, v211\n\t" \
  "v_mfma_f32_16x16x32_bf16 v[168:171], a[68:71], v[140:143], v[168:171]\n\t" \
  "ds_read_b128 a[112:115], %[ka0] offset:17408\n\t" \
  "v_cvt_pk_bf16_f32 v198, v212, v213\n\t" \
  "v_mfma_f32_16x16x32_bf16 v[172:175], a[84:87], v[140:143], v[172:175]\n\t" \
  "ds_read_b128 a[100:103], %[ka1] offset:16384\n\t" \
  "v_cvt_pk_bf16_f32 v199, v214, v215\n\t" \
  "v_mfma_f32_16x16x32_bf16 v[168:171], a[72:75], v[144:147], v[168:171]\n\t" \
  "ds_read_b128 a[116:119], %[ka1] offset:17408\n\t" \
  "v_or3_b32 v224, v196, v197, v224\n\t" \
  "v_mfma_f32_16x16x32_bf16 v[172:175], a[88:91], v[144:147], v[172:175]\n\t" \
  "ds_read_b128 a[104:107], %[ka2] offset:16384\n\t" \
  "v_or3_b32 v224, v198, v199, v224\n\t" \
  "v_mfma_f32_16x16x32_bf16 v[168:171], a[76:79], v[148:151], v[168:171]\n\t" \
  "ds_read_b128 a[120:123], %[ka2] offset:17408\n\t" \
  "v_and_b32 v216, 0x40004000, v224\n\t" \
  "v_mfma_f32_16x16x32_bf16 v[172:175], a[92:95], v[148:151], v[172:175]\n\t" \
  "v_cmp_ne_u32 vcc, 0, v216\n\t" \
  "s_cbranch_vccnz .Lrare3_%=\n\t" \
  ".Lback3_%=:\n\t" \
  "v_mfma_f32_16x16x32_bf16 v[64:67], a[32:35], v[192:195], v[64:67]\n\t" \
  "ds_read_b128 a[108:111], %[ka3] offset:16384\n\t" \
  "buffer_load_dwordx4 %[kv0], %[rk], %[sk] offen offset:1024 nt lds\n\t" \
  "v_mfma_f32_16x16x32_bf16 v[72:75], a[36:39], v[192:195], v[72:75]\n\t" \
  "ds_read_b128 a[124:127], %[ka3] offset:17408\n\t" \
  "v_exp_f32 v208, v168\n\t" \
  "v_mfma_f32_16x16x32_bf16 v[80:83], a[40:43], v[192:195], v[80:83]\n\t" \
  "ds_read_b64_tr_b16 a[0:1], %[va0] offset:24576\n\t" \
  "v_exp_f32 v209, v169\n\t" \
  "v_mfma_f32_16x16x32_bf16 v[88:91], a[44:47], v[192:195], v[88:91]\n\t" \
  "ds_read_b64_tr_b16 a[2:3], %[va0] offset:25600\n\t" \
  "v_exp_f32 v210, v170\n\t" \
  "v_mfma_f32_16x16x32_bf16 v[96:99], a[48:51], v[192:195], v[96:99]\n\t" \
  "ds_read_b64_tr_b16 a[4:5], %[va1] offset:24576\n\t" \
  "v_exp_f32 v211, v171\n\t" \
  "v_mfma_f32_16x16x32_bf16 v[104:107], a[52:55], v[192:195], v[104:107]\n\t" \
  "ds_read_b64_tr_b16 a[6:7], %[va1] offset:25600\n\t" \
  "v_exp_f32 v212, v172\n\t" \
  "v_mfma_f32_16x16x32_bf16 v[112:115], a[56:59], v[192:195], v[112:115]\n\t" \
  "ds_read_b64_tr_b16 a[8:9], %[va2] offset:24576\n\t" \
  "v_exp_f32 v213, v173\n\t" \
  "v_mfma_f32_16x16x32_bf16 v[120:123], a[60:63], v[192:195], v[120:123]\n\t" \
  "s_add_u32 m0, %[mkv], 8192\n\t" \
  "v_exp_f32 v214, v174\n\t" \
  "v_mfma_f32_16x16x32_bf16 v[128:131], a[128:131], v[192:195], v[128:131]\n\t" \
  "s_add_u32 %[sv], %[sv], 0x2000\n\t" \
  "v_exp_f32 v215, v175\n\t" \
  "v_mfma_f32_16x16x32_bf16 v[184:187], a[64:67], v[152:155], v[204:207]\n\t" \
  "buffer_load_dwordx4 %[vv0], %[rv], %[sv] offen offset:0 nt lds\n\t" \
  "v_cvt_pk_bf16_f32 v192, v208, v209\n\t" \
  "v_mfma_f32_16x16x32_bf16 v[188:191], a[80:83], v[152:155], v[204:207]\n\t" \
  "ds_read_b64_tr_b16 a[10:11], %[va2] offset:25600\n\t" \
  "v_cvt_pk_bf16_f32 v193, v210, v211\n\t" \
  "v_mfma_f32_16x16x32_bf16 v[184:187], a[68:71], v[156:159], v[184:187]\n\t" \
  "ds_read_b64_tr_b16 a[12:13], %[va3] offset:24576\n\t" \
  "v_cvt_pk_bf16_f32 v194, v212, v213\n\t" \
  "v_mfma_f32_16x16x32_bf16 v[188:191], a[84:87], v[156:159], v[188:191]\n\t" \
  "ds_read_b64_tr_b16 a[14:15], %[va3] offset:25600\n\t" \
  "v_cvt_pk_bf16_f32 v195, v214, v215\n\t" \
  "v_mfma_f32_16x16x32_bf16 v[184:187], a[72:75], v[160:163], v[184:187]\n\t" \
  "ds_read_b64_tr_b16 a[16:17], %[va4] offset:24576\n\t" \
  "v_or_b32 v224, v192, v193\n\t" \
  "v_mfma_f32_16x16x32_bf16 v[188:191], a[88:91], v[160:163], v[188:191]\n\t" \
  "ds_read_b64_tr_b16 a[18:19], %[va4] offset:25600\n\t" \
  "v_or3_b32 v224, v194, v195, v224\n\t" \
  "v_mfma_f32_16x16x32_bf16 v[184:187], a[76:79], v[164:167], v[184:187]\n\t" \
  "ds_read_b64_tr_b16 a[20:21], %[va5] offset:24576\n\t" \
  "ds_read_b64_tr_b16 a[22:23], %[va5] offset:25600\n\t" \
  "v_mfma_f32_16x16x32_bf16 v[188:191], a[92:95], v[164:167], v[188:191]\n\t" \
  "ds_read_b64_tr_b16 a[24:25], %[va6] offset:24576\n\t" \
  "ds_read_b64_tr_b16 a[26:27], %[va6] offset:25600\n\t" \
  "v_mfma_f32_16x16x32_bf16 v[68:71], a[32:35], v[196:199], v[68:71]\n\t" \
  "ds_read_b64_tr_b16 a[28:29], %[va7] offset:24576\n\t" \
  "buffer_load_dwordx4 %[vv0], %[rv], %[sv] offen offset:1024 nt lds\n\t" \
  "v_mfma_f32_16x16x32_bf16 v[76:79], a[36:39], v[196:199], v[76:79]\n\t" \
  "ds_read_b64_tr_b16 a[30:31], %[va7] offset:25600\n\t" \
  "v_exp_f32 v208, v184\n\t" \
  "v_mfma_f32_16x16x32_bf16 v[84:87], a[40:43], v[196:199], v[84:87]\n\t" \
  "v_exp_f32 v209, v185\n\t" \
  "v_mfma_f32_16x16x32_bf16 v[92:95], a[44:47], v[196:199], v[92:95]\n\t" \
  "v_exp_f32 v210, v186\n\t" \
  "v_mfma_f32_16x16x32_bf16 v[100:103], a[48:51], v[196:199], v[100:103]\n\t" \
  "v_exp_f32 v211, v187\n\t" \
  "v_mfma_f32_16x16x32_bf16 v[108:111], a[52:55], v[196:199], v[108:111]\n\t" \
  "v_exp_f32 v212, v188\n\t" \
  "v_mfma_f32_16x16x32_bf16 v[116:119], a[56:59], v[196:199], v[116:119]\n\t" \
  "v_exp_f32 v213, v189\n\t" \
  "v_mfma_f32_16x16x32_bf16 v[124:127], a[60:63], v[196:199], v[124:127]\n\t" \
  "v_exp_f32 v214, v190\n\t" \
  "v_mfma_f32_16x16x32_bf16 v[132:135], a[128:131], v[196:199], v[132:135]\n\t" \
  "s_sub_u32 %[cnt], %[cnt], 1\n\t" \
  "v_exp_f32 v215, v191\n\t" \
  "s_cmp_lg_u32 %[cnt], 0\n\t" \
  "s_cbranch_scc1 .Lloop_%=\n\t" \
  "s_waitcnt vmcnt(8) lgkmcnt(0)\n\t" \
  "s_add_u32 m0, %[mw], 0\n\t" \
  "s_add_u32 %[st], %[so0], 0\n\t" \
  "s_barrier\n\t" \
  "v_mfma_f32_16x16x32_bf16 v[176:179], a[96:99], v[136:139], v[200:203]\n\t" \
  "buffer_load_dwordx4 %[ov0], %[ro], %[st] offen offset:0 lds\n\t" \
  "v_cvt_pk_bf16_f32 v196, v208, v209\n\t" \
  "v_mfma_f32_16x16x32_bf16 v[180:183], a[112:115], v[136:139], v[200:203]\n\t" \
  "ds_read_b128 a[64:67], %[ka0] offset:32768\n\t" \
  "v_cvt_pk_bf16_f32 v197, v210, v211\n\t" \
  "v_mfma_f32_16x16x32_bf16 v[176:179], a[100:103], v[140:143], v[176:179]\n\t" \
  "ds_read_b128 a[80:83], %[ka0] offset:33792\n\t" \
  "v_cvt_pk_bf16_f32 v198, v212, v213\n\t" \
  "v_mfma_f32_16x16x32_bf16 v[180:183], a[116:119], v[140:143], v[180:183]\n\t" \
  "ds_read_b128 a[68:71], %[ka1] offset:32768\n\t" \
  "v_cvt_pk_bf16_f32 v199, v214, v215\n\t" \
  "v_mfma_f32_16x16x32_bf16 v[176:179], a[104:107], v[144:147], v[176:179]\n\t" \
  "buffer_load_dwordx4 %[ov1], %[ro], %[st] offen offset:1024 lds\n\t" \
  "v_or3_b32 v224, v196, v197, v224\n\t" \
  "v_mfma_f32_16x16x32_bf16 v[180:183], a[120:123], v[144:147], v[180:183]\n\t" \
  "ds_read_b128 a[84:87], %[ka1] offset:33792\n\t" \
  "v_or3_b32 v224, v198, v199, v224\n\t" \
  "v_mfma_f32_16x16x32_bf16 v[176:179], a[108:111], v[148:151], v[176:179]\n\t" \
  "ds_read_b128 a[72:75], %[ka2] offset:32768\n\t" \
  "v_and_b32 v216, 0x40004000, v224\n\t" \
  "v_mfma_f32_16x16x32_bf16 v[180:183], a[124:127], v[148:151], v[180:183]\n\t" \
  "v_cmp_ne_u32 vcc, 0, v216\n\t" \
  "s_cbranch_vccnz .Lrare4_%=\n\t" \
  ".Lback4_%=:\n\t" \
  "v_mfma_f32_16x16x32_bf16 v[64:67], a[0:3], v[192:195], v[64:67]\n\t" \
  "ds_read_b128 a[88:91], %[ka2] offset:33792\n\t" \
  "buffer_load_dwordx4 %[ov2], %[ro], %[st] offen offset:2048 lds\n\t" \
  "v_mfma_f32_16x16x32_bf16 v[72:75], a[4:7], v[192:195], v[72:75]\n\t" \
  "ds_read_b128 a[76:79], %[ka3] offset:32768\n\t" \
  "v_exp_f32 v208, v176\n\t" \
  "v_mfma_f32_16x16x32_bf16 v[80:83], a[8:11], v[192:195], v[80:83]\n\t" \
  "ds_read_b128 a[92:95], %[ka3] offset:33792\n\t" \
  "v_exp_f32 v209, v177\n\t" \
  "v_mfma_f32_16x16x32_bf16 v[88:91], a[12:15], v[192:195], v[88:91]\n\t" \
  "ds_read_b64_tr_b16 a[32:33], %[va0] offset:40960\n\t" \
  "v_exp_f32 v210, v178\n\t" \
  "v_mfma_f32_16x16x32_bf16 v[96:99], a[16:19], v[192:195], v[96:99]\n\t" \
  "buffer_load_dwordx4 %[ov3], %[ro], %[st] offen offset:3072 lds\n\t" \
  "v_exp_f32 v211, v179\n\t" \
  "v_mfma_f32_16x16x32_bf16 v[104:107], a[20:23], v[192:195], v[104:107]\n\t" \
  "ds_read_b64_tr_b16 a[34:35], %[va0] offset:41984\n\t" \
  "v_exp_f32 v212, v180\n\t" \
  "v_mfma_f32_16x16x32_bf16 v[112:115], a[24:27], v[192:195], v[112:115]\n\t" \
  "ds_read_b64_tr_b16 a[36:37], %[va1] offset:40960\n\t" \
  "v_exp_f32 v213, v181\n\t" \
  "v_mfma_f32_16x16x32_bf16 v[120:123], a[28:31], v[192:195], v[120:123]\n\t" \
  "s_add_u32 m0, %[mw], 4096\n\t" \
  "v_exp_f32 v214, v182\n\t" \
  "v_mfma_f32_16x16x32_bf16 v[128:131], a[128:131], v[192:195], v[128:131]\n\t" \
  "s_add_u32 %[st], %[so0], 8192\n\t" \
  "v_exp_f32 v215, v183\n\t" \
  "v_mfma_f32_16x16x32_bf16 v[184:187], a[96:99], v[152:155], v[204:207]\n\t" \
  "buffer_load_dwordx4 %[ov0], %[ro], %[st] offen offset:0 lds\n\t" \
  "v_cvt_pk_bf16_f32 v192, v208, v209\n\t" \
  "v_mfma_f32_16x16x32_bf16 v[188:191], a[112:115], v[152:155], v[204:207]\n\t" \
  "ds_read_b64_tr_b16 a[38:39], %[va1] offset:41984\n\t" \
  "v_cvt_pk_bf16_f32 v193, v210, v211\n\t" \
  "v_mfma_f32_16x16x32_bf16 v[184:187], a[100:103], v[156:159], v[184:187]\n\t" \
  "ds_read_b64_tr_b16 a[40:41], %[va2] offset:40960\n\t" \
  "v_cvt_pk_bf16_f32 v194, v212, v213\n\t" \
  "v_mfma_f32_16x16x32_bf16 v[188:191], a[116:119], v[156:159], v[188:191]\n\t" \
  "ds_read_b64_tr_b16 a[42:43], %[va2] offset:41984\n\t" \
  "v_cvt_pk_bf16_f32 v195, v214, v215\n\t" \
  "v_mfma_f32_16x16x32_bf16 v[184:187], a[104:107], v[160:163], v[184:187]\n\t" \
  "buffer_load_dwordx4 %[ov1], %[ro], %[st] offen offset:1024 lds\n\t" \
  "v_or_b32 v224, v192, v193\n\t" \
  "v_mfma_f32_16x16x32_bf16 v[188:191], a[120:123], v[160:163], v[188:191]\n\t" \
  "ds_read_b64_tr_b16 a[44:45], %[va3] offset:40960\n\t" \
  "v_or3_b32 v224, v194, v195, v224\n\t" \
  "v_mfma_f32_16x16x32_bf16 v[184:187], a[108:111], v[164:167], v[184:187]\n\t" \
  "ds_read_b64_tr_b16 a[46:47], %[va3] offset:41984\n\t" \
  "ds_read_b64_tr_b16 a[48:49], %[va4] offset:40960\n\t" \
  "v_mfma_f32_16x16x32_bf16 v[188:191], a[124:127], v[164:167], v[188:191]\n\t" \
  "ds_read_b64_tr_b16 a[50:51], %[va4] offset:41984\n\t" \
  "ds_read_b64_tr_b16 a[52:53], %[va5] offset:40960\n\t" \
  "v_mfma_f32_16x16x32_bf16 v[68:71], a[0:3], v[196:199], v[68:71]\n\t" \
  "ds_read_b64_tr_b16 a[54:55], %[va5] offset:41984\n\t" \
  "buffer_load_dwordx4 %[ov2], %[ro], %[st] offen offset:2048 lds\n\t" \
  "v_mfma_f32_16x16x32_bf16 v[76:79], a[4:7], v[196:199], v[76:79]\n\t" \
  "ds_read_b64_tr_b16 a[56:57], %[va6] offset:40960\n\t" \
  "v_exp_f32 v208, v184\n\t" \
  "v_mfma_f32_16x16x32_bf16 v[84:87], a[8:11], v[196:199], v[84:87]\n\t" \
  "ds_read_b64_tr_b16 a[58:59], %[va6] offset:41984\n\t" \
  "v_exp_f32 v209, v185\n\t" \
  "v_mfma_f32_16x16x32_bf16 v[92:95], a[12:15], v[196:199], v[92:95]\n\t" \
  "ds_read_b64_tr_b16 a[60:61], %[va7] offset:40960\n\t" \
  "v_exp_f32 v210, v186\n\t" \
  "v_mfma_f32_16x16x32_bf16 v[100:103], a[16:19], v[196:199], v[100:103]\n\t" \
  "buffer_load_dwordx4 %[ov3], %[ro], %[st] offen offset:3072 lds\n\t" \
  "v_exp_f32 v211, v187\n\t" \
  "v_mfma_f32_16x16x32_bf16 v[108:111], a[20:23], v[196:199], v[108:111]\n\t" \
  "ds_read_b64_tr_b16 a[62:63], %[va7] offset:41984\n\t" \
  "v_exp_f32 v212, v188\n\t" \
  "v_mfma_f32_16x16x32_bf16 v[116:119], a[24:27], v[196:199], v[116:119]\n\t" \
  "v_exp_f32 v213, v189\n\t" \
  "v_mfma_f32_16x16x32_bf16 v[124:127], a[28:31], v[196:199], v[124:127]\n\t" \
  "v_exp_f32 v214, v190\n\t" \
  "v_mfma_f32_16x16x32_bf16 v[132:135], a[128:131], v[196:199], v[132:135]\n\t" \
  "v_exp_f32 v215, v191\n\t" \
  "s_waitcnt vmcnt(12) lgkmcnt(0)\n\t" \
  "s_add_u32 m0, %[mw], 8192\n\t" \
  "s_add_u32 %[st], %[so0], 32768\n\t" \
  "s_barrier\n\t" \
  "v_mfma_f32_16x16x32_bf16 v[168:171], a[64:67], v[136:139], v[200:203]\n\t" \
  "buffer_load_dwordx4 %[ov0], %[ro], %[st] offen offset:0 lds\n\t" \
  "v_cvt_pk_bf16_f32 v196, v208, v209\n\t" \
  "v_mfma_f32_16x16x32_bf16 v[172:175], a[80:83], v[136:139], v[200:203]\n\t" \
  "ds_read_b128 a[96:99], %[ka0] offset:49152\n\t" \
  "v_cvt_pk_bf16_f32 v197, v210, v211\n\t" \
  "v_mfma_f32_16x16x32_bf16 v[168:171], a[68:71], v[140:143], v[168:171]\n\t" \
  "ds_read_b128 a[112:115], %[ka0] offset:50176\n\t" \
  "v_cvt_pk_bf16_f32 v198, v212, v213\n\t" \
  "v_mfma_f32_16x16x32_bf16 v[172:175], a[84:87], v[140:143], v[172:175]\n\t" \
  "ds_read_b128 a[100:103], %[ka1] offset:49152\n\t" \
  "v_cvt_pk_bf16_f32 v199, v214, v215\n\t" \
  "v_mfma_f32_16x16x32_bf16 v[168:171], a[72:75], v[144:147], v[168:171]\n\t" \
  "buffer_load_dwordx4 %[ov1], %[ro], %[st] offen offset:1024 lds\n\t" \
  "v_or3_b32 v224, v196, v197, v224\n\t" \
  "v_mfma_f32_16x16x32_bf16 v[172:175], a[88:91], v[144:147], v[172:175]\n\t" \
  "ds_read_b128 a[116:119], %[ka1] offset:50176\n\t" \
  "v_or3_b32 v224, v198, v199, v224\n\t" \
  "v_mfma_f32_16x16x32_bf16 v[168:171], a[76:79], v[148:151], v[168:171]\n\t" \
  "ds_read_b128 a[104:107], %[ka2] offset:49152\n\t" \
  "v_and_b32 v216, 0x40004000, v224\n\t" \
  "v_mfma_f32_16x16x32_bf16 v[172:175], a[92:95], v[148:151], v[172:175]\n\t" \
  "v_cmp_ne_u32 vcc, 0, v216\n\t" \
  "s_cbranch_vccnz .Lrare5_%=\n\t" \
  ".Lback5_%=:\n\t" \
  "v_mfma_f32_16x16x32_bf16 v[64:67], a[32:35], v[192:195], v[64:67]\n\t" \
  "ds_read_b128 a[120:123], %[ka2] offset:50176\n\t" \
  "buffer_load_dwordx4 %[ov2], %[ro], %[st] offen offset:2048 lds\n\t" \
  "v_mfma_f32_16x16x32_bf16 v[72:75], a[36:39], v[192:195], v[72:75]\n\t" \
  "ds_read_b128 a[108:111], %[ka3] offset:49152\n\t" \
  "v_exp_f32 v208, v168\n\t" \
  "v_mfma_f32_16x16x32_bf16 v[80:83], a[40:43], v[192:195], v[80:83]\n\t" \
  "ds_read_b128 a[124:127], %[ka3] offset:50176\n\t" \
  "v_exp_f32 v209, v169\n\t" \
  "v_mfma_f32_16x16x32_bf16 v[88:91], a[44:47], v[192:195], v[88:91]\n\t" \
  "ds_read_b64_tr_b16 a[0:1], %[va0] offset:57344\n\t" \
  "v_exp_f32 v210, v170\n\t" \
  "v_mfma_f32_16x16x32_bf16 v[96:99], a[48:51], v[192:195], v[96:99]\n\t" \
  "buffer_load_dwordx4 %[ov3], %[ro], %[st] offen offset:3072 lds\n\t" \
  "v_exp_f32 v211, v171\n\t" \
  "v_mfma_f32_16x16x32_bf16 v[104:107], a[52:55], v[192:195], v[104:107]\n\t" \
  "ds_read_b64_tr_b16 a[2:3], %[va0] offset:58368\n\t" \
  "v_exp_f32 v212, v172\n\t" \
  "v_mfma_f32_16x16x32_bf16 v[112:115], a[56:59], v[192:195], v[112:115]\n\t" \
  "ds_read_b64_tr_b16 a[4:5], %[va1] offset:57344\n\t" \
  "v_exp_f32 v213, v173\n\t" \
  "v_mfma_f32_16x16x32_bf16 v[120:123], a[60:63], v[192:195], v[120:123]\n\t" \
  "s_add_u32 m0, %[mw], 12288\n\t" \
  "v_exp_f32 v214, v174\n\t" \
  "v_mfma_f32_16x16x32_bf16 v[128:131], a[128:131], v[192:195], v[128:131]\n\t" \
  "s_add_u32 %[st], %[so0], 40960\n\t" \
  "v_exp_f32 v215, v175\n\t" \
  "v_mfma_f32_16x16x32_bf16 v[184:187], a[64:67], v[152:155], v[204:207]\n\t" \
  "buffer_load_dwordx4 %[ov0], %[ro], %[st] offen offset:0 lds\n\t" \
  "v_cvt_pk_bf16_f32 v192, v208, v209\n\t" \
  "v_mfma_f32_16x16x32_bf16 v[188:191], a[80:83], v[152:155], v[204:207]\n\t" \
  "ds_read_b64_tr_b16 a[6:7], %[va1] offset:58368\n\t" \
  "v_cvt_pk_bf16_f32 v193, v210, v211\n\t" \
  "v_mfma_f32_16x16x32_bf16 v[184:187], a[68:71], v[156:159], v[184:187]\n\t" \
  "ds_read_b64_tr_b16 a[8:9], %[va2] offset:57344\n\t" \
  "v_cvt_pk_bf16_f32 v194, v212, v213\n\t" \
  "v_mfma_f32_16x16x32_bf16 v[188:191], a[84:87], v[156:159], v[188:191]\n\t" \
  "ds_read_b64_tr_b16 a[10:11], %[va2] offset:58368\n\t" \
  "v_cvt_pk_bf16_f32 v195, v214, v215\n\t" \
  "v_mfma_f32_16x16x32_bf16 v[184:187], a[72:75], v[160:163], v[184:187]\n\t" \
  "buffer_load_dwordx4 %[ov1], %[ro], %[st] offen offset:1024 lds\n\t" \
  "v_or_b32 v224, v192, v193\n\t" \
  "v_mfma_f32_16x16x32_bf16 v[188:191], a[88:91], v[160:163], v[188:191]\n\t" \
  "ds_read_b64_tr_b16 a[12:13], %[va3] offset:57344\n\t" \
  "v_or3_b32 v224, v194, v195, v224\n\t" \
  "v_mfma_f32_16x16x32_bf16 v[184:187], a[76:79], v[164:167], v[184:187]\n\t" \
  "ds_read_b64_tr_b16 a[14:15], %[va3] offset:58368\n\t" \
  "ds_read_b64_tr_b16 a[16:17], %[va4] offset:57344\n\t" \
  "v_mfma_f32_16x16x32_bf16 v[188:191], a[92:95], v[164:167], v[188:191]\n\t" \
  "ds_read_b64_tr_b16 a[18:19], %[va4] offset:58368\n\t" \
  "ds_read_b64_tr_b16 a[20:21], %[va5] offset:57344\n\t" \
  "v_mfma_f32_16x16x32_bf16 v[68:71], a[32:35], v[196:199], v[68:71]\n\t" \
  "ds_read_b64_tr_b16 a[22:23], %[va5] offset:58368\n\t" \
  "buffer_load_dwordx4 %[ov2], %[ro], %[st] offen offset:2048 lds\n\t" \
  "v_mfma_f32_16x16x32_bf16 v[76:79], a[36:39], v[196:199], v[76:79]\n\t" \
  "ds_read_b64_tr_b16 a[24:25], %[va6] offset:57344\n\t" \
  "v_exp_f32 v208, v184\n\t" \
  "v_mfma_f32_16x16x32_bf16 v[84:87], a[40:43], v[196:199], v[84:87]\n\t" \
  "ds_read_b64_tr_b16 a[26:27], %[va6] offset:58368\n\t" \
  "v_exp_f32 v209, v185\n\t" \
  "v_mfma_f32_16x16x32_bf16 v[92:95], a[44:47], v[196:199], v[92:95]\n\t" \
  "ds_read_b64_tr_b16 a[28:29], %[va7] offset:57344\n\t" \
  "v_exp_f32 v210, v186\n\t" \
  "v_mfma_f32_16x16x32_bf16 v[100:103], a[48:51], v[196:199], v[100:103]\n\t" \
  "buffer_load_dwordx4 %[ov3], %[ro], %[st] offen offset:3072 lds\n\t" \
  "v_exp_f32 v211, v187\n\t" \
  "v_mfma_f32_16x16x32_bf16 v[108:111], a[52:55], v[196:199], v[108:111]\n\t" \
  "ds_read_b64_tr_b16 a[30:31], %[va7] offset:58368\n\t" \
  "v_exp_f32 v212, v188\n\t" \
  "v_mfma_f32_16x16x32_bf16 v[116:119], a[56:59], v[196:199], v[116:119]\n\t" \
  "v_exp_f32 v213, v189\n\t" \
  "v_mfma_f32_16x16x32_bf16 v[124:127], a[60:63], v[196:199], v[124:127]\n\t" \
  "v_exp_f32 v214, v190\n\t" \
  "v_mfma_f32_16x16x32_bf16 v[132:135], a[128:131], v[196:199], v[132:135]\n\t" \
  "v_exp_f32 v215, v191\n\t" \
  "s_waitcnt lgkmcnt(0)\n\t" \
  "s_add_u32 m0, %[mw], 16384\n\t" \
  "s_add_u32 %[st], %[so0], 65536\n\t" \
  "s_barrier\n\t" \
  "v_mfma_f32_16x16x32_bf16 v[176:179], a[96:99], v[136:139], v[200:203]\n\t" \
  "buffer_load_dwordx4 %[ov0], %[ro], %[st] offen offset:0 lds\n\t" \
  "v_cvt_pk_bf16_f32 v196, v208, v209\n\t" \
  "v_mfma_f32_16x16x32_bf16 v[180:183], a[112:115], v[136:139], v[200:203]\n\t" \
  "v_cvt_pk_bf16_f32 v197, v210, v211\n\t" \
  "v_mfma_f32_16x16x32_bf16 v[176:179], a[100:103], v[140:143], v[176:179]\n\t" \
  "v_cvt_pk_bf16_f32 v198, v212, v213\n\t" \
  "v_mfma_f32_16x16x32_bf16 v[180:183], a[116:119], v[140:143], v[180:183]\n\t" \
  "v_cvt_pk_bf16_f32 v199, v214, v215\n\t" \
  "v_mfma_f32_16x16x32_bf16 v[176:179], a[104:107], v[144:147], v[176:179]\n\t" \
  "buffer_load_dwordx4 %[ov1], %[ro], %[st] offen offset:1024 lds\n\t" \
  "v_or3_b32 v224, v196, v197, v224\n\t" \
  "v_mfma_f32_16x16x32_bf16 v[180:183], a[120:123], v[144:147], v[180:183]\n\t" \
  "v_or3_b32 v224, v198, v199, v224\n\t" \
  "v_mfma_f32_16x16x32_bf16 v[176:179], a[108:111], v[148:151], v[176:179]\n\t" \
  "s_nop 0\n\t" \
  "v_and_b32 v216, 0x40004000, v224\n\t" \
  "v_mfma_f32_16x16x32_bf16 v[180:183], a[124:127], v[148:151], v[180:183]\n\t" \
  "v_cmp_ne_u32 vcc, 0, v216\n\t" \
  "s_cbranch_vccnz .Lrare6_%=\n\t" \
  ".Lback6_%=:\n\t" \
  "v_mfma_f32_16x16x32_bf16 v[64:67], a[0:3], v[192:195], v[64:67]\n\t" \
  "buffer_load_dwordx4 %[ov2], %[ro], %[st] offen offset:2048 lds\n\t" \
  "v_mfma_f32_16x16x32_bf16 v[72:75], a[4:7], v[192:195], v[72:75]\n\t" \
  "v_exp_f32 v208, v176\n\t" \
  "v_mfma_f32_16x16x32_bf16 v[80:83], a[8:11], v[192:195], v[80:83]\n\t" \
  "v_exp_f32 v209, v177\n\t" \
  "v_mfma_f32_16x16x32_bf16 v[88:91], a[12:15], v[192:195], v[88:91]\n\t" \
  "v_exp_f32 v210, v178\n\t" \
  "v_mfma_f32_16x16x32_bf16 v[96:99], a[16:19], v[192:195], v[96:99]\n\t" \
  "buffer_load_dwordx4 %[ov3], %[ro], %[st] offen offset:3072 lds\n\t" \
  "v_exp_f32 v211, v179\n\t" \
  "v_mfma_f32_16x16x32_bf16 v[104:107], a[20:23], v[192:195], v[104:107]\n\t" \
  "v_exp_f32 v212, v180\n\t" \
  "v_mfma_f32_16x16x32_bf16 v[112:115], a[24:27], v[192:195], v[112:115]\n\t" \
  "v_exp_f32 v213, v181\n\t" \
  "v_mfma_f32_16x16x32_bf16 v[120:123], a[28:31], v[192:195], v[120:123]\n\t" \
  "s_add_u32 m0, %[mw], 20480\n\t" \
  "v_exp_f32 v214, v182\n\t" \
  "v_mfma_f32_16x16x32_bf16 v[128:131], a[128:131], v[192:195], v[128:131]\n\t" \
  "s_add_u32 %[st], %[so0], 73728\n\t" \
  "v_exp_f32 v215, v183\n\t" \
  "v_mfma_f32_16x16x32_bf16 v[184:187], a[96:99], v[152:155], v[204:207]\n\t" \
  "buffer_load_dwordx4 %[ov0], %[ro], %[st] offen offset:0 lds\n\t" \
  "v_cvt_pk_bf16_f32 v192, v208, v209\n\t" \
  "v_mfma_f32_16x16x32_bf16 v[188:191], a[112:115], v[152:155], v[204:207]\n\t" \
  "v_cvt_pk_bf16_f32 v193, v210, v211\n\t" \
  "v_mfma_f32_16x16x32_bf16 v[184:187], a[100:103], v[156:159], v[184:187]\n\t" \
  "v_cvt_pk_bf16_f32 v194, v212, v213\n\t" \
  "v_mfma_f32_16x16x32_bf16 v[188:191], a[116:119], v[156:159], v[188:191]\n\t" \
  "v_cvt_pk_bf16_f32 v195, v214, v215\n\t" \
  "v_mfma_f32_16x16x32_bf16 v[184:187], a[104:107], v[160:163], v[184:187]\n\t" \
  "buffer_load_dwordx4 %[ov1], %[ro], %[st] offen offset:1024 lds\n\t" \
  "v_or_b32 v224, v192, v193\n\t" \
  "v_mfma_f32_16x16x32_bf16 v[188:191], a[120:123], v[160:163], v[188:191]\n\t" \
  "v_or3_b32 v224, v194, v195, v224\n\t" \
  "v_mfma_f32_16x16x32_bf16 v[184:187], a[108:111], v[164:167], v[184:187]\n\t" \
  "s_nop 0\n\t" \
  "v_mfma_f32_16x16x32_bf16 v[188:191], a[124:127], v[164:167], v[188:191]\n\t" \
  "s_nop 0\n\t" \
  "s_nop 0\n\t" \
  "v_mfma_f32_16x16x32_bf16 v[68:71], a[0:3], v[196:199], v[68:71]\n\t" \
  "buffer_load_dwordx4 %[ov2], %[ro], %[st] offen offset:2048 lds\n\t" \
  "s_nop 0\n\t" \
  "v_mfma_f32_16x16x32_bf16 v[76:79], a[4:7], v[196:199], v[76:79]\n\t" \
  "v_exp_f32 v208, v184\n\t" \
  "v_mfma_f32_16x16x32_bf16 v[84:87], a[8:11], v[196:199], v[84:87]\n\t" \
  "v_exp_f32 v209, v185\n\t" \
  "v_mfma_f32_16x16x32_bf16 v[92:95], a[12:15], v[196:199], v[92:95]\n\t" \
  "v_exp_f32 v210, v186\n\t" \
  "v_mfma_f32_16x16x32_bf16 v[100:103], a[16:19], v[196:199], v[100:103]\n\t" \
  "buffer_load_dwordx4 %[ov3], %[ro], %[st] offen offset:3072 lds\n\t" \
  "v_exp_f32 v211, v187\n\t" \
  "v_mfma_f32_16x16x32_bf16 v[108:111], a[20:23], v[196:199], v[108:111]\n\t" \
  "v_exp_f32 v212, v188\n\t" \
  "v_mfma_f32_16x16x32_bf16 v[116:119], a[24:27], v[196:199], v[116:119]\n\t" \
  "v_exp_f32 v213, v189\n\t" \
  "v_mfma_f32_16x16x32_bf16 v[124:127], a[28:31], v[196:199], v[124:127]\n\t" \
  "v_exp_f32 v214, v190\n\t" \
  "v_mfma_f32_16x16x32_bf16 v[132:135], a[128:131], v[196:199], v[132:135]\n\t" \
  "v_exp_f32 v215, v191\n\t" \
  "s_nop 0\n\t" \
  "v_cvt_pk_bf16_f32 v196, v208, v209\n\t" \
  "v_cvt_pk_bf16_f32 v197, v210, v211\n\t" \
  "v_cvt_pk_bf16_f32 v198, v212, v213\n\t" \
  "v_cvt_pk_bf16_f32 v199, v214, v215\n\t" \
  "v_or3_b32 v224, v196, v197, v224\n\t" \
  "v_or3_b32 v224, v198, v199, v224\n\t" \
  "v_and_b32 v216, 0x40004000, v224\n\t" \
  "v_cmp_ne_u32 vcc, 0, v216\n\t" \
  "s_cbranch_vccnz .Lrare7_%=\n\t" \
  ".Lback7_%=:\n\t" \
  "v_mfma_f32_16x16x32_bf16 v[128:131], a[128:131], v[192:195], v[128:131]\n\t" \
  "v_mfma_f32_16x16x32_bf16 v[132:135], a[128:131], v[196:199], v[132:135]\n\t" \
  "s_waitcnt vmcnt(16)\n\t" \
  "s_barrier\n\t" \
  "ds_read_b64_tr_b16 a[32:33], %[va0] offset:8192\n\t" \
  "ds_read_b64_tr_b16 a[34:35], %[va0] offset:9216\n\t" \
  "ds_read_b64_tr_b16 a[36:37], %[va1] offset:8192\n\t" \
  "ds_read_b64_tr_b16 a[38:39], %[va1] offset:9216\n\t" \
  "ds_read_b64_tr_b16 a[40:41], %[va2] offset:8192\n\t" \
  "ds_read_b64_tr_b16 a[42:43], %[va2] offset:9216\n\t" \
  "ds_read_b64_tr_b16 a[44:45], %[va3] offset:8192\n\t" \
  "ds_read_b64_tr_b16 a[46:47], %[va3] offset:9216\n\t" \
  "ds_read_b64_tr_b16 a[48:49], %[va4] offset:8192\n\t" \
  "ds_read_b64_tr_b16 a[50:51], %[va4] offset:9216\n\t" \
  "ds_read_b64_tr_b16 a[52:53], %[va5] offset:8192\n\t" \
  "ds_read_b64_tr_b16 a[54:55], %[va5] offset:9216\n\t" \
  "ds_read_b64_tr_b16 a[56:57], %[va6] offset:8192\n\t" \
  "ds_read_b64_tr_b16 a[58:59], %[va6] offset:9216\n\t" \
  "ds_read_b64_tr_b16 a[60:61], %[va7] offset:8192\n\t" \
  "ds_read_b64_tr_b16 a[62:63], %[va7] offset:9216\n\t" \
  "v_add_u32 %[va0], %[vdel], %[va0]\n\t" \
  "v_add_u32 %[va1], %[vdel], %[va1]\n\t" \
  "v_add_u32 %[va2], %[vdel], %[va2]\n\t" \
  "v_add_u32 %[va3], %[vdel], %[va3]\n\t" \
  "v_add_u32 %[va4], %[vdel], %[va4]\n\t" \
  "v_add_u32 %[va5], %[vdel], %[va5]\n\t" \
  "v_add_u32 %[va6], %[vdel], %[va6]\n\t" \
  "v_add_u32 %[va7], %[vdel], %[va7]\n\t" \
  "v_rcp_f32 v216, v128\n\t" \
  "v_rcp_f32 v218, v132\n\t" \
  "s_waitcnt lgkmcnt(0)\n\t" \
  "v_mov_b32 v217, v216\n\t" \
  "v_mov_b32 v219, v218\n\t" \
  "v_mfma_f32_16x16x32_bf16 v[64:67], a[32:35], v[192:195], v[64:67]\n\t" \
  "ds_read_b64_tr_b16 a[0:1], %[va0] offset:0\n\t" \
  "ds_read_b64_tr_b16 a[2:3], %[va0] offset:1024\n\t" \
  "v_mfma_f32_16x16x32_bf16 v[72:75], a[36:39], v[192:195], v[72:75]\n\t" \
  "ds_read_b64_tr_b16 a[4:5], %[va1] offset:0\n\t" \
  "ds_read_b64_tr_b16 a[6:7], %[va1] offset:1024\n\t" \
  "v_mfma_f32_16x16x32_bf16 v[80:83], a[40:43], v[192:195], v[80:83]\n\t" \
  "ds_read_b64_tr_b16 a[8:9], %[va2] offset:0\n\t" \
  "ds_read_b64_tr_b16 a[10:11], %[va2] offset:1024\n\t" \
  "v_mfma_f32_16x16x32_bf16 v[88:91], a[44:47], v[192:195], v[88:91]\n\t" \
  "ds_read_b64_tr_b16 a[12:13], %[va3] offset:0\n\t" \
  "ds_read_b64_tr_b16 a[14:15], %[va3] offset:1024\n\t" \
  "v_mfma_f32_16x16x32_bf16 v[96:99], a[48:51], v[192:195], v[96:99]\n\t" \
  "ds_read_b64_tr_b16 a[16:17], %[va4] offset:0\n\t" \
  "ds_read_b64_tr_b16 a[18:19], %[va4] offset:1024\n\t" \
  "v_mfma_f32_16x16x32_bf16 v[104:107], a[52:55], v[192:195], v[104:107]\n\t" \
  "ds_read_b64_tr_b16 a[20:21], %[va5] offset:0\n\t" \
  "ds_read_b64_tr_b16 a[22:23], %[va5] offset:1024\n\t" \
  "v_mfma_f32_16x16x32_bf16 v[112:115], a[56:59], v[192:195], v[112:115]\n\t" \
  "ds_read_b64_tr_b16 a[24:25], %[va6] offset:0\n\t" \
  "ds_read_b64_tr_b16 a[26:27], %[va6] offset:1024\n\t" \
  "v_mfma_f32_16x16x32_bf16 v[120:123], a[60:63], v[192:195], v[120:123]\n\t" \
  "ds_read_b64_tr_b16 a[28:29], %[va7] offset:0\n\t" \
  "ds_read_b64_tr_b16 a[30:31], %[va7] offset:1024\n\t" \
  "v_mfma_f32_16x16x32_bf16 v[68:71], a[32:35], v[196:199], v[68:71]\n\t" \
  "v_mfma_f32_16x16x32_bf16 v[76:79], a[36:39], v[196:199], v[76:79]\n\t" \
  "v_mfma_f32_16x16x32_bf16 v[84:87], a[40:43], v[196:199], v[84:87]\n\t" \
  "v_mfma_f32_16x16x32_bf16 v[92:95], a[44:47], v[196:199], v[92:95]\n\t" \
  "v_mfma_f32_16x16x32_bf16 v[100:103], a[48:51], v[196:199], v[100:103]\n\t" \
  "v_pk_mul_f32 v[64:65], v[64:65], v[216:217]\n\t" \
  "v_pk_mul_f32 v[66:67], v[66:67], v[216:217]\n\t" \
  "v_mfma_f32_16x16x32_bf16 v[108:111], a[52:55], v[196:199], v[108:111]\n\t" \
  "v_pk_mul_f32 v[72:73], v[72:73], v[216:217]\n\t" \
  "v_pk_mul_f32 v[74:75], v[74:75], v[216:217]\n\t" \
  "v_cvt_pk_bf16_f32 v136, v64, v65\n\t" \
  "v_mfma_f32_16x16x32_bf16 v[116:119], a[56:59], v[196:199], v[116:119]\n\t" \
  "v_cvt_pk_bf16_f32 v137, v66, v67\n\t" \
  "v_cvt_pk_bf16_f32 v138, v72, v73\n\t" \
  "v_mfma_f32_16x16x32_bf16 v[124:127], a[60:63], v[196:199], v[124:127]\n\t" \
  "v_cvt_pk_bf16_f32 v139, v74, v75\n\t" \
  "ds_write_b128 %[oa], v[136:139] offset:0\n\t" \
  "v_pk_mul_f32 v[80:81], v[80:81], v[216:217]\n\t" \
  "v_pk_mul_f32 v[82:83], v[82:83], v[216:217]\n\t" \
  "v_pk_mul_f32 v[88:89], v[88:89], v[216:217]\n\t" \
  "v_pk_mul_f32 v[90:91], v[90:91], v[216:217]\n\t" \
  "v_cvt_pk_bf16_f32 v140, v80, v81\n\t" \
  "v_cvt_pk_bf16_f32 v141, v82, v83\n\t" \
  "v_cvt_pk_bf16_f32 v142, v88, v89\n\t" \
  "v_cvt_pk_bf16_f32 v143, v90, v91\n\t" \
  "ds_write_b128 %[oa], v[140:143] offset:1024\n\t" \
  "v_pk_mul_f32 v[96:97], v[96:97], v[216:217]\n\t" \
  "v_pk_mul_f32 v[98:99], v[98:99], v[216:217]\n\t" \
  "v_pk_mul_f32 v[104:105], v[104:105], v[216:217]\n\t" \
  "v_pk_mul_f32 v[106:107], v[106:107], v[216:217]\n\t" \
  "v_cvt_pk_bf16_f32 v144, v96, v97\n\t" \
  "v_cvt_pk_bf16_f32 v145, v98, v99\n\t" \
  "v_cvt_pk_bf16_f32 v146, v104, v105\n\t" \
  "v_cvt_pk_bf16_f32 v147, v106, v107\n\t" \
  "ds_write_b128 %[oa], v[144:147] offset:2048\n\t" \
  "v_pk_mul_f32 v[112:113], v[112:113], v[216:217]\n\t" \
  "v_pk_mul_f32 v[114:115], v[114:115], v[216:217]\n\t" \
  "v_pk_mul_f32 v[120:121], v[120:121], v[216:217]\n\t" \
  "v_pk_mul_f32 v[122:123], v[122:123], v[216:217]\n\t" \
  "v_cvt_pk_bf16_f32 v148, v112, v113\n\t" \
  "v_cvt_pk_bf16_f32 v149, v114, v115\n\t" \
  "v_cvt_pk_bf16_f32 v150, v120, v121\n\t" \
  "v_cvt_pk_bf16_f32 v151, v122, v123\n\t" \
  "ds_write_b128 %[oa], v[148:151] offset:3072\n\t" \
  "v_pk_mul_f32 v[68:69], v[68:69], v[218:219]\n\t" \
  "v_pk_mul_f32 v[70:71], v[70:71], v[218:219]\n\t" \
  "v_pk_mul_f32 v[76:77], v[76:77], v[218:219]\n\t" \
  "v_pk_mul_f32 v[78:79], v[78:79], v[218:219]\n\t" \
  "v_cvt_pk_bf16_f32 v152, v68, v69\n\t" \
  "v_cvt_pk_bf16_f32 v153, v70, v71\n\t" \
  "v_cvt_pk_bf16_f32 v154, v76, v77\n\t" \
  "v_cvt_pk_bf16_f32 v155, v78, v79\n\t" \
  "ds_write_b128 %[oa], v[152:155] offset:4096\n\t" \
  "v_pk_mul_f32 v[84:85], v[84:85], v[218:219]\n\t" \
  "v_pk_mul_f32 v[86:87], v[86:87], v[218:219]\n\t" \
  "v_pk_mul_f32 v[92:93], v[92:93], v[218:219]\n\t" \
  "v_pk_mul_f32 v[94:95], v[94:95], v[218:219]\n\t" \
  "v_cvt_pk_bf16_f32 v156, v84, v85\n\t" \
  "v_cvt_pk_bf16_f32 v157, v86, v87\n\t" \
  "v_cvt_pk_bf16_f32 v158, v92, v93\n\t" \
  "v_cvt_pk_bf16_f32 v159, v94, v95\n\t" \
  "ds_write_b128 %[oa], v[156:159] offset:5120\n\t" \
  "v_pk_mul_f32 v[100:101], v[100:101], v[218:219]\n\t" \
  "v_pk_mul_f32 v[102:103], v[102:103], v[218:219]\n\t" \
  "v_pk_mul_f32 v[108:109], v[108:109], v[218:219]\n\t" \
  "v_pk_mul_f32 v[110:111], v[110:111], v[218:219]\n\t" \
  "v_cvt_pk_bf16_f32 v160, v100, v101\n\t" \
  "v_cvt_pk_bf16_f32 v161, v102, v103\n\t" \
  "v_cvt_pk_bf16_f32 v162, v108, v109\n\t" \
  "v_cvt_pk_bf16_f32 v163, v110, v111\n\t" \
  "ds_write_b128 %[oa], v[160:163] offset:6144\n\t" \
  "v_pk_mul_f32 v[116:117], v[116:117], v[218:219]\n\t" \
  "v_pk_mul_f32 v[118:119], v[118:119], v[218:219]\n\t" \
  "v_pk_mul_f32 v[124:125], v[124:125], v[218:219]\n\t" \
  "v_pk_mul_f32 v[126:127], v[126:127], v[218:219]\n\t" \
  "v_cvt_pk_bf16_f32 v164, v116, v117\n\t" \
  "v_cvt_pk_bf16_f32 v165, v118, v119\n\t" \
  "v_cvt_pk_bf16_f32 v166, v124, v125\n\t" \
  "v_cvt_pk_bf16_f32 v167, v126, v127\n\t" \
  "ds_write_b128 %[oa], v[164:167] offset:7168\n\t" \
  "s_waitcnt lgkmcnt(0)\n\t" \
  "s_barrier\n\t" \
  "ds_read_b128 a[128:131], %[ob] offset:0\n\t" \
  "ds_read_b128 a[132:135], %[ob] offset:4096\n\t" \
  "ds_read_b128 a[136:139], %[ob] offset:1024\n\t" \
  "ds_read_b128 a[140:143], %[ob] offset:5120\n\t" \
  "ds_read_b128 a[144:147], %[ob] offset:2048\n\t" \
  "ds_read_b128 a[148:151], %[ob] offset:6144\n\t" \
  "ds_read_b128 a[152:155], %[ob] offset:3072\n\t" \
  "ds_read_b128 a[156:159], %[ob] offset:7168\n\t" \
  "ds_read_b128 a[160:163], %[ob] offset:8192\n\t" \
  "ds_read_b128 a[164:167], %[ob] offset:12288\n\t" \
  "ds_read_b128 a[168:171], %[ob] offset:9216\n\t" \
  "ds_read_b128 a[172:175], %[ob] offset:13312\n\t" \
  "ds_read_b128 a[176:179], %[ob] offset:10240\n\t" \
  "ds_read_b128 a[180:183], %[ob] offset:14336\n\t" \
  "ds_read_b128 a[184:187], %[ob] offset:11264\n\t" \
  "ds_read_b128 a[188:191], %[ob] offset:15360\n\t" \
  "ds_read_b128 a[192:195], %[ob] offset:16384\n\t" \
  "ds_read_b128 a[196:199], %[ob] offset:20480\n\t" \
  "ds_read_b128 a[200:203], %[ob] offset:17408\n\t" \
  "ds_read_b128 a[204:207], %[ob] offset:21504\n\t" \
  "ds_read_b128 a[208:211], %[ob] offset:18432\n\t" \
  "ds_read_b128 a[212:215], %[ob] offset:22528\n\t" \
  "ds_read_b128 a[216:219], %[ob] offset:19456\n\t" \
  "ds_read_b128 a[220:223], %[ob] offset:23552\n\t" \
  "ds_read_b128 a[224:227], %[ob] offset:24576\n\t" \
  "ds_read_b128 a[228:231], %[ob] offset:28672\n\t" \
  "ds_read_b128 a[232:235], %[ob] offset:25600\n\t" \
  "ds_read_b128 a[236:239], %[ob] offset:29696\n\t" \
  "ds_read_b128 a[240:243], %[ob] offset:26624\n\t" \
  "ds_read_b128 a[244:247], %[ob] offset:30720\n\t" \
  "ds_read_b128 a[248:251], %[ob] offset:27648\n\t" \
  "ds_read_b128 a[252:255], %[ob] offset:31744\n\t" \
  "s_add_u32 m0, %[mw], 0\n\t" \
  "s_add_u32 %[st], %[so0], 98304\n\t" \
  "buffer_load_dwordx4 %[ov0], %[ro], %[st] offen offset:0 lds\n\t" \
  "buffer_load_dwordx4 %[ov1], %[ro], %[st] offen offset:1024 lds\n\t" \
  "buffer_load_dwordx4 %[ov2], %[ro], %[st] offen offset:2048 lds\n\t" \
  "buffer_load_dwordx4 %[ov3], %[ro], %[st] offen offset:3072 lds\n\t" \
  "s_add_u32 m0, %[mw], 4096\n\t" \
  "s_add_u32 %[st], %[so0], 106496\n\t" \
  "buffer_load_dwordx4 %[ov0], %[ro], %[st] offen offset:0 lds\n\t" \
  "buffer_load_dwordx4 %[ov1], %[ro], %[st] offen offset:1024 lds\n\t" \
  "buffer_load_dwordx4 %[ov2], %[ro], %[st] offen offset:2048 lds\n\t" \
  "buffer_load_dwordx4 %[ov3], %[ro], %[st] offen offset:3072 lds\n\t" \
  "s_waitcnt lgkmcnt(0)\n\t" \
  "s_waitcnt vmcnt(16)\n\t" \
  "v_mfma_f32_16x16x32_bf16 v[64:67], a[0:3], a[128:131], 0\n\t" \
  "ds_read_b64_tr_b16 a[32:33], %[va0] offset:8192\n\t" \
  "ds_read_b64_tr_b16 a[34:35], %[va0] offset:9216\n\t" \
  "v_mfma_f32_16x16x32_bf16 v[68:71], a[0:3], a[132:135], 0\n\t" \
  "ds_read_b64_tr_b16 a[36:37], %[va1] offset:8192\n\t" \
  "v_mfma_f32_16x16x32_bf16 v[72:75], a[4:7], a[128:131], 0\n\t" \
  "ds_read_b64_tr_b16 a[38:39], %[va1] offset:9216\n\t" \
  "ds_read_b64_tr_b16 a[40:41], %[va2] offset:8192\n\t" \
  "v_mfma_f32_16x16x32_bf16 v[76:79], a[4:7], a[132:135], 0\n\t" \
  "ds_read_b64_tr_b16 a[42:43], %[va2] offset:9216\n\t" \
  "v_mfma_f32_16x16x32_bf16 v[80:83], a[8:11], a[128:131], 0\n\t" \
  "ds_read_b64_tr_b16 a[44:45], %[va3] offset:8192\n\t" \
  "ds_read_b64_tr_b16 a[46:47], %[va3] offset:9216\n\t" \
  "v_mfma_f32_16x16x32_bf16 v[84:87], a[8:11], a[132:135], 0\n\t" \
  "ds_read_b64_tr_b16 a[48:49], %[va4] offset:8192\n\t" \
  "v_mfma_f32_16x16x32_bf16 v[88:91], a[12:15], a[128:131], 0\n\t" \
  "ds_read_b64_tr_b16 a[50:51], %[va4] offset:9216\n\t" \
  "ds_read_b64_tr_b16 a[52:53], %[va5] offset:8192\n\t" \
  "v_mfma_f32_16x16x32_bf16 v[92:95], a[12:15], a[132:135], 0\n\t" \
  "ds_read_b64_tr_b16 a[54:55], %[va5] offset:9216\n\t" \
  "v_mfma_f32_16x16x32_bf16 v[96:99], a[16:19], a[128:131], 0\n\t" \
  "ds_read_b64_tr_b16 a[56:57], %[va6] offset:8192\n\t" \
  "ds_read_b64_tr_b16 a[58:59], %[va6] offset:9216\n\t" \
  "v_mfma_f32_16x16x32_bf16 v[100:103], a[16:19], a[132:135], 0\n\t" \
  "ds_read_b64_tr_b16 a[60:61], %[va7] offset:8192\n\t" \
  "ds_read_b64_tr_b16 a[62:63], %[va7] offset:9216\n\t" \
  "v_mfma_f32_16x16x32_bf16 v[104:107], a[20:23], a[128:131], 0\n\t" \
  "v_mfma_f32_16x16x32_bf16 v[108:111], a[20:23], a[132:135], 0\n\t" \
  "v_mfma_f32_16x16x32_bf16 v[112:115], a[24:27], a[128:131], 0\n\t" \
  "s_waitcnt lgkmcnt(0)\n\t" \
  "s_add_u32 m0, %[mw], 8192\n\t" \
  "s_add_u32 %[st], %[so0], 131072\n\t" \
  "buffer_load_dwordx4 %[ov0], %[ro], %[st] offen offset:0 lds\n\t" \
  "buffer_load_dwordx4 %[ov1], %[ro], %[st] offen offset:1024 lds\n\t" \
  "v_mfma_f32_16x16x32_bf16 v[116:119], a[24:27], a[132:135], 0\n\t" \
  "buffer_load_dwordx4 %[ov2], %[ro], %[st] offen offset:2048 lds\n\t" \
  "buffer_load_dwordx4 %[ov3], %[ro], %[st] offen offset:3072 lds\n\t" \
  "s_add_u32 m0, %[mw], 12288\n\t" \
  "s_add_u32 %[st], %[so0], 139264\n\t" \
  "v_mfma_f32_16x16x32_bf16 v[120:123], a[28:31], a[128:131], 0\n\t" \
  "buffer_load_dwordx4 %[ov0], %[ro], %[st] offen offset:0 lds\n\t" \
  "buffer_load_dwordx4 %[ov1], %[ro], %[st] offen offset:1024 lds\n\t" \
  "buffer_load_dwordx4 %[ov2], %[ro], %[st] offen offset:2048 lds\n\t" \
  "buffer_load_dwordx4 %[ov3], %[ro], %[st] offen offset:3072 lds\n\t" \
  "v_mfma_f32_16x16x32_bf16 v[124:127], a[28:31], a[132:135], 0\n\t" \
  "s_waitcnt vmcnt(16)\n\t" \
  "v_mfma_f32_16x16x32_bf16 v[64:67], a[32:35], a[136:139], v[64:67]\n\t" \
  "ds_read_b64_tr_b16 a[0:1], %[va0] offset:16384\n\t" \
  "ds_read_b64_tr_b16 a[2:3], %[va0] offset:17408\n\t" \
  "v_mfma_f32_16x16x32_bf16 v[68:71], a[32:35], a[140:143], v[68:71]\n\t" \
  "ds_read_b64_tr_b16 a[4:5], %[va1] offset:16384\n\t" \
  "v_mfma_f32_16x16x32_bf16 v[72:75], a[36:39], a[136:139], v[72:75]\n\t" \
  "ds_read_b64_tr_b16 a[6:7], %[va1] offset:17408\n\t" \
  "ds_read_b64_tr_b16 a[8:9], %[va2] offset:16384\n\t" \
  "v_mfma_f32_16x16x32_bf16 v[76:79], a[36:39], a[140:143], v[76:79]\n\t" \
  "ds_read_b64_tr_b16 a[10:11], %[va2] offset:17408\n\t" \
  "v_mfma_f32_16x16x32_bf16 v[80:83], a[40:43], a[136:139], v[80:83]\n\t" \
  "ds_read_b64_tr_b16 a[12:13], %[va3] offset:16384\n\t" \
  "ds_read_b64_tr_b16 a[14:15], %[va3] offset:17408\n\t" \
  "v_mfma_f32_16x16x32_bf16 v[84:87], a[40:43], a[140:143], v[84:87]\n\t" \
  "ds_read_b64_tr_b16 a[16:17], %[va4] offset:16384\n\t" \
  "v_mfma_f32_16x16x32_bf16 v[88:91], a[44:47], a[136:139], v[88:91]\n\t" \
  "ds_read_b64_tr_b16 a[18:19], %[va4] offset:17408\n\t" \
  "ds_read_b64_tr_b16 a[20:21], %[va5] offset:16384\n\t" \
  "v_mfma_f32_16x16x32_bf16 v[92:95], a[44:47], a[140:143], v[92:95]\n\t" \
  "ds_read_b64_tr_b16 a[22:23], %[va5] offset:17408\n\t" \
  "v_mfma_f32_16x16x32_bf16 v[96:99], a[48:51], a[136:139], v[96:99]\n\t" \
  "ds_read_b64_tr_b16 a[24:25], %[va6] offset:16384\n\t" \
  "ds_read_b64_tr_b16 a[26:27], %[va6] offset:17408\n\t" \
  "v_mfma_f32_16x16x32_bf16 v[100:103], a[48:51], a[140:143], v[100:103]\n\t" \
  "ds_read_b64_tr_b16 a[28:29], %[va7] offset:16384\n\t" \
  "ds_read_b64_tr_b16 a[30:31], %[va7] offset:17408\n\t" \
  "v_mfma_f32_16x16x32_bf16 v[104:107], a[52:55], a[136:139], v[104:107]\n\t" \
  "v_mfma_f32_16x16x32_bf16 v[108:111], a[52:55], a[140:143], v[108:111]\n\t" \
  "v_mfma_f32_16x16x32_bf16 v[112:115], a[56:59], a[136:139], v[112:115]\n\t" \
  "s_waitcnt lgkmcnt(0)\n\t" \
  "s_add_u32 m0, %[mw], 16384\n\t" \
  "s_add_u32 %[st], %[so0], 163840\n\t" \
  "buffer_load_dwordx4 %[ov0], %[ro], %[st] offen offset:0 lds\n\t" \
  "buffer_load_dwordx4 %[ov1], %[ro], %[st] offen offset:1024 lds\n\t" \
  "v_mfma_f32_16x16x32_bf16 v[116:119], a[56:59], a[140:143], v[116:119]\n\t" \
  "buffer_load_dwordx4 %[ov2], %[ro], %[st] offen offset:2048 lds\n\t" \
  "buffer_load_dwordx4 %[ov3], %[ro], %[st] offen offset:3072 lds\n\t" \
  "s_add_u32 m0, %[mw], 20480\n\t" \
  "s_add_u32 %[st], %[so0], 172032\n\t" \
  "v_mfma_f32_16x16x32_bf16 v[120:123], a[60:63], a[136:139], v[120:123]\n\t" \
  "buffer_load_dwordx4 %[ov0], %[ro], %[st] offen offset:0 lds\n\t" \
  "buffer_load_dwordx4 %[ov1], %[ro], %[st] offen offset:1024 lds\n\t" \
  "buffer_load_dwordx4 %[ov2], %[ro], %[st] offen offset:2048 lds\n\t" \
  "buffer_load_dwordx4 %[ov3], %[ro], %[st] offen offset:3072 lds\n\t" \
  "v_mfma_f32_16x16x32_bf16 v[124:127], a[60:63], a[140:143], v[124:127]\n\t" \
  "s_waitcnt vmcnt(16)\n\t" \
  "v_mfma_f32_16x16x32_bf16 v[64:67], a[0:3], a[144:147], v[64:67]\n\t" \
  "ds_read_b64_tr_b16 a[32:33], %[va0] offset:0\n\t" \
  "ds_read_b64_tr_b16 a[34:35], %[va0] offset:1024\n\t" \
  "v_mfma_f32_16x16x32_bf16 v[68:71], a[0:3], a[148:151], v[68:71]\n\t" \
  "ds_read_b64_tr_b16 a[36:37], %[va1] offset:0\n\t" \
  "v_mfma_f32_16x16x32_bf16 v[72:75], a[4:7], a[144:147], v[72:75]\n\t" \
  "ds_read_b64_tr_b16 a[38:39], %[va1] offset:1024\n\t" \
  "ds_read_b64_tr_b16 a[40:41], %[va2] offset:0\n\t" \
  "v_mfma_f32_16x16x32_bf16 v[76:79], a[4:7], a[148:151], v[76:79]\n\t" \
  "ds_read_b64_tr_b16 a[42:43], %[va2] offset:1024\n\t" \
  "v_mfma_f32_16x16x32_bf16 v[80:83], a[8:11], a[144:147], v[80:83]\n\t" \
  "ds_read_b64_tr_b16 a[44:45], %[va3] offset:0\n\t" \
  "ds_read_b64_tr_b16 a[46:47], %[va3] offset:1024\n\t" \
  "v_mfma_f32_16x16x32_bf16 v[84:87], a[8:11], a[148:151], v[84:87]\n\t" \
  "ds_read_b64_tr_b16 a[48:49], %[va4] offset:0\n\t" \
  "v_mfma_f32_16x16x32_bf16 v[88:91], a[12:15], a[144:147], v[88:91]\n\t" \
  "ds_read_b64_tr_b16 a[50:51], %[va4] offset:1024\n\t" \
  "ds_read_b64_tr_b16 a[52:53], %[va5] offset:0\n\t" \
  "v_mfma_f32_16x16x32_bf16 v[92:95], a[12:15], a[148:151], v[92:95]\n\t" \
  "ds_read_b64_tr_b16 a[54:55], %[va5] offset:1024\n\t" \
  "v_mfma_f32_16x16x32_bf16 v[96:99], a[16:19], a[144:147], v[96:99]\n\t" \
  "ds_read_b64_tr_b16 a[56:57], %[va6] offset:0\n\t" \
  "ds_read_b64_tr_b16 a[58:59], %[va6] offset:1024\n\t" \
  "v_mfma_f32_16x16x32_bf16 v[100:103], a[16:19], a[148:151], v[100:103]\n\t" \
  "ds_read_b64_tr_b16 a[60:61], %[va7] offset:0\n\t" \
  "ds_read_b64_tr_b16 a[62:63], %[va7] offset:1024\n\t" \
  "v_mfma_f32_16x16x32_bf16 v[104:107], a[20:23], a[144:147], v[104:107]\n\t" \
  "v_mfma_f32_16x16x32_bf16 v[108:111], a[20:23], a[148:151], v[108:111]\n\t" \
  "v_mfma_f32_16x16x32_bf16 v[112:115], a[24:27], a[144:147], v[112:115]\n\t" \
  "s_waitcnt lgkmcnt(0)\n\t" \
  "s_add_u32 m0, %[mw], 0\n\t" \
  "s_add_u32 %[st], %[so0], 196608\n\t" \
  "buffer_load_dwordx4 %[ov0], %[ro], %[st] offen offset:0 lds\n\t" \
  "buffer_load_dwordx4 %[ov1], %[ro], %[st] offen offset:1024 lds\n\t" \
  "v_mfma_f32_16x16x32_bf16 v[116:119], a[24:27], a[148:151], v[116:119]\n\t" \
  "buffer_load_dwordx4 %[ov2], %[ro], %[st] offen offset:2048 lds\n\t" \
  "buffer_load_dwordx4 %[ov3], %[ro], %[st] offen offset:3072 lds\n\t" \
  "s_add_u32 m0, %[mw], 4096\n\t" \
  "s_add_u32 %[st], %[so0], 204800\n\t" \
  "v_mfma_f32_16x16x32_bf16 v[120:123], a[28:31], a[144:147], v[120:123]\n\t" \
  "buffer_load_dwordx4 %[ov0], %[ro], %[st] offen offset:0 lds\n\t" \
  "buffer_load_dwordx4 %[ov1], %[ro], %[st] offen offset:1024 lds\n\t" \
  "buffer_load_dwordx4 %[ov2], %[ro], %[st] offen offset:2048 lds\n\t" \
  "buffer_load_dwordx4 %[ov3], %[ro], %[st] offen offset:3072 lds\n\t" \
  "v_mfma_f32_16x16x32_bf16 v[124:127], a[28:31], a[148:151], v[124:127]\n\t" \
  "s_waitcnt vmcnt(16)\n\t" \
  "v_mfma_f32_16x16x32_bf16 v[64:67], a[32:35], a[152:155], v[64:67]\n\t" \
  "ds_read_b64_tr_b16 a[0:1], %[va0] offset:8192\n\t" \
  "ds_read_b64_tr_b16 a[2:3], %[va0] offset:9216\n\t" \
  "v_mfma_f32_16x16x32_bf16 v[68:71], a[32:35], a[156:159], v[68:71]\n\t" \
  "ds_read_b64_tr_b16 a[4:5], %[va1] offset:8192\n\t" \
  "v_mfma_f32_16x16x32_bf16 v[72:75], a[36:39], a[152:155], v[72:75]\n\t" \
  "ds_read_b64_tr_b16 a[6:7], %[va1] offset:9216\n\t" \
  "ds_read_b64_tr_b16 a[8:9], %[va2] offset:8192\n\t" \
  "v_mfma_f32_16x16x32_bf16 v[76:79], a[36:39], a[156:159], v[76:79]\n\t" \
  "ds_read_b64_tr_b16 a[10:11], %[va2] offset:9216\n\t" \
  "v_mfma_f32_16x16x32_bf16 v[80:83], a[40:43], a[152:155], v[80:83]\n\t" \
  "ds_read_b64_tr_b16 a[12:13], %[va3] offset:8192\n\t" \
  "ds_read_b64_tr_b16 a[14:15], %[va3] offset:9216\n\t" \
  "v_mfma_f32_16x16x32_bf16 v[84:87], a[40:43], a[156:159], v[84:87]\n\t" \
  "ds_read_b64_tr_b16 a[16:17], %[va4] offset:8192\n\t" \
  "v_mfma_f32_16x16x32_bf16 v[88:91], a[44:47], a[152:155], v[88:91]\n\t" \
  "ds_read_b64_tr_b16 a[18:19], %[va4] offset:9216\n\t" \
  "ds_read_b64_tr_b16 a[20:21], %[va5] offset:8192\n\t" \
  "v_mfma_f32_16x16x32_bf16 v[92:95], a[44:47], a[156:159], v[92:95]\n\t" \
  "ds_read_b64_tr_b16 a[22:23], %[va5] offset:9216\n\t" \
  "v_mfma_f32_16x16x32_bf16 v[96:99], a[48:51], a[152:155], v[96:99]\n\t" \
  "ds_read_b64_tr_b16 a[24:25], %[va6] offset:8192\n\t" \
  "ds_read_b64_tr_b16 a[26:27], %[va6] offset:9216\n\t" \
  "v_mfma_f32_16x16x32_bf16 v[100:103], a[48:51], a[156:159], v[100:103]\n\t" \
  "ds_read_b64_tr_b16 a[28:29], %[va7] offset:8192\n\t" \
  "ds_read_b64_tr_b16 a[30:31], %[va7] offset:9216\n\t" \
  "v_mfma_f32_16x16x32_bf16 v[104:107], a[52:55], a[152:155], v[104:107]\n\t" \
  "v_mfma_f32_16x16x32_bf16 v[108:111], a[52:55], a[156:159], v[108:111]\n\t" \
  "v_mfma_f32_16x16x32_bf16 v[112:115], a[56:59], a[152:155], v[112:115]\n\t" \
  "s_waitcnt lgkmcnt(0)\n\t" \
  "s_add_u32 m0, %[mw], 8192\n\t" \
  "s_add_u32 %[st], %[so0], 229376\n\t" \
  "buffer_load_dwordx4 %[ov0], %[ro], %[st] offen offset:0 lds\n\t" \
  "buffer_load_dwordx4 %[ov1], %[ro], %[st] offen offset:1024 lds\n\t" \
  "v_mfma_f32_16x16x32_bf16 v[116:119], a[56:59], a[156:159], v[116:119]\n\t" \
  "buffer_load_dwordx4 %[ov2], %[ro], %[st] offen offset:2048 lds\n\t" \
  "buffer_load_dwordx4 %[ov3], %[ro], %[st] offen offset:3072 lds\n\t" \
  "s_add_u32 m0, %[mw], 12288\n\t" \
  "s_add_u32 %[st], %[so0], 237568\n\t" \
  "v_mfma_f32_16x16x32_bf16 v[120:123], a[60:63], a[152:155], v[120:123]\n\t" \
  "buffer_load_dwordx4 %[ov0], %[ro], %[st] offen offset:0 lds\n\t" \
  "buffer_load_dwordx4 %[ov1], %[ro], %[st] offen offset:1024 lds\n\t" \
  "buffer_load_dwordx4 %[ov2], %[ro], %[st] offen offset:2048 lds\n\t" \
  "buffer_load_dwordx4 %[ov3], %[ro], %[st] offen offset:3072 lds\n\t" \
  "v_mfma_f32_16x16x32_bf16 v[124:127], a[60:63], a[156:159], v[124:127]\n\t" \
  "s_waitcnt vmcnt(16)\n\t" \
  "v_mfma_f32_16x16x32_bf16 v[64:67], a[0:3], a[160:163], v[64:67]\n\t" \
  "ds_read_b64_tr_b16 a[32:33], %[va0] offset:16384\n\t" \
  "ds_read_b64_tr_b16 a[34:35], %[va0] offset:17408\n\t" \
  "v_mfma_f32_16x16x32_bf16 v[68:71], a[0:3], a[164:167], v[68:71]\n\t" \
  "ds_read_b64_tr_b16 a[36:37], %[va1] offset:16384\n\t" \
  "v_mfma_f32_16x16x32_bf16 v[72:75], a[4:7], a[160:163], v[72:75]\n\t" \
  "ds_read_b64_tr_b16 a[38:39], %[va1] offset:17408\n\t" \
  "ds_read_b64_tr_b16 a[40:41], %[va2] offset:16384\n\t" \
  "v_mfma_f32_16x16x32_bf16 v[76:79], a[4:7], a[164:167], v[76:79]\n\t" \
  "ds_read_b64_tr_b16 a[42:43], %[va2] offset:17408\n\t" \
  "v_mfma_f32_16x16x32_bf16 v[80:83], a[8:11], a[160:163], v[80:83]\n\t" \
  "ds_read_b64_tr_b16 a[44:45], %[va3] offset:16384\n\t" \
  "ds_read_b64_tr_b16 a[46:47], %[va3] offset:17408\n\t" \
  "v_mfma_f32_16x16x32_bf16 v[84:87], a[8:11], a[164:167], v[84:87]\n\t" \
  "ds_read_b64_tr_b16 a[48:49], %[va4] offset:16384\n\t" \
  "v_mfma_f32_16x16x32_bf16 v[88:91], a[12:15], a[160:163], v[88:91]\n\t" \
  "ds_read_b64_tr_b16 a[50:51], %[va4] offset:17408\n\t" \
  "ds_read_b64_tr_b16 a[52:53], %[va5] offset:16384\n\t" \
  "v_mfma_f32_16x16x32_bf16 v[92:95], a[12:15], a[164:167], v[92:95]\n\t" \
  "ds_read_b64_tr_b16 a[54:55], %[va5] offset:17408\n\t" \
  "v_mfma_f32_16x16x32_bf16 v[96:99], a[16:19], a[160:163], v[96:99]\n\t" \
  "ds_read_b64_tr_b16 a[56:57], %[va6] offset:16384\n\t" \
  "ds_read_b64_tr_b16 a[58:59], %[va6] offset:17408\n\t" \
  "v_mfma_f32_16x16x32_bf16 v[100:103], a[16:19], a[164:167], v[100:103]\n\t" \
  "ds_read_b64_tr_b16 a[60:61], %[va7] offset:16384\n\t" \
  "ds_read_b64_tr_b16 a[62:63], %[va7] offset:17408\n\t" \
  "v_mfma_f32_16x16x32_bf16 v[104:107], a[20:23], a[160:163], v[104:107]\n\t" \
  "v_mfma_f32_16x16x32_bf16 v[108:111], a[20:23], a[164:167], v[108:111]\n\t" \
  "v_mfma_f32_16x16x32_bf16 v[112:115], a[24:27], a[160:163], v[112:115]\n\t" \
  "s_waitcnt lgkmcnt(0)\n\t" \
  "s_add_u32 m0, %[mw], 16384\n\t" \
  "s_add_u32 %[st], %[so0], 262144\n\t" \
  "buffer_load_dwordx4 %[ov0], %[ro], %[st] offen offset:0 lds\n\t" \
  "buffer_load_dwordx4 %[ov1], %[ro], %[st] offen offset:1024 lds\n\t" \
  "v_mfma_f32_16x16x32_bf16 v[116:119], a[24:27], a[164:167], v[116:119]\n\t" \
  "buffer_load_dwordx4 %[ov2], %[ro], %[st] offen offset:2048 lds\n\t" \
  "buffer_load_dwordx4 %[ov3], %[ro], %[st] offen offset:3072 lds\n\t" \
  "s_add_u32 m0, %[mw], 20480\n\t" \
  "s_add_u32 %[st], %[so0], 270336\n\t" \
  "v_mfma_f32_16x16x32_bf16 v[120:123], a[28:31], a[160:163], v[120:123]\n\t" \
  "buffer_load_dwordx4 %[ov0], %[ro], %[st] offen offset:0 lds\n\t" \
  "buffer_load_dwordx4 %[ov1], %[ro], %[st] offen offset:1024 lds\n\t" \
  "buffer_load_dwordx4 %[ov2], %[ro], %[st] offen offset:2048 lds\n\t" \
  "buffer_load_dwordx4 %[ov3], %[ro], %[st] offen offset:3072 lds\n\t" \
  "v_mfma_f32_16x16x32_bf16 v[124:127], a[28:31], a[164:167], v[124:127]\n\t" \
  "s_waitcnt vmcnt(16)\n\t" \
  "v_mfma_f32_16x16x32_bf16 v[64:67], a[32:35], a[168:171], v[64:67]\n\t" \
  "ds_read_b64_tr_b16 a[0:1], %[va0] offset:0\n\t" \
  "ds_read_b64_tr_b16 a[2:3], %[va0] offset:1024\n\t" \
  "v_mfma_f32_16x16x32_bf16 v[68:71], a[32:35], a[172:175], v[68:71]\n\t" \
  "ds_read_b64_tr_b16 a[4:5], %[va1] offset:0\n\t" \
  "v_mfma_f32_16x16x32_bf16 v[72:75], a[36:39], a[168:171], v[72:75]\n\t" \
  "ds_read_b64_tr_b16 a[6:7], %[va1] offset:1024\n\t" \
  "ds_read_b64_tr_b16 a[8:9], %[va2] offset:0\n\t" \
  "v_mfma_f32_16x16x32_bf16 v[76:79], a[36:39], a[172:175], v[76:79]\n\t" \
  "ds_read_b64_tr_b16 a[10:11], %[va2] offset:1024\n\t" \
  "v_mfma_f32_16x16x32_bf16 v[80:83], a[40:43], a[168:171], v[80:83]\n\t" \
  "ds_read_b64_tr_b16 a[12:13], %[va3] offset:0\n\t" \
  "ds_read_b64_tr_b16 a[14:15], %[va3] offset:1024\n\t" \
  "v_mfma_f32_16x16x32_bf16 v[84:87], a[40:43], a[172:175], v[84:87]\n\t" \
  "ds_read_b64_tr_b16 a[16:17], %[va4] offset:0\n\t" \
  "v_mfma_f32_16x16x32_bf16 v[88:91], a[44:47], a[168:171], v[88:91]\n\t" \
  "ds_read_b64_tr_b16 a[18:19], %[va4] offset:1024\n\t" \
  "ds_read_b64_tr_b16 a[20:21], %[va5] offset:0\n\t" \
  "v_mfma_f32_16x16x32_bf16 v[92:95], a[44:47], a[172:175], v[92:95]\n\t" \
  "ds_read_b64_tr_b16 a[22:23], %[va5] offset:1024\n\t" \
  "v_mfma_f32_16x16x32_bf16 v[96:99], a[48:51], a[168:171], v[96:99]\n\t" \
  "ds_read_b64_tr_b16 a[24:25], %[va6] offset:0\n\t" \
  "ds_read_b64_tr_b16 a[26:27], %[va6] offset:1024\n\t" \
  "v_mfma_f32_16x16x32_bf16 v[100:103], a[48:51], a[172:175], v[100:103]\n\t" \
  "ds_read_b64_tr_b16 a[28:29], %[va7] offset:0\n\t" \
  "ds_read_b64_tr_b16 a[30:31], %[va7] offset:1024\n\t" \
  "v_mfma_f32_16x16x32_bf16 v[104:107], a[52:55], a[168:171], v[104:107]\n\t" \
  "v_mfma_f32_16x16x32_bf16 v[108:111], a[52:55], a[172:175], v[108:111]\n\t" \
  "v_mfma_f32_16x16x32_bf16 v[112:115], a[56:59], a[168:171], v[112:115]\n\t" \
  "s_waitcnt lgkmcnt(0)\n\t" \
  "s_add_u32 m0, %[mw], 0\n\t" \
  "s_add_u32 %[st], %[so0], 294912\n\t" \
  "buffer_load_dwordx4 %[ov0], %[ro], %[st] offen offset:0 lds\n\t" \
  "buffer_load_dwordx4 %[ov1], %[ro], %[st] offen offset:1024 lds\n\t" \
  "v_mfma_f32_16x16x32_bf16 v[116:119], a[56:59], a[172:175], v[116:119]\n\t" \
  "buffer_load_dwordx4 %[ov2], %[ro], %[st] offen offset:2048 lds\n\t" \
  "buffer_load_dwordx4 %[ov3], %[ro], %[st] offen offset:3072 lds\n\t" \
  "s_add_u32 m0, %[mw], 4096\n\t" \
  "s_add_u32 %[st], %[so0], 303104\n\t" \
  "v_mfma_f32_16x16x32_bf16 v[120:123], a[60:63], a[168:171], v[120:123]\n\t" \
  "buffer_load_dwordx4 %[ov0], %[ro], %[st] offen offset:0 lds\n\t" \
  "buffer_load_dwordx4 %[ov1], %[ro], %[st] offen offset:1024 lds\n\t" \
  "buffer_load_dwordx4 %[ov2], %[ro], %[st] offen offset:2048 lds\n\t" \
  "buffer_load_dwordx4 %[ov3], %[ro], %[st] offen offset:3072 lds\n\t" \
  "v_mfma_f32_16x16x32_bf16 v[124:127], a[60:63], a[172:175], v[124:127]\n\t" \
  "s_waitcnt vmcnt(16)\n\t" \
  "v_mfma_f32_16x16x32_bf16 v[64:67], a[0:3], a[176:179], v[64:67]\n\t" \
  "ds_read_b64_tr_b16 a[32:33], %[va0] offset:8192\n\t" \
  "ds_read_b64_tr_b16 a[34:35], %[va0] offset:9216\n\t" \
  "v_mfma_f32_16x16x32_bf16 v[68:71], a[0:3], a[180:183], v[68:71]\n\t" \
  "ds_read_b64_tr_b16 a[36:37], %[va1] offset:8192\n\t" \
  "v_mfma_f32_16x16x32_bf16 v[72:75], a[4:7], a[176:179], v[72:75]\n\t" \
  "ds_read_b64_tr_b16 a[38:39], %[va1] offset:9216\n\t" \
  "ds_read_b64_tr_b16 a[40:41], %[va2] offset:8192\n\t" \
  "v_mfma_f32_16x16x32_bf16 v[76:79], a[4:7], a[180:183], v[76:79]\n\t" \
  "ds_read_b64_tr_b16 a[42:43], %[va2] offset:9216\n\t" \
  "v_mfma_f32_16x16x32_bf16 v[80:83], a[8:11], a[176:179], v[80:83]\n\t" \
  "ds_read_b64_tr_b16 a[44:45], %[va3] offset:8192\n\t" \
  "ds_read_b64_tr_b16 a[46:47], %[va3] offset:9216\n\t" \
  "v_mfma_f32_16x16x32_bf16 v[84:87], a[8:11], a[180:183], v[84:87]\n\t" \
  "ds_read_b64_tr_b16 a[48:49], %[va4] offset:8192\n\t" \
  "v_mfma_f32_16x16x32_bf16 v[88:91], a[12:15], a[176:179], v[88:91]\n\t" \
  "ds_read_b64_tr_b16 a[50:51], %[va4] offset:9216\n\t" \
  "ds_read_b64_tr_b16 a[52:53], %[va5] offset:8192\n\t" \
  "v_mfma_f32_16x16x32_bf16 v[92:95], a[12:15], a[180:183], v[92:95]\n\t" \
  "ds_read_b64_tr_b16 a[54:55], %[va5] offset:9216\n\t" \
  "v_mfma_f32_16x16x32_bf16 v[96:99], a[16:19], a[176:179], v[96:99]\n\t" \
  "ds_read_b64_tr_b16 a[56:57], %[va6] offset:8192\n\t" \
  "ds_read_b64_tr_b16 a[58:59], %[va6] offset:9216\n\t" \
  "v_mfma_f32_16x16x32_bf16 v[100:103], a[16:19], a[180:183], v[100:103]\n\t" \
  "ds_read_b64_tr_b16 a[60:61], %[va7] offset:8192\n\t" \
  "ds_read_b64_tr_b16 a[62:63], %[va7] offset:9216\n\t" \
  "v_mfma_f32_16x16x32_bf16 v[104:107], a[20:23], a[176:179], v[104:107]\n\t" \
  "v_mfma_f32_16x16x32_bf16 v[108:111], a[20:23], a[180:183], v[108:111]\n\t" \
  "v_mfma_f32_16x16x32_bf16 v[112:115], a[24:27], a[176:179], v[112:115]\n\t" \
  "s_waitcnt lgkmcnt(0)\n\t" \
  "s_add_u32 m0, %[mw], 8192\n\t" \
  "s_add_u32 %[st], %[so0], 327680\n\t" \
  "buffer_load_dwordx4 %[ov0], %[ro], %[st] offen offset:0 lds\n\t" \
  "buffer_load_dwordx4 %[ov1], %[ro], %[st] offen offset:1024 lds\n\t" \
  "v_mfma_f32_16x16x32_bf16 v[116:119], a[24:27], a[180:183], v[116:119]\n\t" \
  "buffer_load_dwordx4 %[ov2], %[ro], %[st] offen offset:2048 lds\n\t" \
  "buffer_load_dwordx4 %[ov3], %[ro], %[st] offen offset:3072 lds\n\t" \
  "s_add_u32 m0, %[mw], 12288\n\t" \
  "s_add_u32 %[st], %[so0], 335872\n\t" \
  "v_mfma_f32_16x16x32_bf16 v[120:123], a[28:31], a[176:179], v[120:123]\n\t" \
  "buffer_load_dwordx4 %[ov0], %[ro], %[st] offen offset:0 lds\n\t" \
  "buffer_load_dwordx4 %[ov1], %[ro], %[st] offen offset:1024 lds\n\t" \
  "buffer_load_dwordx4 %[ov2], %[ro], %[st] offen offset:2048 lds\n\t" \
  "buffer_load_dwordx4 %[ov3], %[ro], %[st] offen offset:3072 lds\n\t" \
  "v_mfma_f32_16x16x32_bf16 v[124:127], a[28:31], a[180:183], v[124:127]\n\t" \
  "s_waitcnt vmcnt(16)\n\t" \
  "v_mfma_f32_16x16x32_bf16 v[64:67], a[32:35], a[184:187], v[64:67]\n\t" \
  "ds_read_b64_tr_b16 a[0:1], %[va0] offset:16384\n\t" \
  "ds_read_b64_tr_b16 a[2:3], %[va0] offset:17408\n\t" \
  "v_mfma_f32_16x16x32_bf16 v[68:71], a[32:35], a[188:191], v[68:71]\n\t" \
  "ds_read_b64_tr_b16 a[4:5], %[va1] offset:16384\n\t" \
  "v_mfma_f32_16x16x32_bf16 v[72:75], a[36:39], a[184:187], v[72:75]\n\t" \
  "ds_read_b64_tr_b16 a[6:7], %[va1] offset:17408\n\t" \
  "ds_read_b64_tr_b16 a[8:9], %[va2] offset:16384\n\t" \
  "v_mfma_f32_16x16x32_bf16 v[76:79], a[36:39], a[188:191], v[76:79]\n\t" \
  "ds_read_b64_tr_b16 a[10:11], %[va2] offset:17408\n\t" \
  "v_mfma_f32_16x16x32_bf16 v[80:83], a[40:43], a[184:187], v[80:83]\n\t" \
  "ds_read_b64_tr_b16 a[12:13], %[va3] offset:16384\n\t" \
  "ds_read_b64_tr_b16 a[14:15], %[va3] offset:17408\n\t" \
  "v_mfma_f32_16x16x32_bf16 v[84:87], a[40:43], a[188:191], v[84:87]\n\t" \
  "ds_read_b64_tr_b16 a[16:17], %[va4] offset:16384\n\t" \
  "v_mfma_f32_16x16x32_bf16 v[88:91], a[44:47], a[184:187], v[88:91]\n\t" \
  "ds_read_b64_tr_b16 a[18:19], %[va4] offset:17408\n\t" \
  "ds_read_b64_tr_b16 a[20:21], %[va5] offset:16384\n\t" \
  "v_mfma_f32_16x16x32_bf16 v[92:95], a[44:47], a[188:191], v[92:95]\n\t" \
  "ds_read_b64_tr_b16 a[22:23], %[va5] offset:17408\n\t" \
  "v_mfma_f32_16x16x32_bf16 v[96:99], a[48:51], a[184:187], v[96:99]\n\t" \
  "ds_read_b64_tr_b16 a[24:25], %[va6] offset:16384\n\t" \
  "ds_read_b64_tr_b16 a[26:27], %[va6] offset:17408\n\t" \
  "v_mfma_f32_16x16x32_bf16 v[100:103], a[48:51], a[188:191], v[100:103]\n\t" \
  "ds_read_b64_tr_b16 a[28:29], %[va7] offset:16384\n\t" \
  "ds_read_b64_tr_b16 a[30:31], %[va7] offset:17408\n\t" \
  "v_mfma_f32_16x16x32_bf16 v[104:107], a[52:55], a[184:187], v[104:107]\n\t" \
  "v_mfma_f32_16x16x32_bf16 v[108:111], a[52:55], a[188:191], v[108:111]\n\t" \
  "v_mfma_f32_16x16x32_bf16 v[112:115], a[56:59], a[184:187], v[112:115]\n\t" \
  "s_waitcnt lgkmcnt(0)\n\t" \
  "s_add_u32 m0, %[mw], 16384\n\t" \
  "s_add_u32 %[st], %[so0], 360448\n\t" \
  "buffer_load_dwordx4 %[ov0], %[ro], %[st] offen offset:0 lds\n\t" \
  "buffer_load_dwordx4 %[ov1], %[ro], %[st] offen offset:1024 lds\n\t" \
  "v_mfma_f32_16x16x32_bf16 v[116:119], a[56:59], a[188:191], v[116:119]\n\t" \
  "buffer_load_dwordx4 %[ov2], %[ro], %[st] offen offset:2048 lds\n\t" \
  "buffer_load_dwordx4 %[ov3], %[ro], %[st] offen offset:3072 lds\n\t" \
  "s_add_u32 m0, %[mw], 20480\n\t" \
  "s_add_u32 %[st], %[so0], 368640\n\t" \
  "v_mfma_f32_16x16x32_bf16 v[120:123], a[60:63], a[184:187], v[120:123]\n\t" \
  "buffer_load_dwordx4 %[ov0], %[ro], %[st] offen offset:0 lds\n\t" \
  "buffer_load_dwordx4 %[ov1], %[ro], %[st] offen offset:1024 lds\n\t" \
  "buffer_load_dwordx4 %[ov2], %[ro], %[st] offen offset:2048 lds\n\t" \
  "buffer_load_dwordx4 %[ov3], %[ro], %[st] offen offset:3072 lds\n\t" \
  "v_mfma_f32_16x16x32_bf16 v[124:127], a[60:63], a[188:191], v[124:127]\n\t" \
  "s_waitcnt vmcnt(16)\n\t" \
  "v_mfma_f32_16x16x32_bf16 v[64:67], a[0:3], a[192:195], v[64:67]\n\t" \
  "ds_read_b64_tr_b16 a[32:33], %[va0] offset:0\n\t" \
  "ds_read_b64_tr_b16 a[34:35], %[va0] offset:1024\n\t" \
  "v_mfma_f32_16x16x32_bf16 v[68:71], a[0:3], a[196:199], v[68:71]\n\t" \
  "ds_read_b64_tr_b16 a[36:37], %[va1] offset:0\n\t" \
  "v_mfma_f32_16x16x32_bf16 v[72:75], a[4:7], a[192:195], v[72:75]\n\t" \
  "ds_read_b64_tr_b16 a[38:39], %[va1] offset:1024\n\t" \
  "ds_read_b64_tr_b16 a[40:41], %[va2] offset:0\n\t" \
  "v_mfma_f32_16x16x32_bf16 v[76:79], a[4:7], a[196:199], v[76:79]\n\t" \
  "ds_read_b64_tr_b16 a[42:43], %[va2] offset:1024\n\t" \
  "v_mfma_f32_16x16x32_bf16 v[80:83], a[8:11], a[192:195], v[80:83]\n\t" \
  "ds_read_b64_tr_b16 a[44:45], %[va3] offset:0\n\t" \
  "ds_read_b64_tr_b16 a[46:47], %[va3] offset:1024\n\t" \
  "v_mfma_f32_16x16x32_bf16 v[84:87], a[8:11], a[196:199], v[84:87]\n\t" \
  "ds_read_b64_tr_b16 a[48:49], %[va4] offset:0\n\t" \
  "v_mfma_f32_16x16x32_bf16 v[88:91], a[12:15], a[192:195], v[88:91]\n\t" \
  "ds_read_b64_tr_b16 a[50:51], %[va4] offset:1024\n\t" \
  "ds_read_b64_tr_b16 a[52:53], %[va5] offset:0\n\t" \
  "v_mfma_f32_16x16x32_bf16 v[92:95], a[12:15], a[196:199], v[92:95]\n\t" \
  "ds_read_b64_tr_b16 a[54:55], %[va5] offset:1024\n\t" \
  "v_mfma_f32_16x16x32_bf16 v[96:99], a[16:19], a[192:195], v[96:99]\n\t" \
  "ds_read_b64_tr_b16 a[56:57], %[va6] offset:0\n\t" \
  "ds_read_b64_tr_b16 a[58:59], %[va6] offset:1024\n\t" \
  "v_mfma_f32_16x16x32_bf16 v[100:103], a[16:19], a[196:199], v[100:103]\n\t" \
  "ds_read_b64_tr_b16 a[60:61], %[va7] offset:0\n\t" \
  "ds_read_b64_tr_b16 a[62:63], %[va7] offset:1024\n\t" \
  "v_mfma_f32_16x16x32_bf16 v[104:107], a[20:23], a[192:195], v[104:107]\n\t" \
  "v_mfma_f32_16x16x32_bf16 v[108:111], a[20:23], a[196:199], v[108:111]\n\t" \
  "v_mfma_f32_16x16x32_bf16 v[112:115], a[24:27], a[192:195], v[112:115]\n\t" \
  "s_waitcnt lgkmcnt(0)\n\t" \
  "s_add_u32 m0, %[mw], 0\n\t" \
  "s_add_u32 %[st], %[so0], 393216\n\t" \
  "buffer_load_dwordx4 %[ov0], %[ro], %[st] offen offset:0 lds\n\t" \
  "buffer_load_dwordx4 %[ov1], %[ro], %[st] offen offset:1024 lds\n\t" \
  "v_mfma_f32_16x16x32_bf16 v[116:119], a[24:27], a[196:199], v[116:119]\n\t" \
  "buffer_load_dwordx4 %[ov2], %[ro], %[st] offen offset:2048 lds\n\t" \
  "buffer_load_dwordx4 %[ov3], %[ro], %[st] offen offset:3072 lds\n\t" \
  "s_add_u32 m0, %[mw], 4096\n\t" \
  "s_add_u32 %[st], %[so0], 401408\n\t" \
  "v_mfma_f32_16x16x32_bf16 v[120:123], a[28:31], a[192:195], v[120:123]\n\t" \
  "buffer_load_dwordx4 %[ov0], %[ro], %[st] offen offset:0 lds\n\t" \
  "buffer_load_dwordx4 %[ov1], %[ro], %[st] offen offset:1024 lds\n\t" \
  "buffer_load_dwordx4 %[ov2], %[ro], %[st] offen offset:2048 lds\n\t" \
  "buffer_load_dwordx4 %[ov3], %[ro], %[st] offen offset:3072 lds\n\t" \
  "v_mfma_f32_16x16x32_bf16 v[124:127], a[28:31], a[196:199], v[124:127]\n\t" \
  "s_waitcnt vmcnt(16)\n\t" \
  "v_mfma_f32_16x16x32_bf16 v[64:67], a[32:35], a[200:203], v[64:67]\n\t" \
  "ds_read_b64_tr_b16 a[0:1], %[va0] offset:8192\n\t" \
  "ds_read_b64_tr_b16 a[2:3], %[va0] offset:9216\n\t" \
  "v_mfma_f32_16x16x32_bf16 v[68:71], a[32:35], a[204:207], v[68:71]\n\t" \
  "ds_read_b64_tr_b16 a[4:5], %[va1] offset:8192\n\t" \
  "v_mfma_f32_16x16x32_bf16 v[72:75], a[36:39], a[200:203], v[72:75]\n\t" \
  "ds_read_b64_tr_b16 a[6:7], %[va1] offset:9216\n\t" \
  "ds_read_b64_tr_b16 a[8:9], %[va2] offset:8192\n\t" \
  "v_mfma_f32_16x16x32_bf16 v[76:79], a[36:39], a[204:207], v[76:79]\n\t" \
  "ds_read_b64_tr_b16 a[10:11], %[va2] offset:9216\n\t" \
  "v_mfma_f32_16x16x32_bf16 v[80:83], a[40:43], a[200:203], v[80:83]\n\t" \
  "ds_read_b64_tr_b16 a[12:13], %[va3] offset:8192\n\t" \
  "ds_read_b64_tr_b16 a[14:15], %[va3] offset:9216\n\t" \
  "v_mfma_f32_16x16x32_bf16 v[84:87], a[40:43], a[204:207], v[84:87]\n\t" \
  "ds_read_b64_tr_b16 a[16:17], %[va4] offset:8192\n\t" \
  "v_mfma_f32_16x16x32_bf16 v[88:91], a[44:47], a[200:203], v[88:91]\n\t" \
  "ds_read_b64_tr_b16 a[18:19], %[va4] offset:9216\n\t" \
  "ds_read_b64_tr_b16 a[20:21], %[va5] offset:8192\n\t" \
  "v_mfma_f32_16x16x32_bf16 v[92:95], a[44:47], a[204:207], v[92:95]\n\t" \
  "ds_read_b64_tr_b16 a[22:23], %[va5] offset:9216\n\t" \
  "v_mfma_f32_16x16x32_bf16 v[96:99], a[48:51], a[200:203], v[96:99]\n\t" \
  "ds_read_b64_tr_b16 a[24:25], %[va6] offset:8192\n\t" \
  "ds_read_b64_tr_b16 a[26:27], %[va6] offset:9216\n\t" \
  "v_mfma_f32_16x16x32_bf16 v[100:103], a[48:51], a[204:207], v[100:103]\n\t" \
  "ds_read_b64_tr_b16 a[28:29], %[va7] offset:8192\n\t" \
  "ds_read_b64_tr_b16 a[30:31], %[va7] offset:9216\n\t" \
  "v_mfma_f32_16x16x32_bf16 v[104:107], a[52:55], a[200:203], v[104:107]\n\t" \
  "v_mfma_f32_16x16x32_bf16 v[108:111], a[52:55], a[204:207], v[108:111]\n\t" \
  "v_mfma_f32_16x16x32_bf16 v[112:115], a[56:59], a[200:203], v[112:115]\n\t" \
  "s_waitcnt lgkmcnt(0)\n\t" \
  "s_add_u32 m0, %[mw], 8192\n\t" \
  "s_add_u32 %[st], %[so0], 425984\n\t" \
  "buffer_load_dwordx4 %[ov0], %[ro], %[st] offen offset:0 lds\n\t" \
  "buffer_load_dwordx4 %[ov1], %[ro], %[st] offen offset:1024 lds\n\t" \
  "v_mfma_f32_16x16x32_bf16 v[116:119], a[56:59], a[204:207], v[116:119]\n\t" \
  "buffer_load_dwordx4 %[ov2], %[ro], %[st] offen offset:2048 lds\n\t" \
  "buffer_load_dwordx4 %[ov3], %[ro], %[st] offen offset:3072 lds\n\t" \
  "s_add_u32 m0, %[mw], 12288\n\t" \
  "s_add_u32 %[st], %[so0], 434176\n\t" \
  "v_mfma_f32_16x16x32_bf16 v[120:123], a[60:63], a[200:203], v[120:123]\n\t" \
  "buffer_load_dwordx4 %[ov0], %[ro], %[st] offen offset:0 lds\n\t" \
  "buffer_load_dwordx4 %[ov1], %[ro], %[st] offen offset:1024 lds\n\t" \
  "buffer_load_dwordx4 %[ov2], %[ro], %[st] offen offset:2048 lds\n\t" \
  "buffer_load_dwordx4 %[ov3], %[ro], %[st] offen offset:3072 lds\n\t" \
  "v_mfma_f32_16x16x32_bf16 v[124:127], a[60:63], a[204:207], v[124:127]\n\t" \
  "s_waitcnt vmcnt(16)\n\t" \
  "v_mfma_f32_16x16x32_bf16 v[64:67], a[0:3], a[208:211], v[64:67]\n\t" \
  "ds_read_b64_tr_b16 a[32:33], %[va0] offset:16384\n\t" \
  "ds_read_b64_tr_b16 a[34:35], %[va0] offset:17408\n\t" \
  "v_mfma_f32_16x16x32_bf16 v[68:71], a[0:3], a[212:215], v[68:71]\n\t" \
  "ds_read_b64_tr_b16 a[36:37], %[va1] offset:16384\n\t" \
  "v_mfma_f32_16x16x32_bf16 v[72:75], a[4:7], a[208:211], v[72:75]\n\t" \
  "ds_read_b64_tr_b16 a[38:39], %[va1] offset:17408\n\t" \
  "ds_read_b64_tr_b16 a[40:41], %[va2] offset:16384\n\t" \
  "v_mfma_f32_16x16x32_bf16 v[76:79], a[4:7], a[212:215], v[76:79]\n\t" \
  "ds_read_b64_tr_b16 a[42:43], %[va2] offset:17408\n\t" \
  "v_mfma_f32_16x16x32_bf16 v[80:83], a[8:11], a[208:211], v[80:83]\n\t" \
  "ds_read_b64_tr_b16 a[44:45], %[va3] offset:16384\n\t" \
  "ds_read_b64_tr_b16 a[46:47], %[va3] offset:17408\n\t" \
  "v_mfma_f32_16x16x32_bf16 v[84:87], a[8:11], a[212:215], v[84:87]\n\t" \
  "ds_read_b64_tr_b16 a[48:49], %[va4] offset:16384\n\t" \
  "v_mfma_f32_16x16x32_bf16 v[88:91], a[12:15], a[208:211], v[88:91]\n\t" \
  "ds_read_b64_tr_b16 a[50:51], %[va4] offset:17408\n\t" \
  "ds_read_b64_tr_b16 a[52:53], %[va5] offset:16384\n\t" \
  "v_mfma_f32_16x16x32_bf16 v[92:95], a[12:15], a[212:215], v[92:95]\n\t" \
  "ds_read_b64_tr_b16 a[54:55], %[va5] offset:17408\n\t" \
  "v_mfma_f32_16x16x32_bf16 v[96:99], a[16:19], a[208:211], v[96:99]\n\t" \
  "ds_read_b64_tr_b16 a[56:57], %[va6] offset:16384\n\t" \
  "ds_read_b64_tr_b16 a[58:59], %[va6] offset:17408\n\t" \
  "v_mfma_f32_16x16x32_bf16 v[100:103], a[16:19], a[212:215], v[100:103]\n\t" \
  "ds_read_b64_tr_b16 a[60:61], %[va7] offset:16384\n\t" \
  "ds_read_b64_tr_b16 a[62:63], %[va7] offset:17408\n\t" \
  "v_mfma_f32_16x16x32_bf16 v[104:107], a[20:23], a[208:211], v[104:107]\n\t" \
  "v_mfma_f32_16x16x32_bf16 v[108:111], a[20:23], a[212:215], v[108:111]\n\t" \
  "v_mfma_f32_16x16x32_bf16 v[112:115], a[24:27], a[208:211], v[112:115]\n\t" \
  "s_waitcnt lgkmcnt(0)\n\t" \
  "s_add_u32 m0, %[mw], 16384\n\t" \
  "s_add_u32 %[st], %[so0], 458752\n\t" \
  "buffer_load_dwordx4 %[ov0], %[ro], %[st] offen offset:0 lds\n\t" \
  "buffer_load_dwordx4 %[ov1], %[ro], %[st] offen offset:1024 lds\n\t" \
  "v_mfma_f32_16x16x32_bf16 v[116:119], a[24:27], a[212:215], v[116:119]\n\t" \
  "buffer_load_dwordx4 %[ov2], %[ro], %[st] offen offset:2048 lds\n\t" \
  "buffer_load_dwordx4 %[ov3], %[ro], %[st] offen offset:3072 lds\n\t" \
  "s_add_u32 m0, %[mw], 20480\n\t" \
  "s_add_u32 %[st], %[so0], 466944\n\t" \
  "v_mfma_f32_16x16x32_bf16 v[120:123], a[28:31], a[208:211], v[120:123]\n\t" \
  "buffer_load_dwordx4 %[ov0], %[ro], %[st] offen offset:0 lds\n\t" \
  "buffer_load_dwordx4 %[ov1], %[ro], %[st] offen offset:1024 lds\n\t" \
  "buffer_load_dwordx4 %[ov2], %[ro], %[st] offen offset:2048 lds\n\t" \
  "buffer_load_dwordx4 %[ov3], %[ro], %[st] offen offset:3072 lds\n\t" \
  "v_mfma_f32_16x16x32_bf16 v[124:127], a[28:31], a[212:215], v[124:127]\n\t" \
  "s_waitcnt vmcnt(16)\n\t" \
  "v_mfma_f32_16x16x32_bf16 v[64:67], a[32:35], a[216:219], v[64:67]\n\t" \
  "ds_read_b64_tr_b16 a[0:1], %[va0] offset:0\n\t" \
  "ds_read_b64_tr_b16 a[2:3], %[va0] offset:1024\n\t" \
  "v_mfma_f32_16x16x32_bf16 v[68:71], a[32:35], a[220:223], v[68:71]\n\t" \
  "ds_read_b64_tr_b16 a[4:5], %[va1] offset:0\n\t" \
  "v_mfma_f32_16x16x32_bf16 v[72:75], a[36:39], a[216:219], v[72:75]\n\t" \
  "ds_read_b64_tr_b16 a[6:7], %[va1] offset:1024\n\t" \
  "ds_read_b64_tr_b16 a[8:9], %[va2] offset:0\n\t" \
  "v_mfma_f32_16x16x32_bf16 v[76:79], a[36:39], a[220:223], v[76:79]\n\t" \
  "ds_read_b64_tr_b16 a[10:11], %[va2] offset:1024\n\t" \
  "v_mfma_f32_16x16x32_bf16 v[80:83], a[40:43], a[216:219], v[80:83]\n\t" \
  "ds_read_b64_tr_b16 a[12:13], %[va3] offset:0\n\t" \
  "ds_read_b64_tr_b16 a[14:15], %[va3] offset:1024\n\t" \
  "v_mfma_f32_16x16x32_bf16 v[84:87], a[40:43], a[220:223], v[84:87]\n\t" \
  "ds_read_b64_tr_b16 a[16:17], %[va4] offset:0\n\t" \
  "v_mfma_f32_16x16x32_bf16 v[88:91], a[44:47], a[216:219], v[88:91]\n\t" \
  "ds_read_b64_tr_b16 a[18:19], %[va4] offset:1024\n\t" \
  "ds_read_b64_tr_b16 a[20:21], %[va5] offset:0\n\t" \
  "v_mfma_f32_16x16x32_bf16 v[92:95], a[44:47], a[220:223], v[92:95]\n\t" \
  "ds_read_b64_tr_b16 a[22:23], %[va5] offset:1024\n\t" \
  "v_mfma_f32_16x16x32_bf16 v[96:99], a[48:51], a[216:219], v[96:99]\n\t" \
  "ds_read_b64_tr_b16 a[24:25], %[va6] offset:0\n\t" \
  "ds_read_b64_tr_b16 a[26:27], %[va6] offset:1024\n\t" \
  "v_mfma_f32_16x16x32_bf16 v[100:103], a[48:51], a[220:223], v[100:103]\n\t" \
  "ds_read_b64_tr_b16 a[28:29], %[va7] offset:0\n\t" \
  "ds_read_b64_tr_b16 a[30:31], %[va7] offset:1024\n\t" \
  "v_mfma_f32_16x16x32_bf16 v[104:107], a[52:55], a[216:219], v[104:107]\n\t" \
  "v_mfma_f32_16x16x32_bf16 v[108:111], a[52:55], a[220:223], v[108:111]\n\t" \
  "v_mfma_f32_16x16x32_bf16 v[112:115], a[56:59], a[216:219], v[112:115]\n\t" \
  "s_waitcnt lgkmcnt(0)\n\t" \
  "s_add_u32 m0, %[mw], 0\n\t" \
  "s_add_u32 %[st], %[so0], 491520\n\t" \
  "buffer_load_dwordx4 %[ov0], %[ro], %[st] offen offset:0 lds\n\t" \
  "buffer_load_dwordx4 %[ov1], %[ro], %[st] offen offset:1024 lds\n\t" \
  "v_mfma_f32_16x16x32_bf16 v[116:119], a[56:59], a[220:223], v[116:119]\n\t" \
  "buffer_load_dwordx4 %[ov2], %[ro], %[st] offen offset:2048 lds\n\t" \
  "buffer_load_dwordx4 %[ov3], %[ro], %[st] offen offset:3072 lds\n\t" \
  "s_add_u32 m0, %[mw], 4096\n\t" \
  "s_add_u32 %[st], %[so0], 499712\n\t" \
  "v_mfma_f32_16x16x32_bf16 v[120:123], a[60:63], a[216:219], v[120:123]\n\t" \
  "buffer_load_dwordx4 %[ov0], %[ro], %[st] offen offset:0 lds\n\t" \
  "buffer_load_dwordx4 %[ov1], %[ro], %[st] offen offset:1024 lds\n\t" \
  "buffer_load_dwordx4 %[ov2], %[ro], %[st] offen offset:2048 lds\n\t" \
  "buffer_load_dwordx4 %[ov3], %[ro], %[st] offen offset:3072 lds\n\t" \
  "v_mfma_f32_16x16x32_bf16 v[124:127], a[60:63], a[220:223], v[124:127]\n\t" \
  "s_waitcnt vmcnt(16)\n\t" \
  "v_mfma_f32_16x16x32_bf16 v[64:67], a[0:3], a[224:227], v[64:67]\n\t" \
  "ds_read_b64_tr_b16 a[32:33], %[va0] offset:8192\n\t" \
  "ds_read_b64_tr_b16 a[34:35], %[va0] offset:9216\n\t" \
  "v_mfma_f32_16x16x32_bf16 v[68:71], a[0:3], a[228:231], v[68:71]\n\t" \
  "ds_read_b64_tr_b16 a[36:37], %[va1] offset:8192\n\t" \
  "v_mfma_f32_16x16x32_bf16 v[72:75], a[4:7], a[224:227], v[72:75]\n\t" \
  "ds_read_b64_tr_b16 a[38:39], %[va1] offset:9216\n\t" \
  "ds_read_b64_tr_b16 a[40:41], %[va2] offset:8192\n\t" \
  "v_mfma_f32_16x16x32_bf16 v[76:79], a[4:7], a[228:231], v[76:79]\n\t" \
  "ds_read_b64_tr_b16 a[42:43], %[va2] offset:9216\n\t" \
  "v_mfma_f32_16x16x32_bf16 v[80:83], a[8:11], a[224:227], v[80:83]\n\t" \
  "ds_read_b64_tr_b16 a[44:45], %[va3] offset:8192\n\t" \
  "ds_read_b64_tr_b16 a[46:47], %[va3] offset:9216\n\t" \
  "v_mfma_f32_16x16x32_bf16 v[84:87], a[8:11], a[228:231], v[84:87]\n\t" \
  "ds_read_b64_tr_b16 a[48:49], %[va4] offset:8192\n\t" \
  "v_mfma_f32_16x16x32_bf16 v[88:91], a[12:15], a[224:227], v[88:91]\n\t" \
  "ds_read_b64_tr_b16 a[50:51], %[va4] offset:9216\n\t" \
  "ds_read_b64_tr_b16 a[52:53], %[va5] offset:8192\n\t" \
  "v_mfma_f32_16x16x32_bf16 v[92:95], a[12:15], a[228:231], v[92:95]\n\t" \
  "ds_read_b64_tr_b16 a[54:55], %[va5] offset:9216\n\t" \
  "v_mfma_f32_16x16x32_bf16 v[96:99], a[16:19], a[224:227], v[96:99]\n\t" \
  "ds_read_b64_tr_b16 a[56:57], %[va6] offset:8192\n\t" \
  "ds_read_b64_tr_b16 a[58:59], %[va6] offset:9216\n\t" \
  "v_mfma_f32_16x16x32_bf16 v[100:103], a[16:19], a[228:231], v[100:103]\n\t" \
  "ds_read_b64_tr_b16 a[60:61], %[va7] offset:8192\n\t" \
  "ds_read_b64_tr_b16 a[62:63], %[va7] offset:9216\n\t" \
  "v_mfma_f32_16x16x32_bf16 v[104:107], a[20:23], a[224:227], v[104:107]\n\t" \
  "v_mfma_f32_16x16x32_bf16 v[108:111], a[20:23], a[228:231], v[108:111]\n\t" \
  "v_mfma_f32_16x16x32_bf16 v[112:115], a[24:27], a[224:227], v[112:115]\n\t" \
  "s_waitcnt lgkmcnt(0)\n\t" \
  "v_mfma_f32_16x16x32_bf16 v[116:119], a[24:27], a[228:231], v[116:119]\n\t" \
  "v_mfma_f32_16x16x32_bf16 v[120:123], a[28:31], a[224:227], v[120:123]\n\t" \
  "v_mfma_f32_16x16x32_bf16 v[124:127], a[28:31], a[228:231], v[124:127]\n\t" \
  "s_waitcnt vmcnt(8)\n\t" \
  "v_mfma_f32_16x16x32_bf16 v[64:67], a[32:35], a[232:235], v[64:67]\n\t" \
  "ds_read_b64_tr_b16 a[0:1], %[va0] offset:16384\n\t" \
  "ds_read_b64_tr_b16 a[2:3], %[va0] offset:17408\n\t" \
  "v_mfma_f32_16x16x32_bf16 v[68:71], a[32:35], a[236:239], v[68:71]\n\t" \
  "ds_read_b64_tr_b16 a[4:5], %[va1] offset:16384\n\t" \
  "v_mfma_f32_16x16x32_bf16 v[72:75], a[36:39], a[232:235], v[72:75]\n\t" \
  "ds_read_b64_tr_b16 a[6:7], %[va1] offset:17408\n\t" \
  "ds_read_b64_tr_b16 a[8:9], %[va2] offset:16384\n\t" \
  "v_mfma_f32_16x16x32_bf16 v[76:79], a[36:39], a[236:239], v[76:79]\n\t" \
  "ds_read_b64_tr_b16 a[10:11], %[va2] offset:17408\n\t" \
  "v_mfma_f32_16x16x32_bf16 v[80:83], a[40:43], a[232:235], v[80:83]\n\t" \
  "ds_read_b64_tr_b16 a[12:13], %[va3] offset:16384\n\t" \
  "ds_read_b64_tr_b16 a[14:15], %[va3] offset:17408\n\t" \
  "v_mfma_f32_16x16x32_bf16 v[84:87], a[40:43], a[236:239], v[84:87]\n\t" \
  "ds_read_b64_tr_b16 a[16:17], %[va4] offset:16384\n\t" \
  "v_mfma_f32_16x16x32_bf16 v[88:91], a[44:47], a[232:235], v[88:91]\n\t" \
  "ds_read_b64_tr_b16 a[18:19], %[va4] offset:17408\n\t" \
  "ds_read_b64_tr_b16 a[20:21], %[va5] offset:16384\n\t" \
  "v_mfma_f32_16x16x32_bf16 v[92:95], a[44:47], a[236:239], v[92:95]\n\t" \
  "ds_read_b64_tr_b16 a[22:23], %[va5] offset:17408\n\t" \
  "v_mfma_f32_16x16x32_bf16 v[96:99], a[48:51], a[232:235], v[96:99]\n\t" \
  "ds_read_b64_tr_b16 a[24:25], %[va6] offset:16384\n\t" \
  "ds_read_b64_tr_b16 a[26:27], %[va6] offset:17408\n\t" \
  "v_mfma_f32_16x16x32_bf16 v[100:103], a[48:51], a[236:239], v[100:103]\n\t" \
  "ds_read_b64_tr_b16 a[28:29], %[va7] offset:16384\n\t" \
  "ds_read_b64_tr_b16 a[30:31], %[va7] offset:17408\n\t" \
  "v_mfma_f32_16x16x32_bf16 v[104:107], a[52:55], a[232:235], v[104:107]\n\t" \
  "v_mfma_f32_16x16x32_bf16 v[108:111], a[52:55], a[236:239], v[108:111]\n\t" \
  "v_mfma_f32_16x16x32_bf16 v[112:115], a[56:59], a[232:235], v[112:115]\n\t" \
  "s_waitcnt lgkmcnt(0)\n\t" \
  "v_mfma_f32_16x16x32_bf16 v[116:119], a[56:59], a[236:239], v[116:119]\n\t" \
  "v_mfma_f32_16x16x32_bf16 v[120:123], a[60:63], a[232:235], v[120:123]\n\t" \
  "v_mfma_f32_16x16x32_bf16 v[124:127], a[60:63], a[236:239], v[124:127]\n\t" \
  "s_waitcnt vmcnt(0)\n\t" \
  "v_mfma_f32_16x16x32_bf16 v[64:67], a[0:3], a[240:243], v[64:67]\n\t" \
  "ds_read_b64_tr_b16 a[32:33], %[va0] offset:0\n\t" \
  "ds_read_b64_tr_b16 a[34:35], %[va0] offset:1024\n\t" \
  "v_mfma_f32_16x16x32_bf16 v[68:71], a[0:3], a[244:247], v[68:71]\n\t" \
  "ds_read_b64_tr_b16 a[36:37], %[va1] offset:0\n\t" \
  "v_mfma_f32_16x16x32_bf16 v[72:75], a[4:7], a[240:243], v[72:75]\n\t" \
  "ds_read_b64_tr_b16 a[38:39], %[va1] offset:1024\n\t" \
  "ds_read_b64_tr_b16 a[40:41], %[va2] offset:0\n\t" \
  "v_mfma_f32_16x16x32_bf16 v[76:79], a[4:7], a[244:247], v[76:79]\n\t" \
  "ds_read_b64_tr_b16 a[42:43], %[va2] offset:1024\n\t" \
  "v_mfma_f32_16x16x32_bf16 v[80:83], a[8:11], a[240:243], v[80:83]\n\t" \
  "ds_read_b64_tr_b16 a[44:45], %[va3] offset:0\n\t" \
  "ds_read_b64_tr_b16 a[46:47], %[va3] offset:1024\n\t" \
  "v_mfma_f32_16x16x32_bf16 v[84:87], a[8:11], a[244:247], v[84:87]\n\t" \
  "ds_read_b64_tr_b16 a[48:49], %[va4] offset:0\n\t" \
  "v_mfma_f32_16x16x32_bf16 v[88:91], a[12:15], a[240:243], v[88:91]\n\t" \
  "ds_read_b64_tr_b16 a[50:51], %[va4] offset:1024\n\t" \
  "ds_read_b64_tr_b16 a[52:53], %[va5] offset:0\n\t" \
  "v_mfma_f32_16x16x32_bf16 v[92:95], a[12:15], a[244:247], v[92:95]\n\t" \
  "ds_read_b64_tr_b16 a[54:55], %[va5] offset:1024\n\t" \
  "v_mfma_f32_16x16x32_bf16 v[96:99], a[16:19], a[240:243], v[96:99]\n\t" \
  "ds_read_b64_tr_b16 a[56:57], %[va6] offset:0\n\t" \
  "ds_read_b64_tr_b16 a[58:59], %[va6] offset:1024\n\t" \
  "v_mfma_f32_16x16x32_bf16 v[100:103], a[16:19], a[244:247], v[100:103]\n\t" \
  "ds_read_b64_tr_b16 a[60:61], %[va7] offset:0\n\t" \
  "ds_read_b64_tr_b16 a[62:63], %[va7] offset:1024\n\t" \
  "v_mfma_f32_16x16x32_bf16 v[104:107], a[20:23], a[240:243], v[104:107]\n\t" \
  "v_mfma_f32_16x16x32_bf16 v[108:111], a[20:23], a[244:247], v[108:111]\n\t" \
  "v_mfma_f32_16x16x32_bf16 v[112:115], a[24:27], a[240:243], v[112:115]\n\t" \
  "s_waitcnt lgkmcnt(0)\n\t" \
  "v_mfma_f32_16x16x32_bf16 v[116:119], a[24:27], a[244:247], v[116:119]\n\t" \
  "v_mfma_f32_16x16x32_bf16 v[120:123], a[28:31], a[240:243], v[120:123]\n\t" \
  "v_mfma_f32_16x16x32_bf16 v[124:127], a[28:31], a[244:247], v[124:127]\n\t" \
  "v_mfma_f32_16x16x32_bf16 v[64:67], a[32:35], a[248:251], v[64:67]\n\t" \
  "v_mfma_f32_16x16x32_bf16 v[68:71], a[32:35], a[252:255], v[68:71]\n\t" \
  "v_mfma_f32_16x16x32_bf16 v[72:75], a[36:39], a[248:251], v[72:75]\n\t" \
  "v_mfma_f32_16x16x32_bf16 v[76:79], a[36:39], a[252:255], v[76:79]\n\t" \
  "v_mfma_f32_16x16x32_bf16 v[80:83], a[40:43], a[248:251], v[80:83]\n\t" \
  "v_mfma_f32_16x16x32_bf16 v[84:87], a[40:43], a[252:255], v[84:87]\n\t" \
  "v_mfma_f32_16x16x32_bf16 v[88:91], a[44:47], a[248:251], v[88:91]\n\t" \
  "v_mfma_f32_16x16x32_bf16 v[92:95], a[44:47], a[252:255], v[92:95]\n\t" \
  "v_mfma_f32_16x16x32_bf16 v[96:99], a[48:51], a[248:251], v[96:99]\n\t" \
  "v_mfma_f32_16x16x32_bf16 v[100:103], a[48:51], a[252:255], v[100:103]\n\t" \
  "v_mfma_f32_16x16x32_bf16 v[104:107], a[52:55], a[248:251], v[104:107]\n\t" \
  "v_mfma_f32_16x16x32_bf16 v[108:111], a[52:55], a[252:255], v[108:111]\n\t" \
  "v_mfma_f32_16x16x32_bf16 v[112:115], a[56:59], a[248:251], v[112:115]\n\t" \
  "v_mfma_f32_16x16x32_bf16 v[116:119], a[56:59], a[252:255], v[116:119]\n\t" \
  "v_mfma_f32_16x16x32_bf16 v[120:123], a[60:63], a[248:251], v[120:123]\n\t" \
  "v_mfma_f32_16x16x32_bf16 v[124:127], a[60:63], a[252:255], v[124:127]\n\t" \
  "s_cmp_lg_u32 %[wid], 0\n\t" \
  "s_cbranch_scc1 .Lgo_%=\n\t" \
  "s_mov_b32 %[st], 0x8000\n\t" \
  ".Lpoll_%=:\n\t" \
  "buffer_load_dwordx2 v[128:129], %[ev], %[rg], 0 offen sc1\n\t" \
  "s_waitcnt vmcnt(0)\n\t" \
  "v_cmp_ne_u64 vcc, %[tag], v[128:129]\n\t" \
  "s_cbranch_vccz .Lgo_%=\n\t" \
  "s_sleep 6\n\t" \
  "s_sub_u32 %[st], %[st], 1\n\t" \
  "s_cbranch_scc0 .Lpoll_%=\n\t" \
  ".Lgo_%=:\n\t" \
  "s_barrier\n\t" \
  "s_nop 7\n\t" \
  "s_add_u32 %[st], %[sp], 0\n\t" \
  "v_cvt_pk_bf16_f32 v168, v64, v65\n\t" \
  "v_cvt_pk_bf16_f32 v169, v66, v67\n\t" \
  "v_cvt_pk_bf16_f32 v170, v72, v73\n\t" \
  "v_cvt_pk_bf16_f32 v171, v74, v75\n\t" \
  "buffer_store_dwordx4 v[168:171], %[zv], %[rp], %[st] offen offset:0 sc1\n\t" \
  "v_cvt_pk_bf16_f32 v172, v80, v81\n\t" \
  "v_cvt_pk_bf16_f32 v173, v82, v83\n\t" \
  "v_cvt_pk_bf16_f32 v174, v88, v89\n\t" \
  "v_cvt_pk_bf16_f32 v175, v90, v91\n\t" \
  "buffer_store_dwordx4 v[172:175], %[zv], %[rp], %[st] offen offset:1024 sc1\n\t" \
  "v_cvt_pk_bf16_f32 v176, v96, v97\n\t" \
  "v_cvt_pk_bf16_f32 v177, v98, v99\n\t" \
  "v_cvt_pk_bf16_f32 v178, v104, v105\n\t" \
  "v_cvt_pk_bf16_f32 v179, v106, v107\n\t" \
  "buffer_store_dwordx4 v[176:179], %[zv], %[rp], %[st] offen offset:2048 sc1\n\t" \
  "v_cvt_pk_bf16_f32 v180, v112, v113\n\t" \
  "v_cvt_pk_bf16_f32 v181, v114, v115\n\t" \
  "v_cvt_pk_bf16_f32 v182, v120, v121\n\t" \
  "v_cvt_pk_bf16_f32 v183, v122, v123\n\t" \
  "buffer_store_dwordx4 v[180:183], %[zv], %[rp], %[st] offen offset:3072 sc1\n\t" \
  "s_add_u32 %[st], %[sp], 4096\n\t" \
  "v_cvt_pk_bf16_f32 v184, v68, v69\n\t" \
  "v_cvt_pk_bf16_f32 v185, v70, v71\n\t" \
  "v_cvt_pk_bf16_f32 v186, v76, v77\n\t" \
  "v_cvt_pk_bf16_f32 v187, v78, v79\n\t" \
  "buffer_store_dwordx4 v[184:187], %[zv], %[rp], %[st] offen offset:0 sc1\n\t" \
  "v_cvt_pk_bf16_f32 v188, v84, v85\n\t" \
  "v_cvt_pk_bf16_f32 v189, v86, v87\n\t" \
  "v_cvt_pk_bf16_f32 v190, v92, v93\n\t" \
  "v_cvt_pk_bf16_f32 v191, v94, v95\n\t" \
  "buffer_store_dwordx4 v[188:191], %[zv], %[rp], %[st] offen offset:1024 sc1\n\t" \
  "v_cvt_pk_bf16_f32 v128, v100, v101\n\t" \
  "v_cvt_pk_bf16_f32 v129, v102, v103\n\t" \
  "v_cvt_pk_bf16_f32 v130, v108, v109\n\t" \
  "v_cvt_pk_bf16_f32 v131, v110, v111\n\t" \
  "buffer_store_dwordx4 v[128:131], %[zv], %[rp], %[st] offen offset:2048 sc1\n\t" \
  "v_cvt_pk_bf16_f32 v132, v116, v117\n\t" \
  "v_cvt_pk_bf16_f32 v133, v118, v119\n\t" \
  "v_cvt_pk_bf16_f32 v134, v124, v125\n\t" \
  "v_cvt_pk_bf16_f32 v135, v126, v127\n\t" \
  "buffer_store_dwordx4 v[132:135], %[zv], %[rp], %[st] offen offset:3072 sc1\n\t" \
  "s_branch .Lend_%=\n\t" \
  ".Lrare0_%=:\n\t" \
  "s_nop 7\n\t" \
  "s_nop 7\n\t" \
  "v_max3_f32 v216, v168, v169, v170\n\t" \
  "v_max3_f32 v217, v171, v172, v173\n\t" \
  "v_max3_f32 v216, v216, v174, v175\n\t" \
  "v_max_f32 v216, v216, v217\n\t" \
  "v_mov_b32 v217, v216\n\t" \
  "s_nop 1\n\t" \
  "v_permlane16_swap_b32 v216, v217\n\t" \
  "s_nop 1\n\t" \
  "v_max_f32 v216, v216, v217\n\t" \
  "v_mov_b32 v217, v216\n\t" \
  "s_nop 1\n\t" \
  "v_permlane32_swap_b32 v216, v217\n\t" \
  "s_nop 1\n\t" \
  "v_max_f32 v216, v216, v217\n\t" \
  "v_add_f32 v218, 0x40c00000, v216\n\t" \
  "v_max_f32 v218, 0, v218\n\t" \
  "v_sub_f32 v168, v168, v218\n\t" \
  "v_sub_f32 v169, v169, v218\n\t" \
  "v_sub_f32 v170, v170, v218\n\t" \
  "v_sub_f32 v171, v171, v218\n\t" \
  "v_sub_f32 v172, v172, v218\n\t" \
  "v_sub_f32 v173, v173, v218\n\t" \
  "v_sub_f32 v174, v174, v218\n\t" \
  "v_sub_f32 v175, v175, v218\n\t" \
  "v_sub_f32 v200, v200, v218\n\t" \
  "v_sub_f32 v201, v201, v218\n\t" \
  "v_sub_f32 v202, v202, v218\n\t" \
  "v_sub_f32 v203, v203, v218\n\t" \
  "v_sub_f32 v176, v176, v218\n\t" \
  "v_sub_f32 v177, v177, v218\n\t" \
  "v_sub_f32 v178, v178, v218\n\t" \
  "v_sub_f32 v179, v179, v218\n\t" \
  "v_sub_f32 v180, v180, v218\n\t" \
  "v_sub_f32 v181, v181, v218\n\t" \
  "v_sub_f32 v182, v182, v218\n\t" \
  "v_sub_f32 v183, v183, v218\n\t" \
  "v_exp_f32 v208, v168\n\t" \
  "v_exp_f32 v209, v169\n\t" \
  "v_exp_f32 v210, v170\n\t" \
  "v_exp_f32 v211, v171\n\t" \
  "v_exp_f32 v212, v172\n\t" \
  "v_exp_f32 v213, v173\n\t" \
  "v_exp_f32 v214, v174\n\t" \
  "v_exp_f32 v215, v175\n\t" \
  "v_exp_f32 v219, -v218\n\t" \
  "s_nop 0\n\t" \
  "v_cvt_pk_bf16_f32 v192, v208, v209\n\t" \
  "v_cvt_pk_bf16_f32 v193, v210, v211\n\t" \
  "v_cvt_pk_bf16_f32 v194, v212, v213\n\t" \
  "v_cvt_pk_bf16_f32 v195, v214, v215\n\t" \
  "v_mul_f32 v128, v128, v219\n\t" \
  "v_mul_f32 v129, v129, v219\n\t" \
  "v_mul_f32 v130, v130, v219\n\t" \
  "v_mul_f32 v131, v131, v219\n\t" \
  "v_mul_f32 v64, v64, v219\n\t" \
  "v_mul_f32 v65, v65, v219\n\t" \
  "v_mul_f32 v66, v66, v219\n\t" \
  "v_mul_f32 v67, v67, v219\n\t" \
  "v_mul_f32 v72, v72, v219\n\t" \
  "v_mul_f32 v73, v73, v219\n\t" \
  "v_mul_f32 v74, v74, v219\n\t" \
  "v_mul_f32 v75, v75, v219\n\t" \
  "v_mul_f32 v80, v80, v219\n\t" \
  "v_mul_f32 v81, v81, v219\n\t" \
  "v_mul_f32 v82, v82, v219\n\t" \
  "v_mul_f32 v83, v83, v219\n\t" \
  "v_mul_f32 v88, v88, v219\n\t" \
  "v_mul_f32 v89, v89, v219\n\t" \
  "v_mul_f32 v90, v90, v219\n\t" \
  "v_mul_f32 v91, v91, v219\n\t" \
  "v_mul_f32 v96, v96, v219\n\t" \
  "v_mul_f32 v97, v97, v219\n\t" \
  "v_mul_f32 v98, v98, v219\n\t" \
  "v_mul_f32 v99, v99, v219\n\t" \
  "v_mul_f32 v104, v104, v219\n\t" \
  "v_mul_f32 v105, v105, v219\n\t" \
  "v_mul_f32 v106, v106, v219\n\t" \
  "v_mul_f32 v107, v107, v219\n\t" \
  "v_mul_f32 v112, v112, v219\n\t" \
  "v_mul_f32 v113, v113, v219\n\t" \
  "v_mul_f32 v114, v114, v219\n\t" \
  "v_mul_f32 v115, v115, v219\n\t" \
  "v_mul_f32 v120, v120, v219\n\t" \
  "v_mul_f32 v121, v121, v219\n\t" \
  "v_mul_f32 v122, v122, v219\n\t" \
  "v_mul_f32 v123, v123, v219\n\t" \
  "v_max3_f32 v216, v184, v185, v186\n\t" \
  "v_max3_f32 v217, v187, v188, v189\n\t" \
  "v_max3_f32 v216, v216, v190, v191\n\t" \
  "v_max_f32 v216, v216, v217\n\t" \
  "v_mov_b32 v217, v216\n\t" \
  "s_nop 1\n\t" \
  "v_permlane16_swap_b32 v216, v217\n\t" \
  "s_nop 1\n\t" \
  "v_max_f32 v216, v216, v217\n\t" \
  "v_mov_b32 v217, v216\n\t" \
  "s_nop 1\n\t" \
  "v_permlane32_swap_b32 v216, v217\n\t" \
  "s_nop 1\n\t" \
  "v_max_f32 v216, v216, v217\n\t" \
  "v_add_f32 v218, 0x40c00000, v216\n\t" \
  "v_max_f32 v218, 0, v218\n\t" \
  "v_sub_f32 v184, v184, v218\n\t" \
  "v_sub_f32 v185, v185, v218\n\t" \
  "v_sub_f32 v186, v186, v218\n\t" \
  "v_sub_f32 v187, v187, v218\n\t" \
  "v_sub_f32 v188, v188, v218\n\t" \
  "v_sub_f32 v189, v189, v218\n\t" \
  "v_sub_f32 v190, v190, v218\n\t" \
  "v_sub_f32 v191, v191, v218\n\t" \
  "v_sub_f32 v204, v204, v218\n\t" \
  "v_sub_f32 v205, v205, v218\n\t" \
  "v_sub_f32 v206, v206, v218\n\t" \
  "v_sub_f32 v207, v207, v218\n\t" \
  "v_exp_f32 v208, v184\n\t" \
  "v_exp_f32 v209, v185\n\t" \
  "v_exp_f32 v210, v186\n\t" \
  "v_exp_f32 v211, v187\n\t" \
  "v_exp_f32 v212, v188\n\t" \
  "v_exp_f32 v213, v189\n\t" \
  "v_exp_f32 v214, v190\n\t" \
  "v_exp_f32 v215, v191\n\t" \
  "v_exp_f32 v219, -v218\n\t" \
  "s_nop 0\n\t" \
  "v_cvt_pk_bf16_f32 v196, v208, v209\n\t" \
  "v_cvt_pk_bf16_f32 v197, v210, v211\n\t" \
  "v_cvt_pk_bf16_f32 v198, v212, v213\n\t" \
  "v_cvt_pk_bf16_f32 v199, v214, v215\n\t" \
  "v_mul_f32 v132, v132, v219\n\t" \
  "v_mul_f32 v133, v133, v219\n\t" \
  "v_mul_f32 v134, v134, v219\n\t" \
  "v_mul_f32 v135, v135, v219\n\t" \
  "v_mul_f32 v68, v68, v219\n\t" \
  "v_mul_f32 v69, v69, v219\n\t" \
  "v_mul_f32 v70, v70, v219\n\t" \
  "v_mul_f32 v71, v71, v219\n\t" \
  "v_mul_f32 v76, v76, v219\n\t" \
  "v_mul_f32 v77, v77, v219\n\t" \
  "v_mul_f32 v78, v78, v219\n\t" \
  "v_mul_f32 v79, v79, v219\n\t" \
  "v_mul_f32 v84, v84, v219\n\t" \
  "v_mul_f32 v85, v85, v219\n\t" \
  "v_mul_f32 v86, v86, v219\n\t" \
  "v_mul_f32 v87, v87, v219\n\t" \
  "v_mul_f32 v92, v92, v219\n\t" \
  "v_mul_f32 v93, v93, v219\n\t" \
  "v_mul_f32 v94, v94, v219\n\t" \
  "v_mul_f32 v95, v95, v219\n\t" \
  "v_mul_f32 v100, v100, v219\n\t" \
  "v_mul_f32 v101, v101, v219\n\t" \
  "v_mul_f32 v102, v102, v219\n\t" \
  "v_mul_f32 v103, v103, v219\n\t" \
  "v_mul_f32 v108, v108, v219\n\t" \
  "v_mul_f32 v109, v109, v219\n\t" \
  "v_mul_f32 v110, v110, v219\n\t" \
  "v_mul_f32 v111, v111, v219\n\t" \
  "v_mul_f32 v116, v116, v219\n\t" \
  "v_mul_f32 v117, v117, v219\n\t" \
  "v_mul_f32 v118, v118, v219\n\t" \
  "v_mul_f32 v119, v119, v219\n\t" \
  "v_mul_f32 v124, v124, v219\n\t" \
  "v_mul_f32 v125, v125, v219\n\t" \
  "v_mul_f32 v126, v126, v219\n\t" \
  "v_mul_f32 v127, v127, v219\n\t" \
  "s_nop 3\n\t" \
  "s_branch .Lback0_%=\n\t" \
  ".Lrare1_%=:\n\t" \
  "s_nop 7\n\t" \
  "s_nop 7\n\t" \
  "v_max3_f32 v216, v176, v177, v178\n\t" \
  "v_max3_f32 v217, v179, v180, v181\n\t" \
  "v_max3_f32 v216, v216, v182, v183\n\t" \
  "v_max_f32 v216, v216, v217\n\t" \
  "v_mov_b32 v217, v216\n\t" \
  "s_nop 1\n\t" \
  "v_permlane16_swap_b32 v216, v217\n\t" \
  "s_nop 1\n\t" \
  "v_max_f32 v216, v216, v217\n\t" \
  "v_mov_b32 v217, v216\n\t" \
  "s_nop 1\n\t" \
  "v_permlane32_swap_b32 v216, v217\n\t" \
  "s_nop 1\n\t" \
  "v_max_f32 v216, v216, v217\n\t" \
  "v_add_f32 v218, 0x40c00000, v216\n\t" \
  "v_max_f32 v218, 0, v218\n\t" \
  "v_sub_f32 v176, v176, v218\n\t" \
  "v_sub_f32 v177, v177, v218\n\t" \
  "v_sub_f32 v178, v178, v218\n\t" \
  "v_sub_f32 v179, v179, v218\n\t" \
  "v_sub_f32 v180, v180, v218\n\t" \
  "v_sub_f32 v181, v181, v218\n\t" \
  "v_sub_f32 v182, v182, v218\n\t" \
  "v_sub_f32 v183, v183, v218\n\t" \
  "v_sub_f32 v200, v200, v218\n\t" \
  "v_sub_f32 v201, v201, v218\n\t" \
  "v_sub_f32 v202, v202, v218\n\t" \
  "v_sub_f32 v203, v203, v218\n\t" \
  "v_sub_f32 v168, v168, v218\n\t" \
  "v_sub_f32 v169, v169, v218\n\t" \
  "v_sub_f32 v170, v170, v218\n\t" \
  "v_sub_f32 v171, v171, v218\n\t" \
  "v_sub_f32 v172, v172, v218\n\t" \
  "v_sub_f32 v173, v173, v218\n\t" \
  "v_sub_f32 v174, v174, v218\n\t" \
  "v_sub_f32 v175, v175, v218\n\t" \
  "v_exp_f32 v208, v176\n\t" \
  "v_exp_f32 v209, v177\n\t" \
  "v_exp_f32 v210, v178\n\t" \
  "v_exp_f32 v211, v179\n\t" \
  "v_exp_f32 v212, v180\n\t" \
  "v_exp_f32 v213, v181\n\t" \
  "v_exp_f32 v214, v182\n\t" \
  "v_exp_f32 v215, v183\n\t" \
  "v_exp_f32 v219, -v218\n\t" \
  "s_nop 0\n\t" \
  "v_cvt_pk_bf16_f32 v192, v208, v209\n\t" \
  "v_cvt_pk_bf16_f32 v193, v210, v211\n\t" \
  "v_cvt_pk_bf16_f32 v194, v212, v213\n\t" \
  "v_cvt_pk_bf16_f32 v195, v214, v215\n\t" \
  "v_mul_f32 v128, v128, v219\n\t" \
  "v_mul_f32 v129, v129, v219\n\t" \
  "v_mul_f32 v130, v130, v219\n\t" \
  "v_mul_f32 v131, v131, v219\n\t" \
  "v_mul_f32 v64, v64, v219\n\t" \
  "v_mul_f32 v65, v65, v219\n\t" \
  "v_mul_f32 v66, v66, v219\n\t" \
  "v_mul_f32 v67, v67, v219\n\t" \
  "v_mul_f32 v72, v72, v219\n\t" \
  "v_mul_f32 v73, v73, v219\n\t" \
  "v_mul_f32 v74, v74, v219\n\t" \
  "v_mul_f32 v75, v75, v219\n\t" \
  "v_mul_f32 v80, v80, v219\n\t" \
  "v_mul_f32 v81, v81, v219\n\t" \
  "v_mul_f32 v82, v82, v219\n\t" \
  "v_mul_f32 v83, v83, v219\n\t" \
  "v_mul_f32 v88, v88, v219\n\t" \
  "v_mul_f32 v89, v89, v219\n\t" \
  "v_mul_f32 v90, v90, v219\n\t" \
  "v_mul_f32 v91, v91, v219\n\t" \
  "v_mul_f32 v96, v96, v219\n\t" \
  "v_mul_f32 v97, v97, v219\n\t" \
  "v_mul_f32 v98, v98, v219\n\t" \
  "v_mul_f32 v99, v99, v219\n\t" \
  "v_mul_f32 v104, v104, v219\n\t" \
  "v_mul_f32 v105, v105, v219\n\t" \
  "v_mul_f32 v106, v106, v219\n\t" \
  "v_mul_f32 v107, v107, v219\n\t" \
  "v_mul_f32 v112, v112, v219\n\t" \
  "v_mul_f32 v113, v113, v219\n\t" \
  "v_mul_f32 v114, v114, v219\n\t" \
  "v_mul_f32 v115, v115, v219\n\t" \
  "v_mul_f32 v120, v120, v219\n\t" \
  "v_mul_f32 v121, v121, v219\n\t" \
  "v_mul_f32 v122, v122, v219\n\t" \
  "v_mul_f32 v123, v123, v219\n\t" \
  "v_max3_f32 v216, v184, v185, v186\n\t" \
  "v_max3_f32 v217, v187, v188, v189\n\t" \
  "v_max3_f32 v216, v216, v190, v191\n\t" \
  "v_max_f32 v216, v216, v217\n\t" \
  "v_mov_b32 v217, v216\n\t" \
  "s_nop 1\n\t" \
  "v_permlane16_swap_b32 v216, v217\n\t" \
  "s_nop 1\n\t" \
  "v_max_f32 v216, v216, v217\n\t" \
  "v_mov_b32 v217, v216\n\t" \
  "s_nop 1\n\t" \
  "v_permlane32_swap_b32 v216, v217\n\t" \
  "s_nop 1\n\t" \
  "v_max_f32 v216, v216, v217\n\t" \
  "v_add_f32 v218, 0x40c00000, v216\n\t" \
  "v_max_f32 v218, 0, v218\n\t" \
  "v_sub_f32 v184, v184, v218\n\t" \
  "v_sub_f32 v185, v185, v218\n\t" \
  "v_sub_f32 v186, v186, v218\n\t" \
  "v_sub_f32 v187, v187, v218\n\t" \
  "v_sub_f32 v188, v188, v218\n\t" \
  "v_sub_f32 v189, v189, v218\n\t" \
  "v_sub_f32 v190, v190, v218\n\t" \
  "v_sub_f32 v191, v191, v218\n\t" \
  "v_sub_f32 v204, v204, v218\n\t" \
  "v_sub_f32 v205, v205, v218\n\t" \
  "v_sub_f32 v206, v206, v218\n\t" \
  "v_sub_f32 v207, v207, v218\n\t" \
  "v_exp_f32 v208, v184\n\t" \
  "v_exp_f32 v209, v185\n\t" \
  "v_exp_f32 v210, v186\n\t" \
  "v_exp_f32 v211, v187\n\t" \
  "v_exp_f32 v212, v188\n\t" \
  "v_exp_f32 v213, v189\n\t" \
  "v_exp_f32 v214, v190\n\t" \
  "v_exp_f32 v215, v191\n\t" \
  "v_exp_f32 v219, -v218\n\t" \
  "s_nop 0\n\t" \
  "v_cvt_pk_bf16_f32 v196, v208, v209\n\t" \
  "v_cvt_pk_bf16_f32 v197, v210, v211\n\t" \
  "v_cvt_pk_bf16_f32 v198, v212, v213\n\t" \
  "v_cvt_pk_bf16_f32 v199, v214, v215\n\t" \
  "v_mul_f32 v132, v132, v219\n\t" \
  "v_mul_f32 v133, v133, v219\n\t" \
  "v_mul_f32 v134, v134, v219\n\t" \
  "v_mul_f32 v135, v135, v219\n\t" \
  "v_mul_f32 v68, v68, v219\n\t" \
  "v_mul_f32 v69, v69, v219\n\t" \
  "v_mul_f32 v70, v70, v219\n\t" \
  "v_mul_f32 v71, v71, v219\n\t" \
  "v_mul_f32 v76, v76, v219\n\t" \
  "v_mul_f32 v77, v77, v219\n\t" \
  "v_mul_f32 v78, v78, v219\n\t" \
  "v_mul_f32 v79, v79, v219\n\t" \
  "v_mul_f32 v84, v84, v219\n\t" \
  "v_mul_f32 v85, v85, v219\n\t" \
  "v_mul_f32 v86, v86, v219\n\t" \
  "v_mul_f32 v87, v87, v219\n\t" \
  "v_mul_f32 v92, v92, v219\n\t" \
  "v_mul_f32 v93, v93, v219\n\t" \
  "v_mul_f32 v94, v94, v219\n\t" \
  "v_mul_f32 v95, v95, v219\n\t" \
  "v_mul_f32 v100, v100, v219\n\t" \
  "v_mul_f32 v101, v101, v219\n\t" \
  "v_mul_f32 v102, v102, v219\n\t" \
  "v_mul_f32 v103, v103, v219\n\t" \
  "v_mul_f32 v108, v108, v219\n\t" \
  "v_mul_f32 v109, v109, v219\n\t" \
  "v_mul_f32 v110, v110, v219\n\t" \
  "v_mul_f32 v111, v111, v219\n\t" \
  "v_mul_f32 v116, v116, v219\n\t" \
  "v_mul_f32 v117, v117, v219\n\t" \
  "v_mul_f32 v118, v118, v219\n\t" \
  "v_mul_f32 v119, v119, v219\n\t" \
  "v_mul_f32 v124, v124, v219\n\t" \
  "v_mul_f32 v125, v125, v219\n\t" \
  "v_mul_f32 v126, v126, v219\n\t" \
  "v_mul_f32 v127, v127, v219\n\t" \
  "s_nop 3\n\t" \
  "s_branch .Lback1_%=\n\t" \
  ".Lrare2_%=:\n\t" \
  "s_nop 7\n\t" \
  "s_nop 7\n\t" \
  "v_max3_f32 v216, v168, v169, v170\n\t" \
  "v_max3_f32 v217, v171, v172, v173\n\t" \
  "v_max3_f32 v216, v216, v174, v175\n\t" \
  "v_max_f32 v216, v216, v217\n\t" \
  "v_mov_b32 v217, v216\n\t" \
  "s_nop 1\n\t" \
  "v_permlane16_swap_b32 v216, v217\n\t" \
  "s_nop 1\n\t" \
  "v_max_f32 v216, v216, v217\n\t" \
  "v_mov_b32 v217, v216\n\t" \
  "s_nop 1\n\t" \
  "v_permlane32_swap_b32 v216, v217\n\t" \
  "s_nop 1\n\t" \
  "v_max_f32 v216, v216, v217\n\t" \
  "v_add_f32 v218, 0x40c00000, v216\n\t" \
  "v_max_f32 v218, 0, v218\n\t" \
  "v_sub_f32 v168, v168, v218\n\t" \
  "v_sub_f32 v169, v169, v218\n\t" \
  "v_sub_f32 v170, v170, v218\n\t" \
  "v_sub_f32 v171, v171, v218\n\t" \
  "v_sub_f32 v172, v172, v218\n\t" \
  "v_sub_f32 v173, v173, v218\n\t" \
  "v_sub_f32 v174, v174, v218\n\t" \
  "v_sub_f32 v175, v175, v218\n\t" \
  "v_sub_f32 v200, v200, v218\n\t" \
  "v_sub_f32 v201, v201, v218\n\t" \
  "v_sub_f32 v202, v202, v218\n\t" \
  "v_sub_f32 v203, v203, v218\n\t" \
  "v_sub_f32 v176, v176, v218\n\t" \
  "v_sub_f32 v177, v177, v218\n\t" \
  "v_sub_f32 v178, v178, v218\n\t" \
  "v_sub_f32 v179, v179, v218\n\t" \
  "v_sub_f32 v180, v180, v218\n\t" \
  "v_sub_f32 v181, v181, v218\n\t" \
  "v_sub_f32 v182, v182, v218\n\t" \
  "v_sub_f32 v183, v183, v218\n\t" \
  "v_exp_f32 v208, v168\n\t" \
  "v_exp_f32 v209, v169\n\t" \
  "v_exp_f32 v210, v170\n\t" \
  "v_exp_f32 v211, v171\n\t" \
  "v_exp_f32 v212, v172\n\t" \
  "v_exp_f32 v213, v173\n\t" \
  "v_exp_f32 v214, v174\n\t" \
  "v_exp_f32 v215, v175\n\t" \
  "v_exp_f32 v219, -v218\n\t" \
  "s_nop 0\n\t" \
  "v_cvt_pk_bf16_f32 v192, v208, v209\n\t" \
  "v_cvt_pk_bf16_f32 v193, v210, v211\n\t" \
  "v_cvt_pk_bf16_f32 v194, v212, v213\n\t" \
  "v_cvt_pk_bf16_f32 v195, v214, v215\n\t" \
  "v_mul_f32 v128, v128, v219\n\t" \
  "v_mul_f32 v129, v129, v219\n\t" \
  "v_mul_f32 v130, v130, v219\n\t" \
  "v_mul_f32 v131, v131, v219\n\t" \
  "v_mul_f32 v64, v64, v219\n\t" \
  "v_mul_f32 v65, v65, v219\n\t" \
  "v_mul_f32 v66, v66, v219\n\t" \
  "v_mul_f32 v67, v67, v219\n\t" \
  "v_mul_f32 v72, v72, v219\n\t" \
  "v_mul_f32 v73, v73, v219\n\t" \
  "v_mul_f32 v74, v74, v219\n\t" \
  "v_mul_f32 v75, v75, v219\n\t" \
  "v_mul_f32 v80, v80, v219\n\t" \
  "v_mul_f32 v81, v81, v219\n\t" \
  "v_mul_f32 v82, v82, v219\n\t" \
  "v_mul_f32 v83, v83, v219\n\t" \
  "v_mul_f32 v88, v88, v219\n\t" \
  "v_mul_f32 v89, v89, v219\n\t" \
  "v_mul_f32 v90, v90, v219\n\t" \
  "v_mul_f32 v91, v91, v219\n\t" \
  "v_mul_f32 v96, v96, v219\n\t" \
  "v_mul_f32 v97, v97, v219\n\t" \
  "v_mul_f32 v98, v98, v219\n\t" \
  "v_mul_f32 v99, v99, v219\n\t" \
  "v_mul_f32 v104, v104, v219\n\t" \
  "v_mul_f32 v105, v105, v219\n\t" \
  "v_mul_f32 v106, v106, v219\n\t" \
  "v_mul_f32 v107, v107, v219\n\t" \
  "v_mul_f32 v112, v112, v219\n\t" \
  "v_mul_f32 v113, v113, v219\n\t" \
  "v_mul_f32 v114, v114, v219\n\t" \
  "v_mul_f32 v115, v115, v219\n\t" \
  "v_mul_f32 v120, v120, v219\n\t" \
  "v_mul_f32 v121, v121, v219\n\t" \
  "v_mul_f32 v122, v122, v219\n\t" \
  "v_mul_f32 v123, v123, v219\n\t" \
  "v_max3_f32 v216, v184, v185, v186\n\t" \
  "v_max3_f32 v217, v187, v188, v189\n\t" \
  "v_max3_f32 v216, v216, v190, v191\n\t" \
  "v_max_f32 v216, v216, v217\n\t" \
  "v_mov_b32 v217, v216\n\t" \
  "s_nop 1\n\t" \
  "v_permlane16_swap_b32 v216, v217\n\t" \
  "s_nop 1\n\t" \
  "v_max_f32 v216, v216, v217\n\t" \
  "v_mov_b32 v217, v216\n\t" \
  "s_nop 1\n\t" \
  "v_permlane32_swap_b32 v216, v217\n\t" \
  "s_nop 1\n\t" \
  "v_max_f32 v216, v216, v217\n\t" \
  "v_add_f32 v218, 0x40c00000, v216\n\t" \
  "v_max_f32 v218, 0, v218\n\t" \
  "v_sub_f32 v184, v184, v218\n\t" \
  "v_sub_f32 v185, v185, v218\n\t" \
  "v_sub_f32 v186, v186, v218\n\t" \
  "v_sub_f32 v187, v187, v218\n\t" \
  "v_sub_f32 v188, v188, v218\n\t" \
  "v_sub_f32 v189, v189, v218\n\t" \
  "v_sub_f32 v190, v190, v218\n\t" \
  "v_sub_f32 v191, v191, v218\n\t" \
  "v_sub_f32 v204, v204, v218\n\t" \
  "v_sub_f32 v205, v205, v218\n\t" \
  "v_sub_f32 v206, v206, v218\n\t" \
  "v_sub_f32 v207, v207, v218\n\t" \
  "v_exp_f32 v208, v184\n\t" \
  "v_exp_f32 v209, v185\n\t" \
  "v_exp_f32 v210, v186\n\t" \
  "v_exp_f32 v211, v187\n\t" \
  "v_exp_f32 v212, v188\n\t" \
  "v_exp_f32 v213, v189\n\t" \
  "v_exp_f32 v214, v190\n\t" \
  "v_exp_f32 v215, v191\n\t" \
  "v_exp_f32 v219, -v218\n\t" \
  "s_nop 0\n\t" \
  "v_cvt_pk_bf16_f32 v196, v208, v209\n\t" \
  "v_cvt_pk_bf16_f32 v197, v210, v211\n\t" \
  "v_cvt_pk_bf16_f32 v198, v212, v213\n\t" \
  "v_cvt_pk_bf16_f32 v199, v214, v215\n\t" \
  "v_mul_f32 v132, v132, v219\n\t" \
  "v_mul_f32 v133, v133, v219\n\t" \
  "v_mul_f32 v134, v134, v219\n\t" \
  "v_mul_f32 v135, v135, v219\n\t" \
  "v_mul_f32 v68, v68, v219\n\t" \
  "v_mul_f32 v69, v69, v219\n\t" \
  "v_mul_f32 v70, v70, v219\n\t" \
  "v_mul_f32 v71, v71, v219\n\t" \
  "v_mul_f32 v76, v76, v219\n\t" \
  "v_mul_f32 v77, v77, v219\n\t" \
  "v_mul_f32 v78, v78, v219\n\t" \
  "v_mul_f32 v79, v79, v219\n\t" \
  "v_mul_f32 v84, v84, v219\n\t" \
  "v_mul_f32 v85, v85, v219\n\t" \
  "v_mul_f32 v86, v86, v219\n\t" \
  "v_mul_f32 v87, v87, v219\n\t" \
  "v_mul_f32 v92, v92, v219\n\t" \
  "v_mul_f32 v93, v93, v219\n\t" \
  "v_mul_f32 v94, v94, v219\n\t" \
  "v_mul_f32 v95, v95, v219\n\t" \
  "v_mul_f32 v100, v100, v219\n\t" \
  "v_mul_f32 v101, v101, v219\n\t" \
  "v_mul_f32 v102, v102, v219\n\t" \
  "v_mul_f32 v103, v103, v219\n\t" \
  "v_mul_f32 v108, v108, v219\n\t" \
  "v_mul_f32 v109, v109, v219\n\t" \
  "v_mul_f32 v110, v110, v219\n\t" \
  "v_mul_f32 v111, v111, v219\n\t" \
  "v_mul_f32 v116, v116, v219\n\t" \
  "v_mul_f32 v117, v117, v219\n\t" \
  "v_mul_f32 v118, v118, v219\n\t" \
  "v_mul_f32 v119, v119, v219\n\t" \
  "v_mul_f32 v124, v124, v219\n\t" \
  "v_mul_f32 v125, v125, v219\n\t" \
  "v_mul_f32 v126, v126, v219\n\t" \
  "v_mul_f32 v127, v127, v219\n\t" \
  "s_nop 3\n\t" \
  "s_branch .Lback2_%=\n\t" \
  ".Lrare3_%=:\n\t" \
  "s_nop 7\n\t" \
  "s_nop 7\n\t" \
  "v_max3_f32 v216, v176, v177, v178\n\t" \
  "v_max3_f32 v217, v179, v180, v181\n\t" \
  "v_max3_f32 v216, v216, v182, v183\n\t" \
  "v_max_f32 v216, v216, v217\n\t" \
  "v_mov_b32 v217, v216\n\t" \
  "s_nop 1\n\t" \
  "v_permlane16_swap_b32 v216, v217\n\t" \
  "s_nop 1\n\t" \
  "v_max_f32 v216, v216, v217\n\t" \
  "v_mov_b32 v217, v216\n\t" \
  "s_nop 1\n\t" \
  "v_permlane32_swap_b32 v216, v217\n\t" \
  "s_nop 1\n\t" \
  "v_max_f32 v216, v216, v217\n\t" \
  "v_add_f32 v218, 0x40c00000, v216\n\t" \
  "v_max_f32 v218, 0, v218\n\t" \
  "v_sub_f32 v176, v176, v218\n\t" \
  "v_sub_f32 v177, v177, v218\n\t" \
  "v_sub_f32 v178, v178, v218\n\t" \
  "v_sub_f32 v179, v179, v218\n\t" \
  "v_sub_f32 v180, v180, v218\n\t" \
  "v_sub_f32 v181, v181, v218\n\t" \
  "v_sub_f32 v182, v182, v218\n\t" \
  "v_sub_f32 v183, v183, v218\n\t" \
  "v_sub_f32 v200, v200, v218\n\t" \
  "v_sub_f32 v201, v201, v218\n\t" \
  "v_sub_f32 v202, v202, v218\n\t" \
  "v_sub_f32 v203, v203, v218\n\t" \
  "v_sub_f32 v168, v168, v218\n\t" \
  "v_sub_f32 v169, v169, v218\n\t" \
  "v_sub_f32 v170, v170, v218\n\t" \
  "v_sub_f32 v171, v171, v218\n\t" \
  "v_sub_f32 v172, v172, v218\n\t" \
  "v_sub_f32 v173, v173, v218\n\t" \
  "v_sub_f32 v174, v174, v218\n\t" \
  "v_sub_f32 v175, v175, v218\n\t" \
  "v_exp_f32 v208, v176\n\t" \
  "v_exp_f32 v209, v177\n\t" \
  "v_exp_f32 v210, v178\n\t" \
  "v_exp_f32 v211, v179\n\t" \
  "v_exp_f32 v212, v180\n\t" \
  "v_exp_f32 v213, v181\n\t" \
  "v_exp_f32 v214, v182\n\t" \
  "v_exp_f32 v215, v183\n\t" \
  "v_exp_f32 v219, -v218\n\t" \
  "s_nop 0\n\t" \
  "v_cvt_pk_bf16_f32 v192, v208, v209\n\t" \
  "v_cvt_pk_bf16_f32 v193, v210, v211\n\t" \
  "v_cvt_pk_bf16_f32 v194, v212, v213\n\t" \
  "v_cvt_pk_bf16_f32 v195, v214, v215\n\t" \
  "v_mul_f32 v128, v128, v219\n\t" \
  "v_mul_f32 v129, v129, v219\n\t" \
  "v_mul_f32 v130, v130, v219\n\t" \
  "v_mul_f32 v131, v131, v219\n\t" \
  "v_mul_f32 v64, v64, v219\n\t" \
  "v_mul_f32 v65, v65, v219\n\t" \
  "v_mul_f32 v66, v66, v219\n\t" \
  "v_mul_f32 v67, v67, v219\n\t" \
  "v_mul_f32 v72, v72, v219\n\t" \
  "v_mul_f32 v73, v73, v219\n\t" \
  "v_mul_f32 v74, v74, v219\n\t" \
  "v_mul_f32 v75, v75, v219\n\t" \
  "v_mul_f32 v80, v80, v219\n\t" \
  "v_mul_f32 v81, v81, v219\n\t" \
  "v_mul_f32 v82, v82, v219\n\t" \
  "v_mul_f32 v83, v83, v219\n\t" \
  "v_mul_f32 v88, v88, v219\n\t" \
  "v_mul_f32 v89, v89, v219\n\t" \
  "v_mul_f32 v90, v90, v219\n\t" \
  "v_mul_f32 v91, v91, v219\n\t" \
  "v_mul_f32 v96, v96, v219\n\t" \
  "v_mul_f32 v97, v97, v219\n\t" \
  "v_mul_f32 v98, v98, v219\n\t" \
  "v_mul_f32 v99, v99, v219\n\t" \
  "v_mul_f32 v104, v104, v219\n\t" \
  "v_mul_f32 v105, v105, v219\n\t" \
  "v_mul_f32 v106, v106, v219\n\t" \
  "v_mul_f32 v107, v107, v219\n\t" \
  "v_mul_f32 v112, v112, v219\n\t" \
  "v_mul_f32 v113, v113, v219\n\t" \
  "v_mul_f32 v114, v114, v219\n\t" \
  "v_mul_f32 v115, v115, v219\n\t" \
  "v_mul_f32 v120, v120, v219\n\t" \
  "v_mul_f32 v121, v121, v219\n\t" \
  "v_mul_f32 v122, v122, v219\n\t" \
  "v_mul_f32 v123, v123, v219\n\t" \
  "v_max3_f32 v216, v184, v185, v186\n\t" \
  "v_max3_f32 v217, v187, v188, v189\n\t" \
  "v_max3_f32 v216, v216, v190, v191\n\t" \
  "v_max_f32 v216, v216, v217\n\t" \
  "v_mov_b32 v217, v216\n\t" \
  "s_nop 1\n\t" \
  "v_permlane16_swap_b32 v216, v217\n\t" \
  "s_nop 1\n\t" \
  "v_max_f32 v216, v216, v217\n\t" \
  "v_mov_b32 v217, v216\n\t" \
  "s_nop 1\n\t" \
  "v_permlane32_swap_b32 v216, v217\n\t" \
  "s_nop 1\n\t" \
  "v_max_f32 v216, v216, v217\n\t" \
  "v_add_f32 v218, 0x40c00000, v216\n\t" \
  "v_max_f32 v218, 0, v218\n\t" \
  "v_sub_f32 v184, v184, v218\n\t" \
  "v_sub_f32 v185, v185, v218\n\t" \
  "v_sub_f32 v186, v186, v218\n\t" \
  "v_sub_f32 v187, v187, v218\n\t" \
  "v_sub_f32 v188, v188, v218\n\t" \
  "v_sub_f32 v189, v189, v218\n\t" \
  "v_sub_f32 v190, v190, v218\n\t" \
  "v_sub_f32 v191, v191, v218\n\t" \
  "v_sub_f32 v204, v204, v218\n\t" \
  "v_sub_f32 v205, v205, v218\n\t" \
  "v_sub_f32 v206, v206, v218\n\t" \
  "v_sub_f32 v207, v207, v218\n\t" \
  "v_exp_f32 v208, v184\n\t" \
  "v_exp_f32 v209, v185\n\t" \
  "v_exp_f32 v210, v186\n\t" \
  "v_exp_f32 v211, v187\n\t" \
  "v_exp_f32 v212, v188\n\t" \
  "v_exp_f32 v213, v189\n\t" \
  "v_exp_f32 v214, v190\n\t" \
  "v_exp_f32 v215, v191\n\t" \
  "v_exp_f32 v219, -v218\n\t" \
  "s_nop 0\n\t" \
  "v_cvt_pk_bf16_f32 v196, v208, v209\n\t" \
  "v_cvt_pk_bf16_f32 v197, v210, v211\n\t" \
  "v_cvt_pk_bf16_f32 v198, v212, v213\n\t" \
  "v_cvt_pk_bf16_f32 v199, v214, v215\n\t" \
  "v_mul_f32 v132, v132, v219\n\t" \
  "v_mul_f32 v133, v133, v219\n\t" \
  "v_mul_f32 v134, v134, v219\n\t" \
  "v_mul_f32 v135, v135, v219\n\t" \
  "v_mul_f32 v68, v68, v219\n\t" \
  "v_mul_f32 v69, v69, v219\n\t" \
  "v_mul_f32 v70, v70, v219\n\t" \
  "v_mul_f32 v71, v71, v219\n\t" \
  "v_mul_f32 v76, v76, v219\n\t" \
  "v_mul_f32 v77, v77, v219\n\t" \
  "v_mul_f32 v78, v78, v219\n\t" \
  "v_mul_f32 v79, v79, v219\n\t" \
  "v_mul_f32 v84, v84, v219\n\t" \
  "v_mul_f32 v85, v85, v219\n\t" \
  "v_mul_f32 v86, v86, v219\n\t" \
  "v_mul_f32 v87, v87, v219\n\t" \
  "v_mul_f32 v92, v92, v219\n\t" \
  "v_mul_f32 v93, v93, v219\n\t" \
  "v_mul_f32 v94, v94, v219\n\t" \
  "v_mul_f32 v95, v95, v219\n\t" \
  "v_mul_f32 v100, v100, v219\n\t" \
  "v_mul_f32 v101, v101, v219\n\t" \
  "v_mul_f32 v102, v102, v219\n\t" \
  "v_mul_f32 v103, v103, v219\n\t" \
  "v_mul_f32 v108, v108, v219\n\t" \
  "v_mul_f32 v109, v109, v219\n\t" \
  "v_mul_f32 v110, v110, v219\n\t" \
  "v_mul_f32 v111, v111, v219\n\t" \
  "v_mul_f32 v116, v116, v219\n\t" \
  "v_mul_f32 v117, v117, v219\n\t" \
  "v_mul_f32 v118, v118, v219\n\t" \
  "v_mul_f32 v119, v119, v219\n\t" \
  "v_mul_f32 v124, v124, v219\n\t" \
  "v_mul_f32 v125, v125, v219\n\t" \
  "v_mul_f32 v126, v126, v219\n\t" \
  "v_mul_f32 v127, v127, v219\n\t" \
  "s_nop 3\n\t" \
  "s_branch .Lback3_%=\n\t" \
  ".Lrare4_%=:\n\t" \
  "s_nop 7\n\t" \
  "s_nop 7\n\t" \
  "v_max3_f32 v216, v168, v169, v170\n\t" \
  "v_max3_f32 v217, v171, v172, v173\n\t" \
  "v_max3_f32 v216, v216, v174, v175\n\t" \
  "v_max_f32 v216, v216, v217\n\t" \
  "v_mov_b32 v217, v216\n\t" \
  "s_nop 1\n\t" \
  "v_permlane16_swap_b32 v216, v217\n\t" \
  "s_nop 1\n\t" \
  "v_max_f32 v216, v216, v217\n\t" \
  "v_mov_b32 v217, v216\n\t" \
  "s_nop 1\n\t" \
  "v_permlane32_swap_b32 v216, v217\n\t" \
  "s_nop 1\n\t" \
  "v_max_f32 v216, v216, v217\n\t" \
  "v_add_f32 v218, 0x40c00000, v216\n\t" \
  "v_max_f32 v218, 0, v218\n\t" \
  "v_sub_f32 v168, v168, v218\n\t" \
  "v_sub_f32 v169, v169, v218\n\t" \
  "v_sub_f32 v170, v170, v218\n\t" \
  "v_sub_f32 v171, v171, v218\n\t" \
  "v_sub_f32 v172, v172, v218\n\t" \
  "v_sub_f32 v173, v173, v218\n\t" \
  "v_sub_f32 v174, v174, v218\n\t" \
  "v_sub_f32 v175, v175, v218\n\t" \
  "v_sub_f32 v200, v200, v218\n\t" \
  "v_sub_f32 v201, v201, v218\n\t" \
  "v_sub_f32 v202, v202, v218\n\t" \
  "v_sub_f32 v203, v203, v218\n\t" \
  "v_sub_f32 v176, v176, v218\n\t" \
  "v_sub_f32 v177, v177, v218\n\t" \
  "v_sub_f32 v178, v178, v218\n\t" \
  "v_sub_f32 v179, v179, v218\n\t" \
  "v_sub_f32 v180, v180, v218\n\t" \
  "v_sub_f32 v181, v181, v218\n\t" \
  "v_sub_f32 v182, v182, v218\n\t" \
  "v_sub_f32 v183, v183, v218\n\t" \
  "v_exp_f32 v208, v168\n\t" \
  "v_exp_f32 v209, v169\n\t" \
  "v_exp_f32 v210, v170\n\t" \
  "v_exp_f32 v211, v171\n\t" \
  "v_exp_f32 v212, v172\n\t" \
  "v_exp_f32 v213, v173\n\t" \
  "v_exp_f32 v214, v174\n\t" \
  "v_exp_f32 v215, v175\n\t" \
  "v_exp_f32 v219, -v218\n\t" \
  "s_nop 0\n\t" \
  "v_cvt_pk_bf16_f32 v192, v208, v209\n\t" \
  "v_cvt_pk_bf16_f32 v193, v210, v211\n\t" \
  "v_cvt_pk_bf16_f32 v194, v212, v213\n\t" \
  "v_cvt_pk_bf16_f32 v195, v214, v215\n\t" \
  "v_mul_f32 v128, v128, v219\n\t" \
  "v_mul_f32 v129, v129, v219\n\t" \
  "v_mul_f32 v130, v130, v219\n\t" \
  "v_mul_f32 v131, v131, v219\n\t" \
  "v_mul_f32 v64, v64, v219\n\t" \
  "v_mul_f32 v65, v65, v219\n\t" \
  "v_mul_f32 v66, v66, v219\n\t" \
  "v_mul_f32 v67, v67, v219\n\t" \
  "v_mul_f32 v72, v72, v219\n\t" \
  "v_mul_f32 v73, v73, v219\n\t" \
  "v_mul_f32 v74, v74, v219\n\t" \
  "v_mul_f32 v75, v75, v219\n\t" \
  "v_mul_f32 v80, v80, v219\n\t" \
  "v_mul_f32 v81, v81, v219\n\t" \
  "v_mul_f32 v82, v82, v219\n\t" \
  "v_mul_f32 v83, v83, v219\n\t" \
  "v_mul_f32 v88, v88, v219\n\t" \
  "v_mul_f32 v89, v89, v219\n\t" \
  "v_mul_f32 v90, v90, v219\n\t" \
  "v_mul_f32 v91, v91, v219\n\t" \
  "v_mul_f32 v96, v96, v219\n\t" \
  "v_mul_f32 v97, v97, v219\n\t" \
  "v_mul_f32 v98, v98, v219\n\t" \
  "v_mul_f32 v99, v99, v219\n\t" \
  "v_mul_f32 v104, v104, v219\n\t" \
  "v_mul_f32 v105, v105, v219\n\t" \
  "v_mul_f32 v106, v106, v219\n\t" \
  "v_mul_f32 v107, v107, v219\n\t" \
  "v_mul_f32 v112, v112, v219\n\t" \
  "v_mul_f32 v113, v113, v219\n\t" \
  "v_mul_f32 v114, v114, v219\n\t" \
  "v_mul_f32 v115, v115, v219\n\t" \
  "v_mul_f32 v120, v120, v219\n\t" \
  "v_mul_f32 v121, v121, v219\n\t" \
  "v_mul_f32 v122, v122, v219\n\t" \
  "v_mul_f32 v123, v123, v219\n\t" \
  "v_max3_f32 v216, v184, v185, v186\n\t" \
  "v_max3_f32 v217, v187, v188, v189\n\t" \
  "v_max3_f32 v216, v216, v190, v191\n\t" \
  "v_max_f32 v216, v216, v217\n\t" \
  "v_mov_b32 v217, v216\n\t" \
  "s_nop 1\n\t" \
  "v_permlane16_swap_b32 v216, v217\n\t" \
  "s_nop 1\n\t" \
  "v_max_f32 v216, v216, v217\n\t" \
  "v_mov_b32 v217, v216\n\t" \
  "s_nop 1\n\t" \
  "v_permlane32_swap_b32 v216, v217\n\t" \
  "s_nop 1\n\t" \
  "v_max_f32 v216, v216, v217\n\t" \
  "v_add_f32 v218, 0x40c00000, v216\n\t" \
  "v_max_f32 v218, 0, v218\n\t" \
  "v_sub_f32 v184, v184, v218\n\t" \
  "v_sub_f32 v185, v185, v218\n\t" \
  "v_sub_f32 v186, v186, v218\n\t" \
  "v_sub_f32 v187, v187, v218\n\t" \
  "v_sub_f32 v188, v188, v218\n\t" \
  "v_sub_f32 v189, v189, v218\n\t" \
  "v_sub_f32 v190, v190, v218\n\t" \
  "v_sub_f32 v191, v191, v218\n\t" \
  "v_sub_f32 v204, v204, v218\n\t" \
  "v_sub_f32 v205, v205, v218\n\t" \
  "v_sub_f32 v206, v206, v218\n\t" \
  "v_sub_f32 v207, v207, v218\n\t" \
  "v_exp_f32 v208, v184\n\t" \
  "v_exp_f32 v209, v185\n\t" \
  "v_exp_f32 v210, v186\n\t" \
  "v_exp_f32 v211, v187\n\t" \
  "v_exp_f32 v212, v188\n\t" \
  "v_exp_f32 v213, v189\n\t" \
  "v_exp_f32 v214, v190\n\t" \
  "v_exp_f32 v215, v191\n\t" \
  "v_exp_f32 v219, -v218\n\t" \
  "s_nop 0\n\t" \
  "v_cvt_pk_bf16_f32 v196, v208, v209\n\t" \
  "v_cvt_pk_bf16_f32 v197, v210, v211\n\t" \
  "v_cvt_pk_bf16_f32 v198, v212, v213\n\t" \
  "v_cvt_pk_bf16_f32 v199, v214, v215\n\t" \
  "v_mul_f32 v132, v132, v219\n\t" \
  "v_mul_f32 v133, v133, v219\n\t" \
  "v_mul_f32 v134, v134, v219\n\t" \
  "v_mul_f32 v135, v135, v219\n\t" \
  "v_mul_f32 v68, v68, v219\n\t" \
  "v_mul_f32 v69, v69, v219\n\t" \
  "v_mul_f32 v70, v70, v219\n\t" \
  "v_mul_f32 v71, v71, v219\n\t" \
  "v_mul_f32 v76, v76, v219\n\t" \
  "v_mul_f32 v77, v77, v219\n\t" \
  "v_mul_f32 v78, v78, v219\n\t" \
  "v_mul_f32 v79, v79, v219\n\t" \
  "v_mul_f32 v84, v84, v219\n\t" \
  "v_mul_f32 v85, v85, v219\n\t" \
  "v_mul_f32 v86, v86, v219\n\t" \
  "v_mul_f32 v87, v87, v219\n\t" \
  "v_mul_f32 v92, v92, v219\n\t" \
  "v_mul_f32 v93, v93, v219\n\t" \
  "v_mul_f32 v94, v94, v219\n\t" \
  "v_mul_f32 v95, v95, v219\n\t" \
  "v_mul_f32 v100, v100, v219\n\t" \
  "v_mul_f32 v101, v101, v219\n\t" \
  "v_mul_f32 v102, v102, v219\n\t" \
  "v_mul_f32 v103, v103, v219\n\t" \
  "v_mul_f32 v108, v108, v219\n\t" \
  "v_mul_f32 v109, v109, v219\n\t" \
  "v_mul_f32 v110, v110, v219\n\t" \
  "v_mul_f32 v111, v111, v219\n\t" \
  "v_mul_f32 v116, v116, v219\n\t" \
  "v_mul_f32 v117, v117, v219\n\t" \
  "v_mul_f32 v118, v118, v219\n\t" \
  "v_mul_f32 v119, v119, v219\n\t" \
  "v_mul_f32 v124, v124, v219\n\t" \
  "v_mul_f32 v125, v125, v219\n\t" \
  "v_mul_f32 v126, v126, v219\n\t" \
  "v_mul_f32 v127, v127, v219\n\t" \
  "s_nop 3\n\t" \
  "s_branch .Lback4_%=\n\t" \
  ".Lrare5_%=:\n\t" \
  "s_nop 7\n\t" \
  "s_nop 7\n\t" \
  "v_max3_f32 v216, v176, v177, v178\n\t" \
  "v_max3_f32 v217, v179, v180, v181\n\t" \
  "v_max3_f32 v216, v216, v182, v183\n\t" \
  "v_max_f32 v216, v216, v217\n\t" \
  "v_mov_b32 v217, v216\n\t" \
  "s_nop 1\n\t" \
  "v_permlane16_swap_b32 v216, v217\n\t" \
  "s_nop 1\n\t" \
  "v_max_f32 v216, v216, v217\n\t" \
  "v_mov_b32 v217, v216\n\t" \
  "s_nop 1\n\t" \
  "v_permlane32_swap_b32 v216, v217\n\t" \
  "s_nop 1\n\t" \
  "v_max_f32 v216, v216, v217\n\t" \
  "v_add_f32 v218, 0x40c00000, v216\n\t" \
  "v_max_f32 v218, 0, v218\n\t" \
  "v_sub_f32 v176, v176, v218\n\t" \
  "v_sub_f32 v177, v177, v218\n\t" \
  "v_sub_f32 v178, v178, v218\n\t" \
  "v_sub_f32 v179, v179, v218\n\t" \
  "v_sub_f32 v180, v180, v218\n\t" \
  "v_sub_f32 v181, v181, v218\n\t" \
  "v_sub_f32 v182, v182, v218\n\t" \
  "v_sub_f32 v183, v183, v218\n\t" \
  "v_sub_f32 v200, v200, v218\n\t" \
  "v_sub_f32 v201, v201, v218\n\t" \
  "v_sub_f32 v202, v202, v218\n\t" \
  "v_sub_f32 v203, v203, v218\n\t" \
  "v_sub_f32 v168, v168, v218\n\t" \
  "v_sub_f32 v169, v169, v218\n\t" \
  "v_sub_f32 v170, v170, v218\n\t" \
  "v_sub_f32 v171, v171, v218\n\t" \
  "v_sub_f32 v172, v172, v218\n\t" \
  "v_sub_f32 v173, v173, v218\n\t" \
  "v_sub_f32 v174, v174, v218\n\t" \
  "v_sub_f32 v175, v175, v218\n\t" \
  "v_exp_f32 v208, v176\n\t" \
  "v_exp_f32 v209, v177\n\t" \
  "v_exp_f32 v210, v178\n\t" \
  "v_exp_f32 v211, v179\n\t" \
  "v_exp_f32 v212, v180\n\t" \
  "v_exp_f32 v213, v181\n\t" \
  "v_exp_f32 v214, v182\n\t" \
  "v_exp_f32 v215, v183\n\t" \
  "v_exp_f32 v219, -v218\n\t" \
  "s_nop 0\n\t" \
  "v_cvt_pk_bf16_f32 v192, v208, v209\n\t" \
  "v_cvt_pk_bf16_f32 v193, v210, v211\n\t" \
  "v_cvt_pk_bf16_f32 v194, v212, v213\n\t" \
  "v_cvt_pk_bf16_f32 v195, v214, v215\n\t" \
  "v_mul_f32 v128, v128, v219\n\t" \
  "v_mul_f32 v129, v129, v219\n\t" \
  "v_mul_f32 v130, v130, v219\n\t" \
  "v_mul_f32 v131, v131, v219\n\t" \
  "v_mul_f32 v64, v64, v219\n\t" \
  "v_mul_f32 v65, v65, v219\n\t" \
  "v_mul_f32 v66, v66, v219\n\t" \
  "v_mul_f32 v67, v67, v219\n\t" \
  "v_mul_f32 v72, v72, v219\n\t" \
  "v_mul_f32 v73, v73, v219\n\t" \
  "v_mul_f32 v74, v74, v219\n\t" \
  "v_mul_f32 v75, v75, v219\n\t" \
  "v_mul_f32 v80, v80, v219\n\t" \
  "v_mul_f32 v81, v81, v219\n\t" \
  "v_mul_f32 v82, v82, v219\n\t" \
  "v_mul_f32 v83, v83, v219\n\t" \
  "v_mul_f32 v88, v88, v219\n\t" \
  "v_mul_f32 v89, v89, v219\n\t" \
  "v_mul_f32 v90, v90, v219\n\t" \
  "v_mul_f32 v91, v91, v219\n\t" \
  "v_mul_f32 v96, v96, v219\n\t" \
  "v_mul_f32 v97, v97, v219\n\t" \
  "v_mul_f32 v98, v98, v219\n\t" \
  "v_mul_f32 v99, v99, v219\n\t" \
  "v_mul_f32 v104, v104, v219\n\t" \
  "v_mul_f32 v105, v105, v219\n\t" \
  "v_mul_f32 v106, v106, v219\n\t" \
  "v_mul_f32 v107, v107, v219\n\t" \
  "v_mul_f32 v112, v112, v219\n\t" \
  "v_mul_f32 v113, v113, v219\n\t" \
  "v_mul_f32 v114, v114, v219\n\t" \
  "v_mul_f32 v115, v115, v219\n\t" \
  "v_mul_f32 v120, v120, v219\n\t" \
  "v_mul_f32 v121, v121, v219\n\t" \
  "v_mul_f32 v122, v122, v219\n\t" \
  "v_mul_f32 v123, v123, v219\n\t" \
  "v_max3_f32 v216, v184, v185, v186\n\t" \
  "v_max3_f32 v217, v187, v188, v189\n\t" \
  "v_max3_f32 v216, v216, v190, v191\n\t" \
  "v_max_f32 v216, v216, v217\n\t" \
  "v_mov_b32 v217, v216\n\t" \
  "s_nop 1\n\t" \
  "v_permlane16_swap_b32 v216, v217\n\t" \
  "s_nop 1\n\t" \
  "v_max_f32 v216, v216, v217\n\t" \
  "v_mov_b32 v217, v216\n\t" \
  "s_nop 1\n\t" \
  "v_permlane32_swap_b32 v216, v217\n\t" \
  "s_nop 1\n\t" \
  "v_max_f32 v216, v216, v217\n\t" \
  "v_add_f32 v218, 0x40c00000, v216\n\t" \
  "v_max_f32 v218, 0, v218\n\t" \
  "v_sub_f32 v184, v184, v218\n\t" \
  "v_sub_f32 v185, v185, v218\n\t" \
  "v_sub_f32 v186, v186, v218\n\t" \
  "v_sub_f32 v187, v187, v218\n\t" \
  "v_sub_f32 v188, v188, v218\n\t" \
  "v_sub_f32 v189, v189, v218\n\t" \
  "v_sub_f32 v190, v190, v218\n\t" \
  "v_sub_f32 v191, v191, v218\n\t" \
  "v_sub_f32 v204, v204, v218\n\t" \
  "v_sub_f32 v205, v205, v218\n\t" \
  "v_sub_f32 v206, v206, v218\n\t" \
  "v_sub_f32 v207, v207, v218\n\t" \
  "v_exp_f32 v208, v184\n\t" \
  "v_exp_f32 v209, v185\n\t" \
  "v_exp_f32 v210, v186\n\t" \
  "v_exp_f32 v211, v187\n\t" \
  "v_exp_f32 v212, v188\n\t" \
  "v_exp_f32 v213, v189\n\t" \
  "v_exp_f32 v214, v190\n\t" \
  "v_exp_f32 v215, v191\n\t" \
  "v_exp_f32 v219, -v218\n\t" \
  "s_nop 0\n\t" \
  "v_cvt_pk_bf16_f32 v196, v208, v209\n\t" \
  "v_cvt_pk_bf16_f32 v197, v210, v211\n\t" \
  "v_cvt_pk_bf16_f32 v198, v212, v213\n\t" \
  "v_cvt_pk_bf16_f32 v199, v214, v215\n\t" \
  "v_mul_f32 v132, v132, v219\n\t" \
  "v_mul_f32 v133, v133, v219\n\t" \
  "v_mul_f32 v134, v134, v219\n\t" \
  "v_mul_f32 v135, v135, v219\n\t" \
  "v_mul_f32 v68, v68, v219\n\t" \
  "v_mul_f32 v69, v69, v219\n\t" \
  "v_mul_f32 v70, v70, v219\n\t" \
  "v_mul_f32 v71, v71, v219\n\t" \
  "v_mul_f32 v76, v76, v219\n\t" \
  "v_mul_f32 v77, v77, v219\n\t" \
  "v_mul_f32 v78, v78, v219\n\t" \
  "v_mul_f32 v79, v79, v219\n\t" \
  "v_mul_f32 v84, v84, v219\n\t" \
  "v_mul_f32 v85, v85, v219\n\t" \
  "v_mul_f32 v86, v86, v219\n\t" \
  "v_mul_f32 v87, v87, v219\n\t" \
  "v_mul_f32 v92, v92, v219\n\t" \
  "v_mul_f32 v93, v93, v219\n\t" \
  "v_mul_f32 v94, v94, v219\n\t" \
  "v_mul_f32 v95, v95, v219\n\t" \
  "v_mul_f32 v100, v100, v219\n\t" \
  "v_mul_f32 v101, v101, v219\n\t" \
  "v_mul_f32 v102, v102, v219\n\t" \
  "v_mul_f32 v103, v103, v219\n\t" \
  "v_mul_f32 v108, v108, v219\n\t" \
  "v_mul_f32 v109, v109, v219\n\t" \
  "v_mul_f32 v110, v110, v219\n\t" \
  "v_mul_f32 v111, v111, v219\n\t" \
  "v_mul_f32 v116, v116, v219\n\t" \
  "v_mul_f32 v117, v117, v219\n\t" \
  "v_mul_f32 v118, v118, v219\n\t" \
  "v_mul_f32 v119, v119, v219\n\t" \
  "v_mul_f32 v124, v124, v219\n\t" \
  "v_mul_f32 v125, v125, v219\n\t" \
  "v_mul_f32 v126, v126, v219\n\t" \
  "v_mul_f32 v127, v127, v219\n\t" \
  "s_nop 3\n\t" \
  "s_branch .Lback5_%=\n\t" \
  ".Lrare6_%=:\n\t" \
  "s_nop 7\n\t" \
  "s_nop 7\n\t" \
  "v_max3_f32 v216, v168, v169, v170\n\t" \
  "v_max3_f32 v217, v171, v172, v173\n\t" \
  "v_max3_f32 v216, v216, v174, v175\n\t" \
  "v_max_f32 v216, v216, v217\n\t" \
  "v_mov_b32 v217, v216\n\t" \
  "s_nop 1\n\t" \
  "v_permlane16_swap_b32 v216, v217\n\t" \
  "s_nop 1\n\t" \
  "v_max_f32 v216, v216, v217\n\t" \
  "v_mov_b32 v217, v216\n\t" \
  "s_nop 1\n\t" \
  "v_permlane32_swap_b32 v216, v217\n\t" \
  "s_nop 1\n\t" \
  "v_max_f32 v216, v216, v217\n\t" \
  "v_add_f32 v218, 0x40c00000, v216\n\t" \
  "v_max_f32 v218, 0, v218\n\t" \
  "v_sub_f32 v168, v168, v218\n\t" \
  "v_sub_f32 v169, v169, v218\n\t" \
  "v_sub_f32 v170, v170, v218\n\t" \
  "v_sub_f32 v171, v171, v218\n\t" \
  "v_sub_f32 v172, v172, v218\n\t" \
  "v_sub_f32 v173, v173, v218\n\t" \
  "v_sub_f32 v174, v174, v218\n\t" \
  "v_sub_f32 v175, v175, v218\n\t" \
  "v_sub_f32 v200, v200, v218\n\t" \
  "v_sub_f32 v201, v201, v218\n\t" \
  "v_sub_f32 v202, v202, v218\n\t" \
  "v_sub_f32 v203, v203, v218\n\t" \
  "v_sub_f32 v176, v176, v218\n\t" \
  "v_sub_f32 v177, v177, v218\n\t" \
  "v_sub_f32 v178, v178, v218\n\t" \
  "v_sub_f32 v179, v179, v218\n\t" \
  "v_sub_f32 v180, v180, v218\n\t" \
  "v_sub_f32 v181, v181, v218\n\t" \
  "v_sub_f32 v182, v182, v218\n\t" \
  "v_sub_f32 v183, v183, v218\n\t" \
  "v_exp_f32 v208, v168\n\t" \
  "v_exp_f32 v209, v169\n\t" \
  "v_exp_f32 v210, v170\n\t" \
  "v_exp_f32 v211, v171\n\t" \
  "v_exp_f32 v212, v172\n\t" \
  "v_exp_f32 v213, v173\n\t" \
  "v_exp_f32 v214, v174\n\t" \
  "v_exp_f32 v215, v175\n\t" \
  "v_exp_f32 v219, -v218\n\t" \
  "s_nop 0\n\t" \
  "v_cvt_pk_bf16_f32 v192, v208, v209\n\t" \
  "v_cvt_pk_bf16_f32 v193, v210, v211\n\t" \
  "v_cvt_pk_bf16_f32 v194, v212, v213\n\t" \
  "v_cvt_pk_bf16_f32 v195, v214, v215\n\t" \
  "v_mul_f32 v128, v128, v219\n\t" \
  "v_mul_f32 v129, v129, v219\n\t" \
  "v_mul_f32 v130, v130, v219\n\t" \
  "v_mul_f32 v131, v131, v219\n\t" \
  "v_mul_f32 v64, v64, v219\n\t" \
  "v_mul_f32 v65, v65, v219\n\t" \
  "v_mul_f32 v66, v66, v219\n\t" \
  "v_mul_f32 v67, v67, v219\n\t" \
  "v_mul_f32 v72, v72, v219\n\t" \
  "v_mul_f32 v73, v73, v219\n\t" \
  "v_mul_f32 v74, v74, v219\n\t" \
  "v_mul_f32 v75, v75, v219\n\t" \
  "v_mul_f32 v80, v80, v219\n\t" \
  "v_mul_f32 v81, v81, v219\n\t" \
  "v_mul_f32 v82, v82, v219\n\t" \
  "v_mul_f32 v83, v83, v219\n\t" \
  "v_mul_f32 v88, v88, v219\n\t" \
  "v_mul_f32 v89, v89, v219\n\t" \
  "v_mul_f32 v90, v90, v219\n\t" \
  "v_mul_f32 v91, v91, v219\n\t" \
  "v_mul_f32 v96, v96, v219\n\t" \
  "v_mul_f32 v97, v97, v219\n\t" \
  "v_mul_f32 v98, v98, v219\n\t" \
  "v_mul_f32 v99, v99, v219\n\t" \
  "v_mul_f32 v104, v104, v219\n\t" \
  "v_mul_f32 v105, v105, v219\n\t" \
  "v_mul_f32 v106, v106, v219\n\t" \
  "v_mul_f32 v107, v107, v219\n\t" \
  "v_mul_f32 v112, v112, v219\n\t" \
  "v_mul_f32 v113, v113, v219\n\t" \
  "v_mul_f32 v114, v114, v219\n\t" \
  "v_mul_f32 v115, v115, v219\n\t" \
  "v_mul_f32 v120, v120, v219\n\t" \
  "v_mul_f32 v121, v121, v219\n\t" \
  "v_mul_f32 v122, v122, v219\n\t" \
  "v_mul_f32 v123, v123, v219\n\t" \
  "v_max3_f32 v216, v184, v185, v186\n\t" \
  "v_max3_f32 v217, v187, v188, v189\n\t" \
  "v_max3_f32 v216, v216, v190, v191\n\t" \
  "v_max_f32 v216, v216, v217\n\t" \
  "v_mov_b32 v217, v216\n\t" \
  "s_nop 1\n\t" \
  "v_permlane16_swap_b32 v216, v217\n\t" \
  "s_nop 1\n\t" \
  "v_max_f32 v216, v216, v217\n\t" \
  "v_mov_b32 v217, v216\n\t" \
  "s_nop 1\n\t" \
  "v_permlane32_swap_b32 v216, v217\n\t" \
  "s_nop 1\n\t" \
  "v_max_f32 v216, v216, v217\n\t" \
  "v_add_f32 v218, 0x40c00000, v216\n\t" \
  "v_max_f32 v218, 0, v218\n\t" \
  "v_sub_f32 v184, v184, v218\n\t" \
  "v_sub_f32 v185, v185, v218\n\t" \
  "v_sub_f32 v186, v186, v218\n\t" \
  "v_sub_f32 v187, v187, v218\n\t" \
  "v_sub_f32 v188, v188, v218\n\t" \
  "v_sub_f32 v189, v189, v218\n\t" \
  "v_sub_f32 v190, v190, v218\n\t" \
  "v_sub_f32 v191, v191, v218\n\t" \
  "v_sub_f32 v204, v204, v218\n\t" \
  "v_sub_f32 v205, v205, v218\n\t" \
  "v_sub_f32 v206, v206, v218\n\t" \
  "v_sub_f32 v207, v207, v218\n\t" \
  "v_exp_f32 v208, v184\n\t" \
  "v_exp_f32 v209, v185\n\t" \
  "v_exp_f32 v210, v186\n\t" \
  "v_exp_f32 v211, v187\n\t" \
  "v_exp_f32 v212, v188\n\t" \
  "v_exp_f32 v213, v189\n\t" \
  "v_exp_f32 v214, v190\n\t" \
  "v_exp_f32 v215, v191\n\t" \
  "v_exp_f32 v219, -v218\n\t" \
  "s_nop 0\n\t" \
  "v_cvt_pk_bf16_f32 v196, v208, v209\n\t" \
  "v_cvt_pk_bf16_f32 v197, v210, v211\n\t" \
  "v_cvt_pk_bf16_f32 v198, v212, v213\n\t" \
  "v_cvt_pk_bf16_f32 v199, v214, v215\n\t" \
  "v_mul_f32 v132, v132, v219\n\t" \
  "v_mul_f32 v133, v133, v219\n\t" \
  "v_mul_f32 v134, v134, v219\n\t" \
  "v_mul_f32 v135, v135, v219\n\t" \
  "v_mul_f32 v68, v68, v219\n\t" \
  "v_mul_f32 v69, v69, v219\n\t" \
  "v_mul_f32 v70, v70, v219\n\t" \
  "v_mul_f32 v71, v71, v219\n\t" \
  "v_mul_f32 v76, v76, v219\n\t" \
  "v_mul_f32 v77, v77, v219\n\t" \
  "v_mul_f32 v78, v78, v219\n\t" \
  "v_mul_f32 v79, v79, v219\n\t" \
  "v_mul_f32 v84, v84, v219\n\t" \
  "v_mul_f32 v85, v85, v219\n\t" \
  "v_mul_f32 v86, v86, v219\n\t" \
  "v_mul_f32 v87, v87, v219\n\t" \
  "v_mul_f32 v92, v92, v219\n\t" \
  "v_mul_f32 v93, v93, v219\n\t" \
  "v_mul_f32 v94, v94, v219\n\t" \
  "v_mul_f32 v95, v95, v219\n\t" \
  "v_mul_f32 v100, v100, v219\n\t" \
  "v_mul_f32 v101, v101, v219\n\t" \
  "v_mul_f32 v102, v102, v219\n\t" \
  "v_mul_f32 v103, v103, v219\n\t" \
  "v_mul_f32 v108, v108, v219\n\t" \
  "v_mul_f32 v109, v109, v219\n\t" \
  "v_mul_f32 v110, v110, v219\n\t" \
  "v_mul_f32 v111, v111, v219\n\t" \
  "v_mul_f32 v116, v116, v219\n\t" \
  "v_mul_f32 v117, v117, v219\n\t" \
  "v_mul_f32 v118, v118, v219\n\t" \
  "v_mul_f32 v119, v119, v219\n\t" \
  "v_mul_f32 v124, v124, v219\n\t" \
  "v_mul_f32 v125, v125, v219\n\t" \
  "v_mul_f32 v126, v126, v219\n\t" \
  "v_mul_f32 v127, v127, v219\n\t" \
  "s_nop 3\n\t" \
  "s_branch .Lback6_%=\n\t" \
  ".Lrare7_%=:\n\t" \
  "s_nop 7\n\t" \
  "s_nop 7\n\t" \
  "v_max3_f32 v216, v176, v177, v178\n\t" \
  "v_max3_f32 v217, v179, v180, v181\n\t" \
  "v_max3_f32 v216, v216, v182, v183\n\t" \
  "v_max_f32 v216, v216, v217\n\t" \
  "v_mov_b32 v217, v216\n\t" \
  "s_nop 1\n\t" \
  "v_permlane16_swap_b32 v216, v217\n\t" \
  "s_nop 1\n\t" \
  "v_max_f32 v216, v216, v217\n\t" \
  "v_mov_b32 v217, v216\n\t" \
  "s_nop 1\n\t" \
  "v_permlane32_swap_b32 v216, v217\n\t" \
  "s_nop 1\n\t" \
  "v_max_f32 v216, v216, v217\n\t" \
  "v_add_f32 v218, 0x40c00000, v216\n\t" \
  "v_max_f32 v218, 0, v218\n\t" \
  "v_sub_f32 v176, v176, v218\n\t" \
  "v_sub_f32 v177, v177, v218\n\t" \
  "v_sub_f32 v178, v178, v218\n\t" \
  "v_sub_f32 v179, v179, v218\n\t" \
  "v_sub_f32 v180, v180, v218\n\t" \
  "v_sub_f32 v181, v181, v218\n\t" \
  "v_sub_f32 v182, v182, v218\n\t" \
  "v_sub_f32 v183, v183, v218\n\t" \
  "v_sub_f32 v200, v200, v218\n\t" \
  "v_sub_f32 v201, v201, v218\n\t" \
  "v_sub_f32 v202, v202, v218\n\t" \
  "v_sub_f32 v203, v203, v218\n\t" \
  "v_exp_f32 v208, v176\n\t" \
  "v_exp_f32 v209, v177\n\t" \
  "v_exp_f32 v210, v178\n\t" \
  "v_exp_f32 v211, v179\n\t" \
  "v_exp_f32 v212, v180\n\t" \
  "v_exp_f32 v213, v181\n\t" \
  "v_exp_f32 v214, v182\n\t" \
  "v_exp_f32 v215, v183\n\t" \
  "v_exp_f32 v219, -v218\n\t" \
  "s_nop 0\n\t" \
  "v_cvt_pk_bf16_f32 v192, v208, v209\n\t" \
  "v_cvt_pk_bf16_f32 v193, v210, v211\n\t" \
  "v_cvt_pk_bf16_f32 v194, v212, v213\n\t" \
  "v_cvt_pk_bf16_f32 v195, v214, v215\n\t" \
  "v_mul_f32 v128, v128, v219\n\t" \
  "v_mul_f32 v129, v129, v219\n\t" \
  "v_mul_f32 v130, v130, v219\n\t" \
  "v_mul_f32 v131, v131, v219\n\t" \
  "v_mul_f32 v64, v64, v219\n\t" \
  "v_mul_f32 v65, v65, v219\n\t" \
  "v_mul_f32 v66, v66, v219\n\t" \
  "v_mul_f32 v67, v67, v219\n\t" \
  "v_mul_f32 v72, v72, v219\n\t" \
  "v_mul_f32 v73, v73, v219\n\t" \
  "v_mul_f32 v74, v74, v219\n\t" \
  "v_mul_f32 v75, v75, v219\n\t" \
  "v_mul_f32 v80, v80, v219\n\t" \
  "v_mul_f32 v81, v81, v219\n\t" \
  "v_mul_f32 v82, v82, v219\n\t" \
  "v_mul_f32 v83, v83, v219\n\t" \
  "v_mul_f32 v88, v88, v219\n\t" \
  "v_mul_f32 v89, v89, v219\n\t" \
  "v_mul_f32 v90, v90, v219\n\t" \
  "v_mul_f32 v91, v91, v219\n\t" \
  "v_mul_f32 v96, v96, v219\n\t" \
  "v_mul_f32 v97, v97, v219\n\t" \
  "v_mul_f32 v98, v98, v219\n\t" \
  "v_mul_f32 v99, v99, v219\n\t" \
  "v_mul_f32 v104, v104, v219\n\t" \
  "v_mul_f32 v105, v105, v219\n\t" \
  "v_mul_f32 v106, v106, v219\n\t" \
  "v_mul_f32 v107, v107, v219\n\t" \
  "v_mul_f32 v112, v112, v219\n\t" \
  "v_mul_f32 v113, v113, v219\n\t" \
  "v_mul_f32 v114, v114, v219\n\t" \
  "v_mul_f32 v115, v115, v219\n\t" \
  "v_mul_f32 v120, v120, v219\n\t" \
  "v_mul_f32 v121, v121, v219\n\t" \
  "v_mul_f32 v122, v122, v219\n\t" \
  "v_mul_f32 v123, v123, v219\n\t" \
  "v_max3_f32 v216, v184, v185, v186\n\t" \
  "v_max3_f32 v217, v187, v188, v189\n\t" \
  "v_max3_f32 v216, v216, v190, v191\n\t" \
  "v_max_f32 v216, v216, v217\n\t" \
  "v_mov_b32 v217, v216\n\t" \
  "s_nop 1\n\t" \
  "v_permlane16_swap_b32 v216, v217\n\t" \
  "s_nop 1\n\t" \
  "v_max_f32 v216, v216, v217\n\t" \
  "v_mov_b32 v217, v216\n\t" \
  "s_nop 1\n\t" \
  "v_permlane32_swap_b32 v216, v217\n\t" \
  "s_nop 1\n\t" \
  "v_max_f32 v216, v216, v217\n\t" \
  "v_add_f32 v218, 0x40c00000, v216\n\t" \
  "v_max_f32 v218, 0, v218\n\t" \
  "v_sub_f32 v184, v184, v218\n\t" \
  "v_sub_f32 v185, v185, v218\n\t" \
  "v_sub_f32 v186, v186, v218\n\t" \
  "v_sub_f32 v187, v187, v218\n\t" \
  "v_sub_f32 v188, v188, v218\n\t" \
  "v_sub_f32 v189, v189, v218\n\t" \
  "v_sub_f32 v190, v190, v218\n\t" \
  "v_sub_f32 v191, v191, v218\n\t" \
  "v_sub_f32 v204, v204, v218\n\t" \
  "v_sub_f32 v205, v205, v218\n\t" \
  "v_sub_f32 v206, v206, v218\n\t" \
  "v_sub_f32 v207, v207, v218\n\t" \
  "v_exp_f32 v208, v184\n\t" \
  "v_exp_f32 v209, v185\n\t" \
  "v_exp_f32 v210, v186\n\t" \
  "v_exp_f32 v211, v187\n\t" \
  "v_exp_f32 v212, v188\n\t" \
  "v_exp_f32 v213, v189\n\t" \
  "v_exp_f32 v214, v190\n\t" \
  "v_exp_f32 v215, v191\n\t" \
  "v_exp_f32 v219, -v218\n\t" \
  "s_nop 0\n\t" \
  "v_cvt_pk_bf16_f32 v196, v208, v209\n\t" \
  "v_cvt_pk_bf16_f32 v197, v210, v211\n\t" \
  "v_cvt_pk_bf16_f32 v198, v212, v213\n\t" \
  "v_cvt_pk_bf16_f32 v199, v214, v215\n\t" \
  "v_mul_f32 v132, v132, v219\n\t" \
  "v_mul_f32 v133, v133, v219\n\t" \
  "v_mul_f32 v134, v134, v219\n\t" \
  "v_mul_f32 v135, v135, v219\n\t" \
  "v_mul_f32 v68, v68, v219\n\t" \
  "v_mul_f32 v69, v69, v219\n\t" \
  "v_mul_f32 v70, v70, v219\n\t" \
  "v_mul_f32 v71, v71, v219\n\t" \
  "v_mul_f32 v76, v76, v219\n\t" \
  "v_mul_f32 v77, v77, v219\n\t" \
  "v_mul_f32 v78, v78, v219\n\t" \
  "v_mul_f32 v79, v79, v219\n\t" \
  "v_mul_f32 v84, v84, v219\n\t" \
  "v_mul_f32 v85, v85, v219\n\t" \
  "v_mul_f32 v86, v86, v219\n\t" \
  "v_mul_f32 v87, v87, v219\n\t" \
  "v_mul_f32 v92, v92, v219\n\t" \
  "v_mul_f32 v93, v93, v219\n\t" \
  "v_mul_f32 v94, v94, v219\n\t" \
  "v_mul_f32 v95, v95, v219\n\t" \
  "v_mul_f32 v100, v100, v219\n\t" \
  "v_mul_f32 v101, v101, v219\n\t" \
  "v_mul_f32 v102, v102, v219\n\t" \
  "v_mul_f32 v103, v103, v219\n\t" \
  "v_mul_f32 v108, v108, v219\n\t" \
  "v_mul_f32 v109, v109, v219\n\t" \
  "v_mul_f32 v110, v110, v219\n\t" \
  "v_mul_f32 v111, v111, v219\n\t" \
  "v_mul_f32 v116, v116, v219\n\t" \
  "v_mul_f32 v117, v117, v219\n\t" \
  "v_mul_f32 v118, v118, v219\n\t" \
  "v_mul_f32 v119, v119, v219\n\t" \
  "v_mul_f32 v124, v124, v219\n\t" \
  "v_mul_f32 v125, v125, v219\n\t" \
  "v_mul_f32 v126, v126, v219\n\t" \
  "v_mul_f32 v127, v127, v219\n\t" \
  "s_nop 3\n\t" \
  "s_branch .Lback7_%=\n\t" \
  ".Lend_%=:\n\t" \
  "s_nop 1\n\t"
#define BODY_B \
  "s_add_u32 %[wb], %[wb], 0x40000\n\t" \
  "s_and_b32 %[wb], %[wb], 0xfffff\n\t" \
  "s_add_u32 m0, %[mw], 8192\n\t" \
  "s_add_u32 %[st], %[wa], %[wb]\n\t" \
  "buffer_load_dwordx4 %[wv0], %[rq], %[st] offen offset:0 lds\n\t" \
  "buffer_load_dwordx4 %[wv1], %[rq], %[st] offen offset:1024 lds\n\t" \
  "buffer_load_dwordx4 %[wv2], %[rq], %[st] offen offset:2048 lds\n\t" \
  "buffer_load_dwordx4 %[wv3], %[rq], %[st] offen offset:3072 lds\n\t" \
  "s_add_u32 %[wb], %[wb], 0x40000\n\t" \
  "s_and_b32 %[wb], %[wb], 0xfffff\n\t" \
  "s_add_u32 m0, %[mw], 16384\n\t" \
  "s_add_u32 %[st], %[wa], %[wb]\n\t" \
  "buffer_load_dwordx4 %[wv0], %[rq], %[st] offen offset:0 lds\n\t" \
  "buffer_load_dwordx4 %[wv1], %[rq], %[st] offen offset:1024 lds\n\t" \
  "buffer_load_dwordx4 %[wv2], %[rq], %[st] offen offset:2048 lds\n\t" \
  "buffer_load_dwordx4 %[wv3], %[rq], %[st] offen offset:3072 lds\n\t" \
  "s_add_u32 %[wb], %[wb], 0x40000\n\t" \
  "s_and_b32 %[wb], %[wb], 0xfffff\n\t" \
  "s_add_u32 m0, %[mw], 24576\n\t" \
  "s_add_u32 %[st], %[wa], %[wb]\n\t" \
  "buffer_load_dwordx4 %[wv0], %[rq], %[st] offen offset:0 lds\n\t" \
  "buffer_load_dwordx4 %[wv1], %[rq], %[st] offen offset:1024 lds\n\t" \
  "buffer_load_dwordx4 %[wv2], %[rq], %[st] offen offset:2048 lds\n\t" \
  "buffer_load_dwordx4 %[wv3], %[rq], %[st] offen offset:3072 lds\n\t" \
  "s_add_u32 %[wb], %[wb], 0x40000\n\t" \
  "s_and_b32 %[wb], %[wb], 0xfffff\n\t" \
  "s_add_u32 %[wa], %[wa], 0x100000\n\t" \
  "s_and_b32 %[wa], %[wa], 0x3fffff\n\t" \
  "s_add_u32 m0, %[mw], 32768\n\t" \
  "s_add_u32 %[st], %[wa], %[wb]\n\t" \
  "buffer_load_dwordx4 %[wv0], %[rq], %[st] offen offset:0 lds\n\t" \
  "buffer_load_dwordx4 %[wv1], %[rq], %[st] offen offset:1024 lds\n\t" \
  "buffer_load_dwordx4 %[wv2], %[rq], %[st] offen offset:2048 lds\n\t" \
  "buffer_load_dwordx4 %[wv3], %[rq], %[st] offen offset:3072 lds\n\t" \
  "s_add_u32 %[wb], %[wb], 0x40000\n\t" \
  "s_and_b32 %[wb], %[wb], 0xfffff\n\t" \
  "s_add_u32 m0, %[mw], 40960\n\t" \
  "s_add_u32 %[st], %[wa], %[wb]\n\t" \
  "buffer_load_dwordx4 %[wv0], %[rq], %[st] offen offset:0 lds\n\t" \
  "buffer_load_dwordx4 %[wv1], %[rq], %[st] offen offset:1024 lds\n\t" \
  "buffer_load_dwordx4 %[wv2], %[rq], %[st] offen offset:2048 lds\n\t" \
  "buffer_load_dwordx4 %[wv3], %[rq], %[st] offen offset:3072 lds\n\t" \
  "s_add_u32 %[wb], %[wb], 0x40000\n\t" \
  "s_and_b32 %[wb], %[wb], 0xfffff\n\t" \
  "s_add_u32 m0, %[mw], 49152\n\t" \
  "s_add_u32 %[st], %[wa], %[wb]\n\t" \
  "buffer_load_dwordx4 %[wv0], %[rq], %[st] offen offset:0 lds\n\t" \
  "buffer_load_dwordx4 %[wv1], %[rq], %[st] offen offset:1024 lds\n\t" \
  "buffer_load_dwordx4 %[wv2], %[rq], %[st] offen offset:2048 lds\n\t" \
  "buffer_load_dwordx4 %[wv3], %[rq], %[st] offen offset:3072 lds\n\t" \
  "s_add_u32 %[wb], %[wb], 0x40000\n\t" \
  "s_and_b32 %[wb], %[wb], 0xfffff\n\t" \
  "s_add_u32 m0, %[mw], 57344\n\t" \
  "s_add_u32 %[st], %[wa], %[wb]\n\t" \
  "buffer_load_dwordx4 %[wv0], %[rq], %[st] offen offset:0 lds\n\t" \
  "buffer_load_dwordx4 %[wv1], %[rq], %[st] offen offset:1024 lds\n\t" \
  "buffer_load_dwordx4 %[wv2], %[rq], %[st] offen offset:2048 lds\n\t" \
  "buffer_load_dwordx4 %[wv3], %[rq], %[st] offen offset:3072 lds\n\t" \
  "buffer_load_dwordx2 v[184:185], off, %[re], 0 sc1\n\t" \
  "v_mov_b32 v64, 0\n\t" \
  "v_mov_b32 v65, 0\n\t" \
  "v_mov_b32 v66, 0\n\t" \
  "v_mov_b32 v67, 0\n\t" \
  "v_mov_b32 v68, 0\n\t" \
  "v_mov_b32 v69, 0\n\t" \
  "v_mov_b32 v70, 0\n\t" \
  "v_mov_b32 v71, 0\n\t" \
  "v_mov_b32 v72, 0\n\t" \
  "v_mov_b32 v73, 0\n\t" \
  "v_mov_b32 v74, 0\n\t" \
  "v_mov_b32 v75, 0\n\t" \
  "v_mov_b32 v76, 0\n\t" \
  "v_mov_b32 v77, 0\n\t" \
  "v_mov_b32 v78, 0\n\t" \
  "v_mov_b32 v79, 0\n\t" \
  "v_mov_b32 v80, 0\n\t" \
  "v_mov_b32 v81, 0\n\t" \
  "v_mov_b32 v82, 0\n\t" \
  "v_mov_b32 v83, 0\n\t" \
  "v_mov_b32 v84, 0\n\t" \
  "v_mov_b32 v85, 0\n\t" \
  "v_mov_b32 v86, 0\n\t" \
  "v_mov_b32 v87, 0\n\t" \
  "v_mov_b32 v88, 0\n\t" \
  "v_mov_b32 v89, 0\n\t" \
  "v_mov_b32 v90, 0\n\t" \
  "v_mov_b32 v91, 0\n\t" \
  "v_mov_b32 v92, 0\n\t" \
  "v_mov_b32 v93, 0\n\t" \
  "v_mov_b32 v94, 0\n\t" \
  "v_mov_b32 v95, 0\n\t" \
  "v_mov_b32 v96, 0\n\t" \
  "v_mov_b32 v97, 0\n\t" \
  "v_mov_b32 v98, 0\n\t" \
  "v_mov_b32 v99, 0\n\t" \
  "v_mov_b32 v100, 0\n\t" \
  "v_mov_b32 v101, 0\n\t" \
  "v_mov_b32 v102, 0\n\t" \
  "v_mov_b32 v103, 0\n\t" \
  "v_mov_b32 v104, 0\n\t" \
  "v_mov_b32 v105, 0\n\t" \
  "v_mov_b32 v106, 0\n\t" \
  "v_mov_b32 v107, 0\n\t" \
  "v_mov_b32 v108, 0\n\t" \
  "v_mov_b32 v109, 0\n\t" \
  "v_mov_b32 v110, 0\n\t" \
  "v_mov_b32 v111, 0\n\t" \
  "v_mov_b32 v112, 0\n\t" \
  "v_mov_b32 v113, 0\n\t" \
  "v_mov_b32 v114, 0\n\t" \
  "v_mov_b32 v115, 0\n\t" \
  "v_mov_b32 v116, 0\n\t" \
  "v_mov_b32 v117, 0\n\t" \
  "v_mov_b32 v118, 0\n\t" \
  "v_mov_b32 v119, 0\n\t" \
  "v_mov_b32 v120, 0\n\t" \
  "v_mov_b32 v121, 0\n\t" \
  "v_mov_b32 v122, 0\n\t" \
  "v_mov_b32 v123, 0\n\t" \
  "v_mov_b32 v124, 0\n\t" \
  "v_mov_b32 v125, 0\n\t" \
  "v_mov_b32 v126, 0\n\t" \
  "v_mov_b32 v127, 0\n\t" \
  "v_mov_b32 v128, 0\n\t" \
  "v_mov_b32 v129, 0\n\t" \
  "v_mov_b32 v130, 0\n\t" \
  "v_mov_b32 v131, 0\n\t" \
  "v_mov_b32 v132, 0\n\t" \
  "v_mov_b32 v133, 0\n\t" \
  "v_mov_b32 v134, 0\n\t" \
  "v_mov_b32 v135, 0\n\t" \
  "s_waitcnt vmcnt(24)\n\t" \
  "s_barrier\n\t" \
  "ds_read_b128 a[128:131], %[xa0] offset:0\n\t" \
  "ds_read_b128 a[132:135], %[xa0] offset:4096\n\t" \
  "ds_read_b128 a[136:139], %[xa1] offset:0\n\t" \
  "ds_read_b128 a[140:143], %[xa1] offset:4096\n\t" \
  "ds_read_b64_tr_b16 a[64:65], %[va0] offset:0\n\t" \
  "ds_read_b64_tr_b16 a[66:67], %[va0] offset:1024\n\t" \
  "ds_read_b64_tr_b16 a[68:69], %[va1] offset:0\n\t" \
  "ds_read_b64_tr_b16 a[70:71], %[va1] offset:1024\n\t" \
  "ds_read_b64_tr_b16 a[72:73], %[va2] offset:0\n\t" \
  "ds_read_b64_tr_b16 a[74:75], %[va2] offset:1024\n\t" \
  "ds_read_b64_tr_b16 a[76:77], %[va3] offset:0\n\t" \
  "ds_read_b64_tr_b16 a[78:79], %[va3] offset:1024\n\t" \
  "ds_read_b64_tr_b16 a[80:81], %[va4] offset:0\n\t" \
  "ds_read_b64_tr_b16 a[82:83], %[va4] offset:1024\n\t" \
  "ds_read_b64_tr_b16 a[84:85], %[va5] offset:0\n\t" \
  "ds_read_b64_tr_b16 a[86:87], %[va5] offset:1024\n\t" \
  "ds_read_b64_tr_b16 a[88:89], %[va6] offset:0\n\t" \
  "ds_read_b64_tr_b16 a[90:91], %[va6] offset:1024\n\t" \
  "ds_read_b64_tr_b16 a[92:93], %[va7] offset:0\n\t" \
  "ds_read_b64_tr_b16 a[94:95], %[va7] offset:1024\n\t" \
  "ds_read_b64_tr_b16 a[96:97], %[va0] offset:8192\n\t" \
  "ds_read_b64_tr_b16 a[98:99], %[va0] offset:9216\n\t" \
  "ds_read_b64_tr_b16 a[100:101], %[va1] offset:8192\n\t" \
  "ds_read_b64_tr_b16 a[102:103], %[va1] offset:9216\n\t" \
  "ds_read_b64_tr_b16 a[104:105], %[va2] offset:8192\n\t" \
  "ds_read_b64_tr_b16 a[106:107], %[va2] offset:9216\n\t" \
  "ds_read_b64_tr_b16 a[108:109], %[va3] offset:8192\n\t" \
  "ds_read_b64_tr_b16 a[110:111], %[va3] offset:9216\n\t" \
  "ds_read_b64_tr_b16 a[112:113], %[va4] offset:8192\n\t" \
  "ds_read_b64_tr_b16 a[114:115], %[va4] offset:9216\n\t" \
  "ds_read_b64_tr_b16 a[116:117], %[va5] offset:8192\n\t" \
  "ds_read_b64_tr_b16 a[118:119], %[va5] offset:9216\n\t" \
  "ds_read_b64_tr_b16 a[120:121], %[va6] offset:8192\n\t" \
  "ds_read_b64_tr_b16 a[122:123], %[va6] offset:9216\n\t" \
  "ds_read_b64_tr_b16 a[124:125], %[va7] offset:8192\n\t" \
  "ds_read_b64_tr_b16 a[126:127], %[va7] offset:9216\n\t" \
  "ds_read_b128 a[144:147], %[xa2] offset:0\n\t" \
  "ds_read_b128 a[148:151], %[xa2] offset:4096\n\t" \
  "ds_read_b128 a[152:155], %[xa3] offset:0\n\t" \
  "ds_read_b128 a[156:159], %[xa3] offset:4096\n\t" \
  "ds_read_b128 a[160:163], %[xa0] offset:16384\n\t" \
  "ds_read_b128 a[164:167], %[xa0] offset:20480\n\t" \
  "ds_read_b128 a[168:171], %[xa1] offset:16384\n\t" \
  "ds_read_b128 a[172:175], %[xa1] offset:20480\n\t" \
  "ds_read_b128 a[176:179], %[xa2] offset:16384\n\t" \
  "ds_read_b128 a[180:183], %[xa2] offset:20480\n\t" \
  "ds_read_b128 a[184:187], %[xa3] offset:16384\n\t" \
  "ds_read_b128 a[188:191], %[xa3] offset:20480\n\t" \
  "s_waitcnt vmcnt(16) lgkmcnt(0)\n\t" \
  "s_barrier\n\t" \
  "v_mfma_f32_16x16x32_bf16 v[192:195], a[64:67], a[128:131], 0\n\t" \
  "ds_read_b64_tr_b16 a[0:1], %[va0] offset:16384\n\t" \
  "ds_read_b64_tr_b16 a[2:3], %[va0] offset:17408\n\t" \
  "s_add_u32 m0, %[mz], 0\n\t" \
  "v_mfma_f32_16x16x32_bf16 v[196:199], a[64:67], a[132:135], 0\n\t" \
  "ds_read_b64_tr_b16 a[4:5], %[va1] offset:16384\n\t" \
  "ds_read_b64_tr_b16 a[6:7], %[va1] offset:17408\n\t" \
  "s_add_u32 %[st], %[sx1], 0\n\t" \
  "v_mfma_f32_16x16x32_bf16 v[200:203], a[68:71], a[128:131], 0\n\t" \
  "ds_read_b64_tr_b16 a[8:9], %[va2] offset:16384\n\t" \
  "ds_read_b64_tr_b16 a[10:11], %[va2] offset:17408\n\t" \
  "buffer_load_dwordx4 %[xv0], %[rx], %[st] offen offset:0 lds\n\t" \
  "v_mfma_f32_16x16x32_bf16 v[204:207], a[68:71], a[132:135], 0\n\t" \
  "ds_read_b64_tr_b16 a[12:13], %[va3] offset:16384\n\t" \
  "ds_read_b64_tr_b16 a[14:15], %[va3] offset:17408\n\t" \
  "buffer_load_dwordx4 %[xv1], %[rx], %[st] offen offset:1024 lds\n\t" \
  "v_mfma_f32_16x16x32_bf16 v[208:211], a[72:75], a[128:131], 0\n\t" \
  "ds_read_b64_tr_b16 a[16:17], %[va4] offset:16384\n\t" \
  "ds_read_b64_tr_b16 a[18:19], %[va4] offset:17408\n\t" \
  "buffer_load_dwordx4 %[xv2], %[rx], %[st] offen offset:2048 lds\n\t" \
  "v_mfma_f32_16x16x32_bf16 v[212:215], a[72:75], a[132:135], 0\n\t" \
  "ds_read_b64_tr_b16 a[20:21], %[va5] offset:16384\n\t" \
  "buffer_load_dwordx4 %[xv3], %[rx], %[st] offen offset:3072 lds\n\t" \
  "v_mfma_f32_16x16x32_bf16 v[216:219], a[76:79], a[128:131], 0\n\t" \
  "ds_read_b64_tr_b16 a[22:23], %[va5] offset:17408\n\t" \
  "s_add_u32 m0, %[mz], 4096\n\t" \
  "v_mfma_f32_16x16x32_bf16 v[220:223], a[76:79], a[132:135], 0\n\t" \
  "ds_read_b64_tr_b16 a[24:25], %[va6] offset:16384\n\t" \
  "s_add_u32 %[st], %[sx1], 16384\n\t" \
  "v_mfma_f32_16x16x32_bf16 v[224:227], a[80:83], a[128:131], 0\n\t" \
  "ds_read_b64_tr_b16 a[26:27], %[va6] offset:17408\n\t" \
  "buffer_load_dwordx4 %[xv0], %[rx], %[st] offen offset:0 lds\n\t" \
  "v_mfma_f32_16x16x32_bf16 v[228:231], a[80:83], a[132:135], 0\n\t" \
  "ds_read_b64_tr_b16 a[28:29], %[va7] offset:16384\n\t" \
  "buffer_load_dwordx4 %[xv1], %[rx], %[st] offen offset:1024 lds\n\t" \
  "v_mfma_f32_16x16x32_bf16 v[232:235], a[84:87], a[128:131], 0\n\t" \
  "ds_read_b64_tr_b16 a[30:31], %[va7] offset:17408\n\t" \
  "buffer_load_dwordx4 %[xv2], %[rx], %[st] offen offset:2048 lds\n\t" \
  "v_mfma_f32_16x16x32_bf16 v[236:239], a[84:87], a[132:135], 0\n\t" \
  "ds_read_b64_tr_b16 a[32:33], %[va0] offset:24576\n\t" \
  "buffer_load_dwordx4 %[xv3], %[rx], %[st] offen offset:3072 lds\n\t" \
  "v_mfma_f32_16x16x32_bf16 v[240:243], a[88:91], a[128:131], 0\n\t" \
  "ds_read_b64_tr_b16 a[34:35], %[va0] offset:25600\n\t" \
  "s_add_u32 %[wb], %[wb], 0x40000\n\t" \
  "v_mfma_f32_16x16x32_bf16 v[244:247], a[88:91], a[132:135], 0\n\t" \
  "ds_read_b64_tr_b16 a[36:37], %[va1] offset:24576\n\t" \
  "s_and_b32 %[wb], %[wb], 0xfffff\n\t" \
  "v_mfma_f32_16x16x32_bf16 v[248:251], a[92:95], a[128:131], 0\n\t" \
  "ds_read_b64_tr_b16 a[38:39], %[va1] offset:25600\n\t" \
  "s_add_u32 %[wa], %[wa], 0x100000\n\t" \
  "v_mfma_f32_16x16x32_bf16 v[252:255], a[92:95], a[132:135], 0\n\t" \
  "ds_read_b64_tr_b16 a[40:41], %[va2] offset:24576\n\t" \
  "s_and_b32 %[wa], %[wa], 0x3fffff\n\t" \
  "v_mfma_f32_16x16x32_bf16 v[192:195], a[96:99], a[136:139], v[192:195]\n\t" \
  "ds_read_b64_tr_b16 a[42:43], %[va2] offset:25600\n\t" \
  "s_add_u32 m0, %[mw], 0\n\t" \
  "v_mfma_f32_16x16x32_bf16 v[196:199], a[96:99], a[140:143], v[196:199]\n\t" \
  "ds_read_b64_tr_b16 a[44:45], %[va3] offset:24576\n\t" \
  "s_add_u32 %[st], %[wa], %[wb]\n\t" \
  "v_mfma_f32_16x16x32_bf16 v[200:203], a[100:103], a[136:139], v[200:203]\n\t" \
  "ds_read_b64_tr_b16 a[46:47], %[va3] offset:25600\n\t" \
  "buffer_load_dwordx4 %[wv0], %[rq], %[st] offen offset:0 lds\n\t" \
  "v_mfma_f32_16x16x32_bf16 v[204:207], a[100:103], a[140:143], v[204:207]\n\t" \
  "ds_read_b64_tr_b16 a[48:49], %[va4] offset:24576\n\t" \
  "buffer_load_dwordx4 %[wv1], %[rq], %[st] offen offset:1024 lds\n\t" \
  "v_mfma_f32_16x16x32_bf16 v[208:211], a[104:107], a[136:139], v[208:211]\n\t" \
  "ds_read_b64_tr_b16 a[50:51], %[va4] offset:25600\n\t" \
  "buffer_load_dwordx4 %[wv2], %[rq], %[st] offen offset:2048 lds\n\t" \
  "v_mfma_f32_16x16x32_bf16 v[212:215], a[104:107], a[140:143], v[212:215]\n\t" \
  "ds_read_b64_tr_b16 a[52:53], %[va5] offset:24576\n\t" \
  "buffer_load_dwordx4 %[wv3], %[rq], %[st] offen offset:3072 lds\n\t" \
  "v_mfma_f32_16x16x32_bf16 v[216:219], a[108:111], a[136:139], v[216:219]\n\t" \
  "ds_read_b64_tr_b16 a[54:55], %[va5] offset:25600\n\t" \
  "s_add_u32 %[wb], %[wb], 0x40000\n\t" \
  "v_mfma_f32_16x16x32_bf16 v[220:223], a[108:111], a[140:143], v[220:223]\n\t" \
  "ds_read_b64_tr_b16 a[56:57], %[va6] offset:24576\n\t" \
  "s_and_b32 %[wb], %[wb], 0xfffff\n\t" \
  "v_mfma_f32_16x16x32_bf16 v[224:227], a[112:115], a[136:139], v[224:227]\n\t" \
  "ds_read_b64_tr_b16 a[58:59], %[va6] offset:25600\n\t" \
  "s_add_u32 m0, %[mw], 8192\n\t" \
  "v_mfma_f32_16x16x32_bf16 v[228:231], a[112:115], a[140:143], v[228:231]\n\t" \
  "ds_read_b64_tr_b16 a[60:61], %[va7] offset:24576\n\t" \
  "s_add_u32 %[st], %[wa], %[wb]\n\t" \
  "v_mfma_f32_16x16x32_bf16 v[232:235], a[116:119], a[136:139], v[232:235]\n\t" \
  "ds_read_b64_tr_b16 a[62:63], %[va7] offset:25600\n\t" \
  "buffer_load_dwordx4 %[wv0], %[rq], %[st] offen offset:0 lds\n\t" \
  "v_mfma_f32_16x16x32_bf16 v[236:239], a[116:119], a[140:143], v[236:239]\n\t" \
  "buffer_load_dwordx4 %[wv1], %[rq], %[st] offen offset:1024 lds\n\t" \
  "v_mfma_f32_16x16x32_bf16 v[240:243], a[120:123], a[136:139], v[240:243]\n\t" \
  "buffer_load_dwordx4 %[wv2], %[rq], %[st] offen offset:2048 lds\n\t" \
  "v_mfma_f32_16x16x32_bf16 v[244:247], a[120:123], a[140:143], v[244:247]\n\t" \
  "buffer_load_dwordx4 %[wv3], %[rq], %[st] offen offset:3072 lds\n\t" \
  "v_mfma_f32_16x16x32_bf16 v[248:251], a[124:127], a[136:139], v[248:251]\n\t" \
  "v_mfma_f32_16x16x32_bf16 v[252:255], a[124:127], a[140:143], v[252:255]\n\t" \
  "s_waitcnt vmcnt(24) lgkmcnt(0)\n\t" \
  "s_barrier\n\t" \
  "v_mfma_f32_16x16x32_bf16 v[192:195], a[0:3], a[144:147], v[192:195]\n\t" \
  "ds_read_b64_tr_b16 a[64:65], %[va0] offset:32768\n\t" \
  "s_add_u32 %[wb], %[wb], 0x40000\n\t" \
  "v_mfma_f32_16x16x32_bf16 v[196:199], a[0:3], a[148:151], v[196:199]\n\t" \
  "ds_read_b64_tr_b16 a[66:67], %[va0] offset:33792\n\t" \
  "s_and_b32 %[wb], %[wb], 0xfffff\n\t" \
  "v_mfma_f32_16x16x32_bf16 v[200:203], a[4:7], a[144:147], v[200:203]\n\t" \
  "ds_read_b64_tr_b16 a[68:69], %[va1] offset:32768\n\t" \
  "ds_read_b64_tr_b16 a[70:71], %[va1] offset:33792\n\t" \
  "v_mfma_f32_16x16x32_bf16 v[204:207], a[4:7], a[148:151], v[204:207]\n\t" \
  "ds_read_b64_tr_b16 a[72:73], %[va2] offset:32768\n\t" \
  "s_add_u32 m0, %[mw], 16384\n\t" \
  "v_mfma_f32_16x16x32_bf16 v[208:211], a[8:11], a[144:147], v[208:211]\n\t" \
  "ds_read_b64_tr_b16 a[74:75], %[va2] offset:33792\n\t" \
  "v_mfma_f32_16x16x32_bf16 v[212:215], a[8:11], a[148:151], v[212:215]\n\t" \
  "ds_read_b64_tr_b16 a[76:77], %[va3] offset:32768\n\t" \
  "s_add_u32 %[st], %[wa], %[wb]\n\t" \
  "v_mfma_f32_16x16x32_bf16 v[216:219], a[12:15], a[144:147], v[216:219]\n\t" \
  "ds_read_b64_tr_b16 a[78:79], %[va3] offset:33792\n\t" \
  "ds_read_b64_tr_b16 a[80:81], %[va4] offset:32768\n\t" \
  "v_mfma_f32_16x16x32_bf16 v[220:223], a[12:15], a[148:151], v[220:223]\n\t" \
  "ds_read_b64_tr_b16 a[82:83], %[va4] offset:33792\n\t" \
  "buffer_load_dwordx4 %[wv0], %[rq], %[st] offen offset:0 lds\n\t" \
  "v_mfma_f32_16x16x32_bf16 v[224:227], a[16:19], a[144:147], v[224:227]\n\t" \
  "ds_read_b64_tr_b16 a[84:85], %[va5] offset:32768\n\t" \
  "v_mfma_f32_16x16x32_bf16 v[228:231], a[16:19], a[148:151], v[228:231]\n\t" \
  "ds_read_b64_tr_b16 a[86:87], %[va5] offset:33792\n\t" \
  "buffer_load_dwordx4 %[wv1], %[rq], %[st] offen offset:1024 lds\n\t" \
  "v_mfma_f32_16x16x32_bf16 v[232:235], a[20:23], a[144:147], v[232:235]\n\t" \
  "ds_read_b64_tr_b16 a[88:89], %[va6] offset:32768\n\t" \
  "ds_read_b64_tr_b16 a[90:91], %[va6] offset:33792\n\t" \
  "v_mfma_f32_16x16x32_bf16 v[236:239], a[20:23], a[148:151], v[236:239]\n\t" \
  "ds_read_b64_tr_b16 a[92:93], %[va7] offset:32768\n\t" \
  "buffer_load_dwordx4 %[wv2], %[rq], %[st] offen offset:2048 lds\n\t" \
  "v_mfma_f32_16x16x32_bf16 v[240:243], a[24:27], a[144:147], v[240:243]\n\t" \
  "ds_read_b64_tr_b16 a[94:95], %[va7] offset:33792\n\t" \
  "v_mfma_f32_16x16x32_bf16 v[244:247], a[24:27], a[148:151], v[244:247]\n\t" \
  "ds_read_b64_tr_b16 a[96:97], %[va0] offset:40960\n\t" \
  "buffer_load_dwordx4 %[wv3], %[rq], %[st] offen offset:3072 lds\n\t" \
  "v_mfma_f32_16x16x32_bf16 v[248:251], a[28:31], a[144:147], v[248:251]\n\t" \
  "ds_read_b64_tr_b16 a[98:99], %[va0] offset:41984\n\t" \
  "ds_read_b64_tr_b16 a[100:101], %[va1] offset:40960\n\t" \
  "v_mfma_f32_16x16x32_bf16 v[252:255], a[28:31], a[148:151], v[252:255]\n\t" \
  "ds_read_b64_tr_b16 a[102:103], %[va1] offset:41984\n\t" \
  "s_add_u32 %[wb], %[wb], 0x40000\n\t" \
  "v_mfma_f32_16x16x32_bf16 v[192:195], a[32:35], a[152:155], v[192:195]\n\t" \
  "ds_read_b64_tr_b16 a[104:105], %[va2] offset:40960\n\t" \
  "v_mfma_f32_16x16x32_bf16 v[196:199], a[32:35], a[156:159], v[196:199]\n\t" \
  "ds_read_b64_tr_b16 a[106:107], %[va2] offset:41984\n\t" \
  "s_and_b32 %[wb], %[wb], 0xfffff\n\t" \
  "v_mfma_f32_16x16x32_bf16 v[200:203], a[36:39], a[152:155], v[200:203]\n\t" \
  "ds_read_b64_tr_b16 a[108:109], %[va3] offset:40960\n\t" \
  "ds_read_b64_tr_b16 a[110:111], %[va3] offset:41984\n\t" \
  "v_mfma_f32_16x16x32_bf16 v[204:207], a[36:39], a[156:159], v[204:207]\n\t" \
  "ds_read_b64_tr_b16 a[112:113], %[va4] offset:40960\n\t" \
  "s_add_u32 m0, %[mw], 24576\n\t" \
  "v_mfma_f32_16x16x32_bf16 v[208:211], a[40:43], a[152:155], v[208:211]\n\t" \
  "ds_read_b64_tr_b16 a[114:115], %[va4] offset:41984\n\t" \
  "v_mfma_f32_16x16x32_bf16 v[212:215], a[40:43], a[156:159], v[212:215]\n\t" \
  "ds_read_b64_tr_b16 a[116:117], %[va5] offset:40960\n\t" \
  "s_add_u32 %[st], %[wa], %[wb]\n\t" \
  "v_mfma_f32_16x16x32_bf16 v[216:219], a[44:47], a[152:155], v[216:219]\n\t" \
  "ds_read_b64_tr_b16 a[118:119], %[va5] offset:41984\n\t" \
  "v_mfma_f32_16x16x32_bf16 v[220:223], a[44:47], a[156:159], v[220:223]\n\t" \
  "ds_read_b64_tr_b16 a[120:121], %[va6] offset:40960\n\t" \
  "buffer_load_dwordx4 %[wv0], %[rq], %[st] offen offset:0 lds\n\t" \
  "v_mfma_f32_16x16x32_bf16 v[224:227], a[48:51], a[152:155], v[224:227]\n\t" \
  "ds_read_b64_tr_b16 a[122:123], %[va6] offset:41984\n\t" \
  "v_mfma_f32_16x16x32_bf16 v[228:231], a[48:51], a[156:159], v[228:231]\n\t" \
  "ds_read_b64_tr_b16 a[124:125], %[va7] offset:40960\n\t" \
  "buffer_load_dwordx4 %[wv1], %[rq], %[st] offen offset:1024 lds\n\t" \
  "v_mfma_f32_16x16x32_bf16 v[232:235], a[52:55], a[152:155], v[232:235]\n\t" \
  "ds_read_b64_tr_b16 a[126:127], %[va7] offset:41984\n\t" \
  "v_mfma_f32_16x16x32_bf16 v[236:239], a[52:55], a[156:159], v[236:239]\n\t" \
  "buffer_load_dwordx4 %[wv2], %[rq], %[st] offen offset:2048 lds\n\t" \
  "v_mfma_f32_16x16x32_bf16 v[240:243], a[56:59], a[152:155], v[240:243]\n\t" \
  "v_mfma_f32_16x16x32_bf16 v[244:247], a[56:59], a[156:159], v[244:247]\n\t" \
  "buffer_load_dwordx4 %[wv3], %[rq], %[st] offen offset:3072 lds\n\t" \
  "v_mfma_f32_16x16x32_bf16 v[248:251], a[60:63], a[152:155], v[248:251]\n\t" \
  "v_mfma_f32_16x16x32_bf16 v[252:255], a[60:63], a[156:159], v[252:255]\n\t" \
  "s_waitcnt vmcnt(16) lgkmcnt(0)\n\t" \
  "s_barrier\n\t" \
  "v_mfma_f32_16x16x32_bf16 v[192:195], a[64:67], a[160:163], v[192:195]\n\t" \
  "ds_read_b64_tr_b16 a[0:1], %[va0] offset:49152\n\t" \
  "ds_read_b64_tr_b16 a[2:3], %[va0] offset:50176\n\t" \
  "s_add_u32 %[wb], %[wb], 0x40000\n\t" \
  "v_mfma_f32_16x16x32_bf16 v[196:199], a[64:67], a[164:167], v[196:199]\n\t" \
  "ds_read_b64_tr_b16 a[4:5], %[va1] offset:49152\n\t" \
  "ds_read_b64_tr_b16 a[6:7], %[va1] offset:50176\n\t" \
  "s_and_b32 %[wb], %[wb], 0xfffff\n\t" \
  "v_mfma_f32_16x16x32_bf16 v[200:203], a[68:71], a[160:163], v[200:203]\n\t" \
  "ds_read_b64_tr_b16 a[8:9], %[va2] offset:49152\n\t" \
  "ds_read_b64_tr_b16 a[10:11], %[va2] offset:50176\n\t" \
  "ds_read_b64_tr_b16 a[12:13], %[va3] offset:49152\n\t" \
  "v_mfma_f32_16x16x32_bf16 v[204:207], a[68:71], a[164:167], v[204:207]\n\t" \
  "ds_read_b64_tr_b16 a[14:15], %[va3] offset:50176\n\t" \
  "ds_read_b64_tr_b16 a[16:17], %[va4] offset:49152\n\t" \
  "s_add_u32 %[wa], %[wa], 0x100000\n\t" \
  "v_mfma_f32_16x16x32_bf16 v[208:211], a[72:75], a[160:163], v[208:211]\n\t" \
  "ds_read_b64_tr_b16 a[18:19], %[va4] offset:50176\n\t" \
  "ds_read_b64_tr_b16 a[20:21], %[va5] offset:49152\n\t" \
  "ds_read_b64_tr_b16 a[22:23], %[va5] offset:50176\n\t" \
  "v_mfma_f32_16x16x32_bf16 v[212:215], a[72:75], a[164:167], v[212:215]\n\t" \
  "ds_read_b64_tr_b16 a[24:25], %[va6] offset:49152\n\t" \
  "ds_read_b64_tr_b16 a[26:27], %[va6] offset:50176\n\t" \
  "s_and_b32 %[wa], %[wa], 0x3fffff\n\t" \
  "v_mfma_f32_16x16x32_bf16 v[216:219], a[76:79], a[160:163], v[216:219]\n\t" \
  "ds_read_b64_tr_b16 a[28:29], %[va7] offset:49152\n\t" \
  "ds_read_b64_tr_b16 a[30:31], %[va7] offset:50176\n\t" \
  "s_add_u32 m0, %[mw], 32768\n\t" \
  "v_mfma_f32_16x16x32_bf16 v[220:223], a[76:79], a[164:167], v[220:223]\n\t" \
  "ds_read_b64_tr_b16 a[32:33], %[va0] offset:57344\n\t" \
  "ds_read_b64_tr_b16 a[34:35], %[va0] offset:58368\n\t" \
  "ds_read_b64_tr_b16 a[36:37], %[va1] offset:57344\n\t" \
  "v_mfma_f32_16x16x32_bf16 v[224:227], a[80:83], a[160:163], v[224:227]\n\t" \
  "ds_read_b64_tr_b16 a[38:39], %[va1] offset:58368\n\t" \
  "ds_read_b64_tr_b16 a[40:41], %[va2] offset:57344\n\t" \
  "s_add_u32 %[st], %[wa], %[wb]\n\t" \
  "v_mfma_f32_16x16x32_bf16 v[228:231], a[80:83], a[164:167], v[228:231]\n\t" \
  "ds_read_b64_tr_b16 a[42:43], %[va2] offset:58368\n\t" \
  "ds_read_b64_tr_b16 a[44:45], %[va3] offset:57344\n\t" \
  "ds_read_b64_tr_b16 a[46:47], %[va3] offset:58368\n\t" \
  "v_mfma_f32_16x16x32_bf16 v[232:235], a[84:87], a[160:163], v[232:235]\n\t" \
  "ds_read_b64_tr_b16 a[48:49], %[va4] offset:57344\n\t" \
  "buffer_load_dwordx4 %[wv0], %[rq], %[st] offen offset:0 lds\n\t" \
  "v_mfma_f32_16x16x32_bf16 v[236:239], a[84:87], a[164:167], v[236:239]\n\t" \
  "ds_read_b64_tr_b16 a[50:51], %[va4] offset:58368\n\t" \
  "ds_read_b64_tr_b16 a[52:53], %[va5] offset:57344\n\t" \
  "v_mfma_f32_16x16x32_bf16 v[240:243], a[88:91], a[160:163], v[240:243]\n\t" \
  "ds_read_b64_tr_b16 a[54:55], %[va5] offset:58368\n\t" \
  "buffer_load_dwordx4 %[wv1], %[rq], %[st] offen offset:1024 lds\n\t" \
  "v_mfma_f32_16x16x32_bf16 v[244:247], a[88:91], a[164:167], v[244:247]\n\t" \
  "ds_read_b64_tr_b16 a[56:57], %[va6] offset:57344\n\t" \
  "buffer_load_dwordx4 %[wv2], %[rq], %[st] offen offset:2048 lds\n\t" \
  "v_mfma_f32_16x16x32_bf16 v[248:251], a[92:95], a[160:163], v[248:251]\n\t" \
  "ds_read_b64_tr_b16 a[58:59], %[va6] offset:58368\n\t" \
  "ds_read_b64_tr_b16 a[60:61], %[va7] offset:57344\n\t" \
  "v_mfma_f32_16x16x32_bf16 v[252:255], a[92:95], a[164:167], v[252:255]\n\t" \
  "ds_read_b64_tr_b16 a[62:63], %[va7] offset:58368\n\t" \
  "buffer_load_dwordx4 %[wv3], %[rq], %[st] offen offset:3072 lds\n\t" \
  "v_mfma_f32_16x16x32_bf16 v[192:195], a[96:99], a[168:171], v[192:195]\n\t" \
  "ds_read_b128 a[192:195], %[xa0] offset:0\n\t" \
  "ds_read_b128 a[196:199], %[xa0] offset:4096\n\t" \
  "v_mfma_f32_16x16x32_bf16 v[196:199], a[96:99], a[172:175], v[196:199]\n\t" \
  "ds_read_b128 a[200:203], %[xa1] offset:0\n\t" \
  "s_add_u32 %[wb], %[wb], 0x40000\n\t" \
  "v_mfma_f32_16x16x32_bf16 v[200:203], a[100:103], a[168:171], v[200:203]\n\t" \
  "ds_read_b128 a[204:207], %[xa1] offset:4096\n\t" \
  "s_and_b32 %[wb], %[wb], 0xfffff\n\t" \
  "v_mfma_f32_16x16x32_bf16 v[204:207], a[100:103], a[172:175], v[204:207]\n\t" \
  "ds_read_b128 a[208:211], %[xa2] offset:0\n\t" \
  "ds_read_b128 a[212:215], %[xa2] offset:4096\n\t" \
  "v_mfma_f32_16x16x32_bf16 v[208:211], a[104:107], a[168:171], v[208:211]\n\t" \
  "ds_read_b128 a[216:219], %[xa3] offset:0\n\t" \
  "s_add_u32 m0, %[mw], 40960\n\t" \
  "v_mfma_f32_16x16x32_bf16 v[212:215], a[104:107], a[172:175], v[212:215]\n\t" \
  "ds_read_b128 a[220:223], %[xa3] offset:4096\n\t" \
  "ds_read_b128 a[224:227], %[xa0] offset:16384\n\t" \
  "v_mfma_f32_16x16x32_bf16 v[216:219], a[108:111], a[168:171], v[216:219]\n\t" \
  "ds_read_b128 a[228:231], %[xa0] offset:20480\n\t" \
  "s_add_u32 %[st], %[wa], %[wb]\n\t" \
  "v_mfma_f32_16x16x32_bf16 v[220:223], a[108:111], a[172:175], v[220:223]\n\t" \
  "ds_read_b128 a[232:235], %[xa1] offset:16384\n\t" \
  "ds_read_b128 a[236:239], %[xa1] offset:20480\n\t" \
  "v_mfma_f32_16x16x32_bf16 v[224:227], a[112:115], a[168:171], v[224:227]\n\t" \
  "ds_read_b128 a[240:243], %[xa2] offset:16384\n\t" \
  "buffer_load_dwordx4 %[wv0], %[rq], %[st] offen offset:0 lds\n\t" \
  "v_mfma_f32_16x16x32_bf16 v[228:231], a[112:115], a[172:175], v[228:231]\n\t" \
  "ds_read_b128 a[244:247], %[xa2] offset:20480\n\t" \
  "buffer_load_dwordx4 %[wv1], %[rq], %[st] offen offset:1024 lds\n\t" \
  "v_mfma_f32_16x16x32_bf16 v[232:235], a[116:119], a[168:171], v[232:235]\n\t" \
  "ds_read_b128 a[248:251], %[xa3] offset:16384\n\t" \
  "ds_read_b128 a[252:255], %[xa3] offset:20480\n\t" \
  "v_mfma_f32_16x16x32_bf16 v[236:239], a[116:119], a[172:175], v[236:239]\n\t" \
  "buffer_load_dwordx4 %[wv2], %[rq], %[st] offen offset:2048 lds\n\t" \
  "v_mfma_f32_16x16x32_bf16 v[240:243], a[120:123], a[168:171], v[240:243]\n\t" \
  "v_mfma_f32_16x16x32_bf16 v[244:247], a[120:123], a[172:175], v[244:247]\n\t" \
  "buffer_load_dwordx4 %[wv3], %[rq], %[st] offen offset:3072 lds\n\t" \
  "v_mfma_f32_16x16x32_bf16 v[248:251], a[124:127], a[168:171], v[248:251]\n\t" \
  "v_mfma_f32_16x16x32_bf16 v[252:255], a[124:127], a[172:175], v[252:255]\n\t" \
  "s_waitcnt vmcnt(16) lgkmcnt(0)\n\t" \
  "s_barrier\n\t" \
  "v_mfma_f32_16x16x32_bf16 v[192:195], a[0:3], a[176:179], v[192:195]\n\t" \
  "ds_read_b64_tr_b16 a[64:65], %[va0] offset:0\n\t" \
  "s_add_u32 %[wb], %[wb], 0x40000\n\t" \
  "v_mfma_f32_16x16x32_bf16 v[196:199], a[0:3], a[180:183], v[196:199]\n\t" \
  "ds_read_b64_tr_b16 a[66:67], %[va0] offset:1024\n\t" \
  "s_and_b32 %[wb], %[wb], 0xfffff\n\t" \
  "v_mfma_f32_16x16x32_bf16 v[200:203], a[4:7], a[176:179], v[200:203]\n\t" \
  "ds_read_b64_tr_b16 a[68:69], %[va1] offset:0\n\t" \
  "ds_read_b64_tr_b16 a[70:71], %[va1] offset:1024\n\t" \
  "v_mfma_f32_16x16x32_bf16 v[204:207], a[4:7], a[180:183], v[204:207]\n\t" \
  "ds_read_b64_tr_b16 a[72:73], %[va2] offset:0\n\t" \
  "s_add_u32 m0, %[mw], 49152\n\t" \
  "v_mfma_f32_16x16x32_bf16 v[208:211], a[8:11], a[176:179], v[208:211]\n\t" \
  "ds_read_b64_tr_b16 a[74:75], %[va2] offset:1024\n\t" \
  "v_mfma_f32_16x16x32_bf16 v[212:215], a[8:11], a[180:183], v[212:215]\n\t" \
  "ds_read_b64_tr_b16 a[76:77], %[va3] offset:0\n\t" \
  "s_add_u32 %[st], %[wa], %[wb]\n\t" \
  "v_mfma_f32_16x16x32_bf16 v[216:219], a[12:15], a[176:179], v[216:219]\n\t" \
  "ds_read_b64_tr_b16 a[78:79], %[va3] offset:1024\n\t" \
  "ds_read_b64_tr_b16 a[80:81], %[va4] offset:0\n\t" \
  "v_mfma_f32_16x16x32_bf16 v[220:223], a[12:15], a[180:183], v[220:223]\n\t" \
  "ds_read_b64_tr_b16 a[82:83], %[va4] offset:1024\n\t" \
  "buffer_load_dwordx4 %[wv0], %[rq], %[st] offen offset:0 lds\n\t" \
  "v_mfma_f32_16x16x32_bf16 v[224:227], a[16:19], a[176:179], v[224:227]\n\t" \
  "ds_read_b64_tr_b16 a[84:85], %[va5] offset:0\n\t" \
  "v_mfma_f32_16x16x32_bf16 v[228:231], a[16:19], a[180:183], v[228:231]\n\t" \
  "ds_read_b64_tr_b16 a[86:87], %[va5] offset:1024\n\t" \
  "buffer_load_dwordx4 %[wv1], %[rq], %[st] offen offset:1024 lds\n\t" \
  "v_mfma_f32_16x16x32_bf16 v[232:235], a[20:23], a[176:179], v[232:235]\n\t" \
  "ds_read_b64_tr_b16 a[88:89], %[va6] offset:0\n\t" \
  "ds_read_b64_tr_b16 a[90:91], %[va6] offset:1024\n\t" \
  "v_mfma_f32_16x16x32_bf16 v[236:239], a[20:23], a[180:183], v[236:239]\n\t" \
  "ds_read_b64_tr_b16 a[92:93], %[va7] offset:0\n\t" \
  "buffer_load_dwordx4 %[wv2], %[rq], %[st] offen offset:2048 lds\n\t" \
  "v_mfma_f32_16x16x32_bf16 v[240:243], a[24:27], a[176:179], v[240:243]\n\t" \
  "ds_read_b64_tr_b16 a[94:95], %[va7] offset:1024\n\t" \
  "v_mfma_f32_16x16x32_bf16 v[244:247], a[24:27], a[180:183], v[244:247]\n\t" \
  "ds_read_b64_tr_b16 a[96:97], %[va0] offset:8192\n\t" \
  "buffer_load_dwordx4 %[wv3], %[rq], %[st] offen offset:3072 lds\n\t" \
  "v_mfma_f32_16x16x32_bf16 v[248:251], a[28:31], a[176:179], v[248:251]\n\t" \
  "ds_read_b64_tr_b16 a[98:99], %[va0] offset:9216\n\t" \
  "ds_read_b64_tr_b16 a[100:101], %[va1] offset:8192\n\t" \
  "v_mfma_f32_16x16x32_bf16 v[252:255], a[28:31], a[180:183], v[252:255]\n\t" \
  "ds_read_b64_tr_b16 a[102:103], %[va1] offset:9216\n\t" \
  "s_add_u32 %[wb], %[wb], 0x40000\n\t" \
  "v_mfma_f32_16x16x32_bf16 v[192:195], a[32:35], a[184:187], v[192:195]\n\t" \
  "ds_read_b64_tr_b16 a[104:105], %[va2] offset:8192\n\t" \
  "v_mfma_f32_16x16x32_bf16 v[196:199], a[32:35], a[188:191], v[196:199]\n\t" \
  "ds_read_b64_tr_b16 a[106:107], %[va2] offset:9216\n\t" \
  "s_and_b32 %[wb], %[wb], 0xfffff\n\t" \
  "v_mfma_f32_16x16x32_bf16 v[200:203], a[36:39], a[184:187], v[200:203]\n\t" \
  "ds_read_b64_tr_b16 a[108:109], %[va3] offset:8192\n\t" \
  "ds_read_b64_tr_b16 a[110:111], %[va3] offset:9216\n\t" \
  "v_mfma_f32_16x16x32_bf16 v[204:207], a[36:39], a[188:191], v[204:207]\n\t" \
  "ds_read_b64_tr_b16 a[112:113], %[va4] offset:8192\n\t" \
  "s_add_u32 m0, %[mw], 57344\n\t" \
  "v_mfma_f32_16x16x32_bf16 v[208:211], a[40:43], a[184:187], v[208:211]\n\t" \
  "ds_read_b64_tr_b16 a[114:115], %[va4] offset:9216\n\t" \
  "v_mfma_f32_16x16x32_bf16 v[212:215], a[40:43], a[188:191], v[212:215]\n\t" \
  "ds_read_b64_tr_b16 a[116:117], %[va5] offset:8192\n\t" \
  "s_add_u32 %[st], %[wa], %[wb]\n\t" \
  "v_mfma_f32_16x16x32_bf16 v[216:219], a[44:47], a[184:187], v[216:219]\n\t" \
  "ds_read_b64_tr_b16 a[118:119], %[va5] offset:9216\n\t" \
  "v_mfma_f32_16x16x32_bf16 v[220:223], a[44:47], a[188:191], v[220:223]\n\t" \
  "ds_read_b64_tr_b16 a[120:121], %[va6] offset:8192\n\t" \
  "buffer_load_dwordx4 %[wv0], %[rq], %[st] offen offset:0 lds\n\t" \
  "v_mfma_f32_16x16x32_bf16 v[224:227], a[48:51], a[184:187], v[224:227]\n\t" \
  "ds_read_b64_tr_b16 a[122:123], %[va6] offset:9216\n\t" \
  "v_mfma_f32_16x16x32_bf16 v[228:231], a[48:51], a[188:191], v[228:231]\n\t" \
  "ds_read_b64_tr_b16 a[124:125], %[va7] offset:8192\n\t" \
  "buffer_load_dwordx4 %[wv1], %[rq], %[st] offen offset:1024 lds\n\t" \
  "v_mfma_f32_16x16x32_bf16 v[232:235], a[52:55], a[184:187], v[232:235]\n\t" \
  "ds_read_b64_tr_b16 a[126:127], %[va7] offset:9216\n\t" \
  "v_mfma_f32_16x16x32_bf16 v[236:239], a[52:55], a[188:191], v[236:239]\n\t" \
  "buffer_load_dwordx4 %[wv2], %[rq], %[st] offen offset:2048 lds\n\t" \
  "v_mfma_f32_16x16x32_bf16 v[240:243], a[56:59], a[184:187], v[240:243]\n\t" \
  "v_mfma_f32_16x16x32_bf16 v[244:247], a[56:59], a[188:191], v[244:247]\n\t" \
  "buffer_load_dwordx4 %[wv3], %[rq], %[st] offen offset:3072 lds\n\t" \
  "v_mfma_f32_16x16x32_bf16 v[248:251], a[60:63], a[184:187], v[248:251]\n\t" \
  "v_mfma_f32_16x16x32_bf16 v[252:255], a[60:63], a[188:191], v[252:255]\n\t" \
  "s_waitcnt vmcnt(16) lgkmcnt(0)\n\t" \
  "s_barrier\n\t" \
  "v_mfma_f32_16x16x32_bf16 v[192:195], a[64:67], a[192:195], v[192:195]\n\t" \
  "ds_read_b64_tr_b16 a[0:1], %[va0] offset:16384\n\t" \
  "s_add_u32 m0, %[mkv], 0\n\t" \
  "v_mfma_f32_16x16x32_bf16 v[196:199], a[64:67], a[196:199], v[196:199]\n\t" \
  "ds_read_b64_tr_b16 a[2:3], %[va0] offset:17408\n\t" \
  "ds_read_b64_tr_b16 a[4:5], %[va1] offset:16384\n\t" \
  "v_mfma_f32_16x16x32_bf16 v[200:203], a[68:71], a[192:195], v[200:203]\n\t" \
  "ds_read_b64_tr_b16 a[6:7], %[va1] offset:17408\n\t" \
  "v_mfma_f32_16x16x32_bf16 v[204:207], a[68:71], a[196:199], v[204:207]\n\t" \
  "ds_read_b64_tr_b16 a[8:9], %[va2] offset:16384\n\t" \
  "ds_read_b64_tr_b16 a[10:11], %[va2] offset:17408\n\t" \
  "v_mfma_f32_16x16x32_bf16 v[208:211], a[72:75], a[192:195], v[208:211]\n\t" \
  "ds_read_b64_tr_b16 a[12:13], %[va3] offset:16384\n\t" \
  "v_mfma_f32_16x16x32_bf16 v[212:215], a[72:75], a[196:199], v[212:215]\n\t" \
  "ds_read_b64_tr_b16 a[14:15], %[va3] offset:17408\n\t" \
  "s_add_u32 %[sk], %[sk], 0x2000\n\t" \
  "v_mfma_f32_16x16x32_bf16 v[216:219], a[76:79], a[192:195], v[216:219]\n\t" \
  "ds_read_b64_tr_b16 a[16:17], %[va4] offset:16384\n\t" \
  "ds_read_b64_tr_b16 a[18:19], %[va4] offset:17408\n\t" \
  "v_mfma_f32_16x16x32_bf16 v[220:223], a[76:79], a[196:199], v[220:223]\n\t" \
  "ds_read_b64_tr_b16 a[20:21], %[va5] offset:16384\n\t" \
  "v_mfma_f32_16x16x32_bf16 v[224:227], a[80:83], a[192:195], v[224:227]\n\t" \
  "ds_read_b64_tr_b16 a[22:23], %[va5] offset:17408\n\t" \
  "ds_read_b64_tr_b16 a[24:25], %[va6] offset:16384\n\t" \
  "v_mfma_f32_16x16x32_bf16 v[228:231], a[80:83], a[196:199], v[228:231]\n\t" \
  "ds_read_b64_tr_b16 a[26:27], %[va6] offset:17408\n\t" \
  "v_mfma_f32_16x16x32_bf16 v[232:235], a[84:87], a[192:195], v[232:235]\n\t" \
  "ds_read_b64_tr_b16 a[28:29], %[va7] offset:16384\n\t" \
  "buffer_load_dwordx4 %[kv0], %[rk], %[sk] offen offset:0 nt lds\n\t" \
  "v_mfma_f32_16x16x32_bf16 v[236:239], a[84:87], a[196:199], v[236:239]\n\t" \
  "ds_read_b64_tr_b16 a[30:31], %[va7] offset:17408\n\t" \
  "ds_read_b64_tr_b16 a[32:33], %[va0] offset:24576\n\t" \
  "v_mfma_f32_16x16x32_bf16 v[240:243], a[88:91], a[192:195], v[240:243]\n\t" \
  "ds_read_b64_tr_b16 a[34:35], %[va0] offset:25600\n\t" \
  "v_mfma_f32_16x16x32_bf16 v[244:247], a[88:91], a[196:199], v[244:247]\n\t" \
  "ds_read_b64_tr_b16 a[36:37], %[va1] offset:24576\n\t" \
  "v_mfma_f32_16x16x32_bf16 v[248:251], a[92:95], a[192:195], v[248:251]\n\t" \
  "ds_read_b64_tr_b16 a[38:39], %[va1] offset:25600\n\t" \
  "v_mfma_f32_16x16x32_bf16 v[252:255], a[92:95], a[196:199], v[252:255]\n\t" \
  "ds_read_b64_tr_b16 a[40:41], %[va2] offset:24576\n\t" \
  "buffer_load_dwordx4 %[kv0], %[rk], %[sk] offen offset:1024 nt lds\n\t" \
  "v_mfma_f32_16x16x32_bf16 v[192:195], a[96:99], a[200:203], v[192:195]\n\t" \
  "ds_read_b64_tr_b16 a[42:43], %[va2] offset:25600\n\t" \
  "v_mfma_f32_16x16x32_bf16 v[196:199], a[96:99], a[204:207], v[196:199]\n\t" \
  "ds_read_b64_tr_b16 a[44:45], %[va3] offset:24576\n\t" \
  "v_mfma_f32_16x16x32_bf16 v[200:203], a[100:103], a[200:203], v[200:203]\n\t" \
  "ds_read_b64_tr_b16 a[46:47], %[va3] offset:25600\n\t" \
  "v_mfma_f32_16x16x32_bf16 v[204:207], a[100:103], a[204:207], v[204:207]\n\t" \
  "ds_read_b64_tr_b16 a[48:49], %[va4] offset:24576\n\t" \
  "v_mfma_f32_16x16x32_bf16 v[208:211], a[104:107], a[200:203], v[208:211]\n\t" \
  "ds_read_b64_tr_b16 a[50:51], %[va4] offset:25600\n\t" \
  "buffer_load_dwordx4 %[kv1], %[rk], %[sk] offen offset:2048 nt lds\n\t" \
  "v_mfma_f32_16x16x32_bf16 v[212:215], a[104:107], a[204:207], v[212:215]\n\t" \
  "ds_read_b64_tr_b16 a[52:53], %[va5] offset:24576\n\t" \
  "v_mfma_f32_16x16x32_bf16 v[216:219], a[108:111], a[200:203], v[216:219]\n\t" \
  "ds_read_b64_tr_b16 a[54:55], %[va5] offset:25600\n\t" \
  "v_mfma_f32_16x16x32_bf16 v[220:223], a[108:111], a[204:207], v[220:223]\n\t" \
  "ds_read_b64_tr_b16 a[56:57], %[va6] offset:24576\n\t" \
  "v_mfma_f32_16x16x32_bf16 v[224:227], a[112:115], a[200:203], v[224:227]\n\t" \
  "ds_read_b64_tr_b16 a[58:59], %[va6] offset:25600\n\t" \
  "v_mfma_f32_16x16x32_bf16 v[228:231], a[112:115], a[204:207], v[228:231]\n\t" \
  "ds_read_b64_tr_b16 a[60:61], %[va7] offset:24576\n\t" \
  "buffer_load_dwordx4 %[kv1], %[rk], %[sk] offen offset:3072 nt lds\n\t" \
  "v_mfma_f32_16x16x32_bf16 v[232:235], a[116:119], a[200:203], v[232:235]\n\t" \
  "ds_read_b64_tr_b16 a[62:63], %[va7] offset:25600\n\t" \
  "v_mfma_f32_16x16x32_bf16 v[236:239], a[116:119], a[204:207], v[236:239]\n\t" \
  "v_mfma_f32_16x16x32_bf16 v[240:243], a[120:123], a[200:203], v[240:243]\n\t" \
  "v_mfma_f32_16x16x32_bf16 v[244:247], a[120:123], a[204:207], v[244:247]\n\t" \
  "v_mfma_f32_16x16x32_bf16 v[248:251], a[124:127], a[200:203], v[248:251]\n\t" \
  "v_mfma_f32_16x16x32_bf16 v[252:255], a[124:127], a[204:207], v[252:255]\n\t" \
  "s_waitcnt vmcnt(12) lgkmcnt(0)\n\t" \
  "s_barrier\n\t" \
  "v_mfma_f32_16x16x32_bf16 v[192:195], a[0:3], a[208:211], v[192:195]\n\t" \
  "ds_read_b64_tr_b16 a[64:65], %[va0] offset:32768\n\t" \
  "s_add_u32 m0, %[mkv], 16384\n\t" \
  "v_mfma_f32_16x16x32_bf16 v[196:199], a[0:3], a[212:215], v[196:199]\n\t" \
  "ds_read_b64_tr_b16 a[66:67], %[va0] offset:33792\n\t" \
  "ds_read_b64_tr_b16 a[68:69], %[va1] offset:32768\n\t" \
  "v_mfma_f32_16x16x32_bf16 v[200:203], a[4:7], a[208:211], v[200:203]\n\t" \
  "ds_read_b64_tr_b16 a[70:71], %[va1] offset:33792\n\t" \
  "s_add_u32 %[sk], %[sk], 0x2000\n\t" \
  "v_mfma_f32_16x16x32_bf16 v[204:207], a[4:7], a[212:215], v[204:207]\n\t" \
  "ds_read_b64_tr_b16 a[72:73], %[va2] offset:32768\n\t" \
  "v_mfma_f32_16x16x32_bf16 v[208:211], a[8:11], a[208:211], v[208:211]\n\t" \
  "ds_read_b64_tr_b16 a[74:75], %[va2] offset:33792\n\t" \
  "ds_read_b64_tr_b16 a[76:77], %[va3] offset:32768\n\t" \
  "v_mfma_f32_16x16x32_bf16 v[212:215], a[8:11], a[212:215], v[212:215]\n\t" \
  "ds_read_b64_tr_b16 a[78:79], %[va3] offset:33792\n\t" \
  "buffer_load_dwordx4 %[kv0], %[rk], %[sk] offen offset:0 nt lds\n\t" \
  "v_mfma_f32_16x16x32_bf16 v[216:219], a[12:15], a[208:211], v[216:219]\n\t" \
  "ds_read_b64_tr_b16 a[80:81], %[va4] offset:32768\n\t" \
  "v_mfma_f32_16x16x32_bf16 v[220:223], a[12:15], a[212:215], v[220:223]\n\t" \
  "ds_read_b64_tr_b16 a[82:83], %[va4] offset:33792\n\t" \
  "buffer_load_dwordx4 %[kv0], %[rk], %[sk] offen offset:1024 nt lds\n\t" \
  "v_mfma_f32_16x16x32_bf16 v[224:227], a[16:19], a[208:211], v[224:227]\n\t" \
  "ds_read_b64_tr_b16 a[84:85], %[va5] offset:32768\n\t" \
  "ds_read_b64_tr_b16 a[86:87], %[va5] offset:33792\n\t" \
  "v_mfma_f32_16x16x32_bf16 v[228:231], a[16:19], a[212:215], v[228:231]\n\t" \
  "ds_read_b64_tr_b16 a[88:89], %[va6] offset:32768\n\t" \
  "v_mfma_f32_16x16x32_bf16 v[232:235], a[20:23], a[208:211], v[232:235]\n\t" \
  "ds_read_b64_tr_b16 a[90:91], %[va6] offset:33792\n\t" \
  "buffer_load_dwordx4 %[kv1], %[rk], %[sk] offen offset:2048 nt lds\n\t" \
  "v_mfma_f32_16x16x32_bf16 v[236:239], a[20:23], a[212:215], v[236:239]\n\t" \
  "ds_read_b64_tr_b16 a[92:93], %[va7] offset:32768\n\t" \
  "ds_read_b64_tr_b16 a[94:95], %[va7] offset:33792\n\t" \
  "v_mfma_f32_16x16x32_bf16 v[240:243], a[24:27], a[208:211], v[240:243]\n\t" \
  "ds_read_b64_tr_b16 a[96:97], %[va0] offset:40960\n\t" \
  "buffer_load_dwordx4 %[kv1], %[rk], %[sk] offen offset:3072 nt lds\n\t" \
  "v_mfma_f32_16x16x32_bf16 v[244:247], a[24:27], a[212:215], v[244:247]\n\t" \
  "ds_read_b64_tr_b16 a[98:99], %[va0] offset:41984\n\t" \
  "v_mfma_f32_16x16x32_bf16 v[248:251], a[28:31], a[208:211], v[248:251]\n\t" \
  "ds_read_b64_tr_b16 a[100:101], %[va1] offset:40960\n\t" \
  "ds_read_b64_tr_b16 a[102:103], %[va1] offset:41984\n\t" \
  "v_mfma_f32_16x16x32_bf16 v[252:255], a[28:31], a[212:215], v[252:255]\n\t" \
  "ds_read_b64_tr_b16 a[104:105], %[va2] offset:40960\n\t" \
  "s_add_u32 m0, %[mkv], 24576\n\t" \
  "v_mfma_f32_16x16x32_bf16 v[192:195], a[32:35], a[216:219], v[192:195]\n\t" \
  "ds_read_b64_tr_b16 a[106:107], %[va2] offset:41984\n\t" \
  "v_mfma_f32_16x16x32_bf16 v[196:199], a[32:35], a[220:223], v[196:199]\n\t" \
  "ds_read_b64_tr_b16 a[108:109], %[va3] offset:40960\n\t" \
  "v_mfma_f32_16x16x32_bf16 v[200:203], a[36:39], a[216:219], v[200:203]\n\t" \
  "ds_read_b64_tr_b16 a[110:111], %[va3] offset:41984\n\t" \
  "s_add_u32 %[sv], %[sv], 0x2000\n\t" \
  "v_mfma_f32_16x16x32_bf16 v[204:207], a[36:39], a[220:223], v[204:207]\n\t" \
  "ds_read_b64_tr_b16 a[112:113], %[va4] offset:40960\n\t" \
  "v_mfma_f32_16x16x32_bf16 v[208:211], a[40:43], a[216:219], v[208:211]\n\t" \
  "ds_read_b64_tr_b16 a[114:115], %[va4] offset:41984\n\t" \
  "buffer_load_dwordx4 %[vv0], %[rv], %[sv] offen offset:0 nt lds\n\t" \
  "v_mfma_f32_16x16x32_bf16 v[212:215], a[40:43], a[220:223], v[212:215]\n\t" \
  "ds_read_b64_tr_b16 a[116:117], %[va5] offset:40960\n\t" \
  "v_mfma_f32_16x16x32_bf16 v[216:219], a[44:47], a[216:219], v[216:219]\n\t" \
  "ds_read_b64_tr_b16 a[118:119], %[va5] offset:41984\n\t" \
  "v_mfma_f32_16x16x32_bf16 v[220:223], a[44:47], a[220:223], v[220:223]\n\t" \
  "ds_read_b64_tr_b16 a[120:121], %[va6] offset:40960\n\t" \
  "buffer_load_dwordx4 %[vv0], %[rv], %[sv] offen offset:1024 nt lds\n\t" \
  "v_mfma_f32_16x16x32_bf16 v[224:227], a[48:51], a[216:219], v[224:227]\n\t" \
  "ds_read_b64_tr_b16 a[122:123], %[va6] offset:41984\n\t" \
  "v_mfma_f32_16x16x32_bf16 v[228:231], a[48:51], a[220:223], v[228:231]\n\t" \
  "ds_read_b64_tr_b16 a[124:125], %[va7] offset:40960\n\t" \
  "buffer_load_dwordx4 %[vv1], %[rv], %[sv] offen offset:2048 nt lds\n\t" \
  "v_mfma_f32_16x16x32_bf16 v[232:235], a[52:55], a[216:219], v[232:235]\n\t" \
  "ds_read_b64_tr_b16 a[126:127], %[va7] offset:41984\n\t" \
  "v_mfma_f32_16x16x32_bf16 v[236:239], a[52:55], a[220:223], v[236:239]\n\t" \
  "v_mfma_f32_16x16x32_bf16 v[240:243], a[56:59], a[216:219], v[240:243]\n\t" \
  "buffer_load_dwordx4 %[vv1], %[rv], %[sv] offen offset:3072 nt lds\n\t" \
  "v_mfma_f32_16x16x32_bf16 v[244:247], a[56:59], a[220:223], v[244:247]\n\t" \
  "v_mfma_f32_16x16x32_bf16 v[248:251], a[60:63], a[216:219], v[248:251]\n\t" \
  "v_mfma_f32_16x16x32_bf16 v[252:255], a[60:63], a[220:223], v[252:255]\n\t" \
  "s_waitcnt vmcnt(12) lgkmcnt(0)\n\t" \
  "s_barrier\n\t" \
  "v_mfma_f32_16x16x32_bf16 v[192:195], a[64:67], a[224:227], v[192:195]\n\t" \
  "ds_read_b64_tr_b16 a[0:1], %[va0] offset:49152\n\t" \
  "s_add_u32 m0, %[mkv], 32768\n\t" \
  "v_mfma_f32_16x16x32_bf16 v[196:199], a[64:67], a[228:231], v[196:199]\n\t" \
  "ds_read_b64_tr_b16 a[2:3], %[va0] offset:50176\n\t" \
  "ds_read_b64_tr_b16 a[4:5], %[va1] offset:49152\n\t" \
  "v_mfma_f32_16x16x32_bf16 v[200:203], a[68:71], a[224:227], v[200:203]\n\t" \
  "ds_read_b64_tr_b16 a[6:7], %[va1] offset:50176\n\t" \
  "s_add_u32 %[sk], %[sk], 0x2000\n\t" \
  "v_mfma_f32_16x16x32_bf16 v[204:207], a[68:71], a[228:231], v[204:207]\n\t" \
  "ds_read_b64_tr_b16 a[8:9], %[va2] offset:49152\n\t" \
  "v_mfma_f32_16x16x32_bf16 v[208:211], a[72:75], a[224:227], v[208:211]\n\t" \
  "ds_read_b64_tr_b16 a[10:11], %[va2] offset:50176\n\t" \
  "ds_read_b64_tr_b16 a[12:13], %[va3] offset:49152\n\t" \
  "v_mfma_f32_16x16x32_bf16 v[212:215], a[72:75], a[228:231], v[212:215]\n\t" \
  "ds_read_b64_tr_b16 a[14:15], %[va3] offset:50176\n\t" \
  "buffer_load_dwordx4 %[kv0], %[rk], %[sk] offen offset:0 nt lds\n\t" \
  "v_mfma_f32_16x16x32_bf16 v[216:219], a[76:79], a[224:227], v[216:219]\n\t" \
  "ds_read_b64_tr_b16 a[16:17], %[va4] offset:49152\n\t" \
  "v_mfma_f32_16x16x32_bf16 v[220:223], a[76:79], a[228:231], v[220:223]\n\t" \
  "ds_read_b64_tr_b16 a[18:19], %[va4] offset:50176\n\t" \
  "buffer_load_dwordx4 %[kv0], %[rk], %[sk] offen offset:1024 nt lds\n\t" \
  "v_mfma_f32_16x16x32_bf16 v[224:227], a[80:83], a[224:227], v[224:227]\n\t" \
  "ds_read_b64_tr_b16 a[20:21], %[va5] offset:49152\n\t" \
  "ds_read_b64_tr_b16 a[22:23], %[va5] offset:50176\n\t" \
  "v_mfma_f32_16x16x32_bf16 v[228:231], a[80:83], a[228:231], v[228:231]\n\t" \
  "ds_read_b64_tr_b16 a[24:25], %[va6] offset:49152\n\t" \
  "v_mfma_f32_16x16x32_bf16 v[232:235], a[84:87], a[224:227], v[232:235]\n\t" \
  "ds_read_b64_tr_b16 a[26:27], %[va6] offset:50176\n\t" \
  "buffer_load_dwordx4 %[kv1], %[rk], %[sk] offen offset:2048 nt lds\n\t" \
  "v_mfma_f32_16x16x32_bf16 v[236:239], a[84:87], a[228:231], v[236:239]\n\t" \
  "ds_read_b64_tr_b16 a[28:29], %[va7] offset:49152\n\t" \
  "ds_read_b64_tr_b16 a[30:31], %[va7] offset:50176\n\t" \
  "v_mfma_f32_16x16x32_bf16 v[240:243], a[88:91], a[224:227], v[240:243]\n\t" \
  "ds_read_b64_tr_b16 a[32:33], %[va0] offset:57344\n\t" \
  "buffer_load_dwordx4 %[kv1], %[rk], %[sk] offen offset:3072 nt lds\n\t" \
  "v_mfma_f32_16x16x32_bf16 v[244:247], a[88:91], a[228:231], v[244:247]\n\t" \
  "ds_read_b64_tr_b16 a[34:35], %[va0] offset:58368\n\t" \
  "v_mfma_f32_16x16x32_bf16 v[248:251], a[92:95], a[224:227], v[248:251]\n\t" \
  "ds_read_b64_tr_b16 a[36:37], %[va1] offset:57344\n\t" \
  "ds_read_b64_tr_b16 a[38:39], %[va1] offset:58368\n\t" \
  "v_mfma_f32_16x16x32_bf16 v[252:255], a[92:95], a[228:231], v[252:255]\n\t" \
  "ds_read_b64_tr_b16 a[40:41], %[va2] offset:57344\n\t" \
  "s_add_u32 m0, %[mkv], 40960\n\t" \
  "v_mfma_f32_16x16x32_bf16 v[192:195], a[96:99], a[232:235], v[192:195]\n\t" \
  "ds_read_b64_tr_b16 a[42:43], %[va2] offset:58368\n\t" \
  "v_mfma_f32_16x16x32_bf16 v[196:199], a[96:99], a[236:239], v[196:199]\n\t" \
  "ds_read_b64_tr_b16 a[44:45], %[va3] offset:57344\n\t" \
  "v_mfma_f32_16x16x32_bf16 v[200:203], a[100:103], a[232:235], v[200:203]\n\t" \
  "ds_read_b64_tr_b16 a[46:47], %[va3] offset:58368\n\t" \
  "s_add_u32 %[sv], %[sv], 0x2000\n\t" \
  "v_mfma_f32_16x16x32_bf16 v[204:207], a[100:103], a[236:239], v[204:207]\n\t" \
  "ds_read_b64_tr_b16 a[48:49], %[va4] offset:57344\n\t" \
  "v_mfma_f32_16x16x32_bf16 v[208:211], a[104:107], a[232:235], v[208:211]\n\t" \
  "ds_read_b64_tr_b16 a[50:51], %[va4] offset:58368\n\t" \
  "buffer_load_dwordx4 %[vv0], %[rv], %[sv] offen offset:0 nt lds\n\t" \
  "v_mfma_f32_16x16x32_bf16 v[212:215], a[104:107], a[236:239], v[212:215]\n\t" \
  "ds_read_b64_tr_b16 a[52:53], %[va5] offset:57344\n\t" \
  "v_mfma_f32_16x16x32_bf16 v[216:219], a[108:111], a[232:235], v[216:219]\n\t" \
  "ds_read_b64_tr_b16 a[54:55], %[va5] offset:58368\n\t" \
  "v_mfma_f32_16x16x32_bf16 v[220:223], a[108:111], a[236:239], v[220:223]\n\t" \
  "ds_read_b64_tr_b16 a[56:57], %[va6] offset:57344\n\t" \
  "buffer_load_dwordx4 %[vv0], %[rv], %[sv] offen offset:1024 nt lds\n\t" \
  "v_mfma_f32_16x16x32_bf16 v[224:227], a[112:115], a[232:235], v[224:227]\n\t" \
  "ds_read_b64_tr_b16 a[58:59], %[va6] offset:58368\n\t" \
  "v_mfma_f32_16x16x32_bf16 v[228:231], a[112:115], a[236:239], v[228:231]\n\t" \
  "ds_read_b64_tr_b16 a[60:61], %[va7] offset:57344\n\t" \
  "buffer_load_dwordx4 %[vv1], %[rv], %[sv] offen offset:2048 nt lds\n\t" \
  "v_mfma_f32_16x16x32_bf16 v[232:235], a[116:119], a[232:235], v[232:235]\n\t" \
  "ds_read_b64_tr_b16 a[62:63], %[va7] offset:58368\n\t" \
  "v_mfma_f32_16x16x32_bf16 v[236:239], a[116:119], a[236:239], v[236:239]\n\t" \
  "v_mfma_f32_16x16x32_bf16 v[240:243], a[120:123], a[232:235], v[240:243]\n\t" \
  "buffer_load_dwordx4 %[vv1], %[rv], %[sv] offen offset:3072 nt lds\n\t" \
  "v_mfma_f32_16x16x32_bf16 v[244:247], a[120:123], a[236:239], v[244:247]\n\t" \
  "v_mfma_f32_16x16x32_bf16 v[248:251], a[124:127], a[232:235], v[248:251]\n\t" \
  "v_mfma_f32_16x16x32_bf16 v[252:255], a[124:127], a[236:239], v[252:255]\n\t" \
  "s_waitcnt lgkmcnt(0)\n\t" \
  "s_barrier\n\t" \
  "v_mfma_f32_16x16x32_bf16 v[192:195], a[0:3], a[240:243], v[192:195]\n\t" \
  "s_add_u32 m0, %[mkv], 49152\n\t" \
  "v_mfma_f32_16x16x32_bf16 v[196:199], a[0:3], a[244:247], v[196:199]\n\t" \
  "v_mfma_f32_16x16x32_bf16 v[200:203], a[4:7], a[240:243], v[200:203]\n\t" \
  "s_add_u32 %[sk], %[sk], 0x2000\n\t" \
  "v_mfma_f32_16x16x32_bf16 v[204:207], a[4:7], a[244:247], v[204:207]\n\t" \
  "v_mfma_f32_16x16x32_bf16 v[208:211], a[8:11], a[240:243], v[208:211]\n\t" \
  "v_mfma_f32_16x16x32_bf16 v[212:215], a[8:11], a[244:247], v[212:215]\n\t" \
  "buffer_load_dwordx4 %[kv0], %[rk], %[sk] offen offset:0 nt lds\n\t" \
  "v_mfma_f32_16x16x32_bf16 v[216:219], a[12:15], a[240:243], v[216:219]\n\t" \
  "v_mfma_f32_16x16x32_bf16 v[220:223], a[12:15], a[244:247], v[220:223]\n\t" \
  "buffer_load_dwordx4 %[kv0], %[rk], %[sk] offen offset:1024 nt lds\n\t" \
  "v_mfma_f32_16x16x32_bf16 v[224:227], a[16:19], a[240:243], v[224:227]\n\t" \
  "v_mfma_f32_16x16x32_bf16 v[228:231], a[16:19], a[244:247], v[228:231]\n\t" \
  "v_mfma_f32_16x16x32_bf16 v[232:235], a[20:23], a[240:243], v[232:235]\n\t" \
  "buffer_load_dwordx4 %[kv1], %[rk], %[sk] offen offset:2048 nt lds\n\t" \
  "v_mfma_f32_16x16x32_bf16 v[236:239], a[20:23], a[244:247], v[236:239]\n\t" \
  "v_mfma_f32_16x16x32_bf16 v[240:243], a[24:27], a[240:243], v[240:243]\n\t" \
  "buffer_load_dwordx4 %[kv1], %[rk], %[sk] offen offset:3072 nt lds\n\t" \
  "v_mfma_f32_16x16x32_bf16 v[244:247], a[24:27], a[244:247], v[244:247]\n\t" \
  "v_mfma_f32_16x16x32_bf16 v[248:251], a[28:31], a[240:243], v[248:251]\n\t" \
  "v_mfma_f32_16x16x32_bf16 v[252:255], a[28:31], a[244:247], v[252:255]\n\t" \
  "s_add_u32 m0, %[mkv], 57344\n\t" \
  "v_mfma_f32_16x16x32_bf16 v[192:195], a[32:35], a[248:251], v[192:195]\n\t" \
  "v_mfma_f32_16x16x32_bf16 v[196:199], a[32:35], a[252:255], v[196:199]\n\t" \
  "v_mfma_f32_16x16x32_bf16 v[200:203], a[36:39], a[248:251], v[200:203]\n\t" \
  "s_add_u32 %[sv], %[sv], 0x2000\n\t" \
  "v_mfma_f32_16x16x32_bf16 v[204:207], a[36:39], a[252:255], v[204:207]\n\t" \
  "v_mfma_f32_16x16x32_bf16 v[208:211], a[40:43], a[248:251], v[208:211]\n\t" \
  "buffer_load_dwordx4 %[vv0], %[rv], %[sv] offen offset:0 nt lds\n\t" \
  "v_mfma_f32_16x16x32_bf16 v[212:215], a[40:43], a[252:255], v[212:215]\n\t" \
  "v_mfma_f32_16x16x32_bf16 v[216:219], a[44:47], a[248:251], v[216:219]\n\t" \
  "v_mfma_f32_16x16x32_bf16 v[220:223], a[44:47], a[252:255], v[220:223]\n\t" \
  "buffer_load_dwordx4 %[vv0], %[rv], %[sv] offen offset:1024 nt lds\n\t" \
  "v_mfma_f32_16x16x32_bf16 v[224:227], a[48:51], a[248:251], v[224:227]\n\t" \
  "v_mfma_f32_16x16x32_bf16 v[228:231], a[48:51], a[252:255], v[228:231]\n\t" \
  "buffer_load_dwordx4 %[vv1], %[rv], %[sv] offen offset:2048 nt lds\n\t" \
  "v_mfma_f32_16x16x32_bf16 v[232:235], a[52:55], a[248:251], v[232:235]\n\t" \
  "v_mfma_f32_16x16x32_bf16 v[236:239], a[52:55], a[252:255], v[236:239]\n\t" \
  "v_mfma_f32_16x16x32_bf16 v[240:243], a[56:59], a[248:251], v[240:243]\n\t" \
  "buffer_load_dwordx4 %[vv1], %[rv], %[sv] offen offset:3072 nt lds\n\t" \
  "v_mfma_f32_16x16x32_bf16 v[244:247], a[56:59], a[252:255], v[244:247]\n\t" \
  "v_mfma_f32_16x16x32_bf16 v[248:251], a[60:63], a[248:251], v[248:251]\n\t" \
  "v_mfma_f32_16x16x32_bf16 v[252:255], a[60:63], a[252:255], v[252:255]\n\t" \
  "s_waitcnt vmcnt(24)\n\t" \
  "s_barrier\n\t" \
  "ds_read_b128 a[64:67], %[ka0] offset:0\n\t" \
  "ds_read_b128 a[80:83], %[ka0] offset:1024\n\t" \
  "ds_read_b128 a[68:71], %[ka1] offset:0\n\t" \
  "ds_read_b128 a[84:87], %[ka1] offset:1024\n\t" \
  "ds_read_b128 a[72:75], %[ka2] offset:0\n\t" \
  "ds_read_b128 a[88:91], %[ka2] offset:1024\n\t" \
  "ds_read_b128 a[76:79], %[ka3] offset:0\n\t" \
  "ds_read_b128 a[92:95], %[ka3] offset:1024\n\t" \
  "s_mov_b32 %[st], 0x3e0293ee\n\t" \
  "v_mul_f32 v192, %[st], v192\n\t" \
  "v_mul_f32 v193, %[st], v193\n\t" \
  "v_mul_f32 v194, %[st], v194\n\t" \
  "v_mul_f32 v195, %[st], v195\n\t" \
  "v_cvt_pk_bf16_f32 v136, v192, v193\n\t" \
  "v_cvt_pk_bf16_f32 v137, v194, v195\n\t" \
  "v_mul_f32 v200, %[st], v200\n\t" \
  "v_mul_f32 v201, %[st], v201\n\t" \
  "v_mul_f32 v202, %[st], v202\n\t" \
  "v_mul_f32 v203, %[st], v203\n\t" \
  "v_cvt_pk_bf16_f32 v138, v200, v201\n\t" \
  "v_cvt_pk_bf16_f32 v139, v202, v203\n\t" \
  "v_mul_f32 v208, %[st], v208\n\t" \
  "v_mul_f32 v209, %[st], v209\n\t" \
  "v_mul_f32 v210, %[st], v210\n\t" \
  "v_mul_f32 v211, %[st], v211\n\t" \
  "v_cvt_pk_bf16_f32 v140, v208, v209\n\t" \
  "v_cvt_pk_bf16_f32 v141, v210, v211\n\t" \
  "v_mul_f32 v216, %[st], v216\n\t" \
  "v_mul_f32 v217, %[st], v217\n\t" \
  "v_mul_f32 v218, %[st], v218\n\t" \
  "v_mul_f32 v219, %[st], v219\n\t" \
  "v_cvt_pk_bf16_f32 v142, v216, v217\n\t" \
  "v_cvt_pk_bf16_f32 v143, v218, v219\n\t" \
  "v_mul_f32 v224, %[st], v224\n\t" \
  "v_mul_f32 v225, %[st], v225\n\t" \
  "v_mul_f32 v226, %[st], v226\n\t" \
  "v_mul_f32 v227, %[st], v227\n\t" \
  "v_cvt_pk_bf16_f32 v144, v224, v225\n\t" \
  "v_cvt_pk_bf16_f32 v145, v226, v227\n\t" \
  "v_mul_f32 v232, %[st], v232\n\t" \
  "v_mul_f32 v233, %[st], v233\n\t" \
  "v_mul_f32 v234, %[st], v234\n\t" \
  "v_mul_f32 v235, %[st], v235\n\t" \
  "v_cvt_pk_bf16_f32 v146, v232, v233\n\t" \
  "v_cvt_pk_bf16_f32 v147, v234, v235\n\t" \
  "v_mul_f32 v240, %[st], v240\n\t" \
  "v_mul_f32 v241, %[st], v241\n\t" \
  "v_mul_f32 v242, %[st], v242\n\t" \
  "v_mul_f32 v243, %[st], v243\n\t" \
  "v_cvt_pk_bf16_f32 v148, v240, v241\n\t" \
  "v_cvt_pk_bf16_f32 v149, v242, v243\n\t" \
  "v_mul_f32 v248, %[st], v248\n\t" \
  "v_mul_f32 v249, %[st], v249\n\t" \
  "v_mul_f32 v250, %[st], v250\n\t" \
  "v_mul_f32 v251, %[st], v251\n\t" \
  "v_cvt_pk_bf16_f32 v150, v248, v249\n\t" \
  "v_cvt_pk_bf16_f32 v151, v250, v251\n\t" \
  "v_mul_f32 v196, %[st], v196\n\t" \
  "v_mul_f32 v197, %[st], v197\n\t" \
  "v_mul_f32 v198, %[st], v198\n\t" \
  "v_mul_f32 v199, %[st], v199\n\t" \
  "v_cvt_pk_bf16_f32 v152, v196, v197\n\t" \
  "v_cvt_pk_bf16_f32 v153, v198, v199\n\t" \
  "v_mul_f32 v204, %[st], v204\n\t" \
  "v_mul_f32 v205, %[st], v205\n\t" \
  "v_mul_f32 v206, %[st], v206\n\t" \
  "v_mul_f32 v207, %[st], v207\n\t" \
  "v_cvt_pk_bf16_f32 v154, v204, v205\n\t" \
  "v_cvt_pk_bf16_f32 v155, v206, v207\n\t" \
  "v_mul_f32 v212, %[st], v212\n\t" \
  "v_mul_f32 v213, %[st], v213\n\t" \
  "v_mul_f32 v214, %[st], v214\n\t" \
  "v_mul_f32 v215, %[st], v215\n\t" \
  "v_cvt_pk_bf16_f32 v156, v212, v213\n\t" \
  "v_cvt_pk_bf16_f32 v157, v214, v215\n\t" \
  "v_mul_f32 v220, %[st], v220\n\t" \
  "v_mul_f32 v221, %[st], v221\n\t" \
  "v_mul_f32 v222, %[st], v222\n\t" \
  "v_mul_f32 v223, %[st], v223\n\t" \
  "v_cvt_pk_bf16_f32 v158, v220, v221\n\t" \
  "v_cvt_pk_bf16_f32 v159, v222, v223\n\t" \
  "v_mul_f32 v228, %[st], v228\n\t" \
  "v_mul_f32 v229, %[st], v229\n\t" \
  "v_mul_f32 v230, %[st], v230\n\t" \
  "v_mul_f32 v231, %[st], v231\n\t" \
  "v_cvt_pk_bf16_f32 v160, v228, v229\n\t" \
  "v_cvt_pk_bf16_f32 v161, v230, v231\n\t" \
  "v_mul_f32 v236, %[st], v236\n\t" \
  "v_mul_f32 v237, %[st], v237\n\t" \
  "v_mul_f32 v238, %[st], v238\n\t" \
  "v_mul_f32 v239, %[st], v239\n\t" \
  "v_cvt_pk_bf16_f32 v162, v236, v237\n\t" \
  "v_cvt_pk_bf16_f32 v163, v238, v239\n\t" \
  "v_mul_f32 v244, %[st], v244\n\t" \
  "v_mul_f32 v245, %[st], v245\n\t" \
  "v_mul_f32 v246, %[st], v246\n\t" \
  "v_mul_f32 v247, %[st], v247\n\t" \
  "v_cvt_pk_bf16_f32 v164, v244, v245\n\t" \
  "v_cvt_pk_bf16_f32 v165, v246, v247\n\t" \
  "v_mul_f32 v252, %[st], v252\n\t" \
  "v_mul_f32 v253, %[st], v253\n\t" \
  "v_mul_f32 v254, %[st], v254\n\t" \
  "v_mul_f32 v255, %[st], v255\n\t" \
  "v_cvt_pk_bf16_f32 v166, v252, v253\n\t" \
  "v_cvt_pk_bf16_f32 v167, v254, v255\n\t" \
  "v_lshl_add_u64 v[226:227], v[184:185], 0, 1\n\t" \
  "v_mov_b32 v216, 0x3f803f80\n\t" \
  "v_readfirstlane_b32 vcc_lo, v226\n\t" \
  "v_readfirstlane_b32 vcc_hi, v227\n\t" \
  "v_mov_b32 v200, 0\n\t" \
  "v_mov_b32 v201, 0\n\t" \
  "v_mov_b32 v202, 0\n\t" \
  "v_mov_b32 v203, 0\n\t" \
  "v_mov_b32 v204, 0\n\t" \
  "v_mov_b32 v205, 0\n\t" \
  "v_mov_b32 v206, 0\n\t" \
  "v_mov_b32 v207, 0\n\t" \
  "v_mov_b32 v224, 0\n\t" \
  "s_mov_b64 %[tag], vcc\n\t" \
  "v_accvgpr_write_b32 a128, v216\n\t" \
  "v_accvgpr_write_b32 a129, v216\n\t" \
  "v_accvgpr_write_b32 a130, v216\n\t" \
  "v_accvgpr_write_b32 a131, v216\n\t" \
  "v_permlane16_swap_b32 v136, v138\n\t" \
  "v_permlane16_swap_b32 v137, v139\n\t" \
  "v_permlane16_swap_b32 v140, v142\n\t" \
  "v_permlane16_swap_b32 v141, v143\n\t" \
  "v_permlane16_swap_b32 v144, v146\n\t" \
  "v_permlane16_swap_b32 v145, v147\n\t" \
  "v_permlane16_swap_b32 v148, v150\n\t" \
  "v_permlane16_swap_b32 v149, v151\n\t" \
  "v_permlane16_swap_b32 v152, v154\n\t" \
  "v_permlane16_swap_b32 v153, v155\n\t" \
  "v_permlane16_swap_b32 v156, v158\n\t" \
  "v_permlane16_swap_b32 v157, v159\n\t" \
  "v_permlane16_swap_b32 v160, v162\n\t" \
  "v_permlane16_swap_b32 v161, v163\n\t" \
  "v_permlane16_swap_b32 v164, v166\n\t" \
  "v_permlane16_swap_b32 v165, v167\n\t" \
  "s_waitcnt vmcnt(16) lgkmcnt(0)\n\t" \
  "s_barrier\n\t" \
  "v_mfma_f32_16x16x32_bf16 v[168:171], a[64:67], v[136:139], v[200:203]\n\t" \
  "s_add_u32 m0, %[mkv], 0\n\t" \
  "ds_read_b128 a[96:99], %[ka0] offset:16384\n\t" \
  "v_mfma_f32_16x16x32_bf16 v[172:175], a[80:83], v[136:139], v[200:203]\n\t" \
  "ds_read_b128 a[112:115], %[ka0] offset:17408\n\t" \
  "s_add_u32 %[sk], %[sk], 0x2000\n\t" \
  "ds_read_b128 a[100:103], %[ka1] offset:16384\n\t" \
  "v_mfma_f32_16x16x32_bf16 v[168:171], a[68:71], v[140:143], v[168:171]\n\t" \
  "ds_read_b128 a[116:119], %[ka1] offset:17408\n\t" \
  "buffer_load_dwordx4 %[kv0], %[rk], %[sk] offen offset:0 nt lds\n\t" \
  "v_mfma_f32_16x16x32_bf16 v[172:175], a[84:87], v[140:143], v[172:175]\n\t" \
  "ds_read_b128 a[104:107], %[ka2] offset:16384\n\t" \
  "ds_read_b128 a[120:123], %[ka2] offset:17408\n\t" \
  "v_mfma_f32_16x16x32_bf16 v[168:171], a[72:75], v[144:147], v[168:171]\n\t" \
  "buffer_load_dwordx4 %[kv0], %[rk], %[sk] offen offset:1024 nt lds\n\t" \
  "ds_read_b128 a[108:111], %[ka3] offset:16384\n\t" \
  "v_mfma_f32_16x16x32_bf16 v[172:175], a[88:91], v[144:147], v[172:175]\n\t" \
  "ds_read_b128 a[124:127], %[ka3] offset:17408\n\t" \
  "buffer_load_dwordx4 %[kv1], %[rk], %[sk] offen offset:2048 nt lds\n\t" \
  "ds_read_b64_tr_b16 a[0:1], %[va0] offset:24576\n\t" \
  "v_mfma_f32_16x16x32_bf16 v[168:171], a[76:79], v[148:151], v[168:171]\n\t" \
  "ds_read_b64_tr_b16 a[2:3], %[va0] offset:25600\n\t" \
  "buffer_load_dwordx4 %[kv1], %[rk], %[sk] offen offset:3072 nt lds\n\t" \
  "v_mfma_f32_16x16x32_bf16 v[172:175], a[92:95], v[148:151], v[172:175]\n\t" \
  "ds_read_b64_tr_b16 a[4:5], %[va1] offset:24576\n\t" \
  "ds_read_b64_tr_b16 a[6:7], %[va1] offset:25600\n\t" \
  "v_mfma_f32_16x16x32_bf16 v[184:187], a[64:67], v[152:155], v[204:207]\n\t" \
  "s_add_u32 m0, %[mkv], 8192\n\t" \
  "ds_read_b64_tr_b16 a[8:9], %[va2] offset:24576\n\t" \
  "v_mfma_f32_16x16x32_bf16 v[188:191], a[80:83], v[152:155], v[204:207]\n\t" \
  "ds_read_b64_tr_b16 a[10:11], %[va2] offset:25600\n\t" \
  "s_add_u32 %[sv], %[sv], 0x2000\n\t" \
  "ds_read_b64_tr_b16 a[12:13], %[va3] offset:24576\n\t" \
  "v_mfma_f32_16x16x32_bf16 v[184:187], a[68:71], v[156:159], v[184:187]\n\t" \
  "ds_read_b64_tr_b16 a[14:15], %[va3] offset:25600\n\t" \
  "buffer_load_dwordx4 %[vv0], %[rv], %[sv] offen offset:0 nt lds\n\t" \
  "v_mfma_f32_16x16x32_bf16 v[188:191], a[84:87], v[156:159], v[188:191]\n\t" \
  "ds_read_b64_tr_b16 a[16:17], %[va4] offset:24576\n\t" \
  "ds_read_b64_tr_b16 a[18:19], %[va4] offset:25600\n\t" \
  "v_mfma_f32_16x16x32_bf16 v[184:187], a[72:75], v[160:163], v[184:187]\n\t" \
  "buffer_load_dwordx4 %[vv0], %[rv], %[sv] offen offset:1024 nt lds\n\t" \
  "ds_read_b64_tr_b16 a[20:21], %[va5] offset:24576\n\t" \
  "v_mfma_f32_16x16x32_bf16 v[188:191], a[88:91], v[160:163], v[188:191]\n\t" \
  "ds_read_b64_tr_b16 a[22:23], %[va5] offset:25600\n\t" \
  "buffer_load_dwordx4 %[vv1], %[rv], %[sv] offen offset:2048 nt lds\n\t" \
  "ds_read_b64_tr_b16 a[24:25], %[va6] offset:24576\n\t" \
  "v_mfma_f32_16x16x32_bf16 v[184:187], a[76:79], v[164:167], v[184:187]\n\t" \
  "ds_read_b64_tr_b16 a[26:27], %[va6] offset:25600\n\t" \
  "buffer_load_dwordx4 %[vv1], %[rv], %[sv] offen offset:3072 nt lds\n\t" \
  "v_mfma_f32_16x16x32_bf16 v[188:191], a[92:95], v[164:167], v[188:191]\n\t" \
  "ds_read_b64_tr_b16 a[28:29], %[va7] offset:24576\n\t" \
  "ds_read_b64_tr_b16 a[30:31], %[va7] offset:25600\n\t" \
  "s_nop 7\n\t" \
  "s_nop 7\n\t" \
  "v_max3_f32 v216, v168, v169, v170\n\t" \
  "v_max3_f32 v217, v171, v172, v173\n\t" \
  "v_max3_f32 v216, v216, v174, v175\n\t" \
  "v_max_f32 v216, v216, v217\n\t" \
  "v_mov_b32 v217, v216\n\t" \
  "s_nop 1\n\t" \
  "v_permlane16_swap_b32 v216, v217\n\t" \
  "s_nop 1\n\t" \
  "v_max_f32 v216, v216, v217\n\t" \
  "v_mov_b32 v217, v216\n\t" \
  "s_nop 1\n\t" \
  "v_permlane32_swap_b32 v216, v217\n\t" \
  "s_nop 1\n\t" \
  "v_max_f32 v216, v216, v217\n\t" \
  "v_add_f32 v218, 0x40c00000, v216\n\t" \
  "v_sub_f32 v168, v168, v218\n\t" \
  "v_sub_f32 v169, v169, v218\n\t" \
  "v_sub_f32 v170, v170, v218\n\t" \
  "v_sub_f32 v171, v171, v218\n\t" \
  "v_sub_f32 v172, v172, v218\n\t" \
  "v_sub_f32 v173, v173, v218\n\t" \
  "v_sub_f32 v174, v174, v218\n\t" \
  "v_sub_f32 v175, v175, v218\n\t" \
  "v_sub_f32 v200, v200, v218\n\t" \
  "v_sub_f32 v201, v201, v218\n\t" \
  "v_sub_f32 v202, v202, v218\n\t" \
  "v_sub_f32 v203, v203, v218\n\t" \
  "v_exp_f32 v208, v168\n\t" \
  "v_exp_f32 v209, v169\n\t" \
  "v_exp_f32 v210, v170\n\t" \
  "v_exp_f32 v211, v171\n\t" \
  "v_exp_f32 v212, v172\n\t" \
  "v_exp_f32 v213, v173\n\t" \
  "v_exp_f32 v214, v174\n\t" \
  "v_exp_f32 v215, v175\n\t" \
  "s_nop 0\n\t" \
  "v_cvt_pk_bf16_f32 v192, v208, v209\n\t" \
  "v_cvt_pk_bf16_f32 v193, v210, v211\n\t" \
  "v_cvt_pk_bf16_f32 v194, v212, v213\n\t" \
  "v_cvt_pk_bf16_f32 v195, v214, v215\n\t" \
  "v_max3_f32 v216, v184, v185, v186\n\t" \
  "v_max3_f32 v217, v187, v188, v189\n\t" \
  "v_max3_f32 v216, v216, v190, v191\n\t" \
  "v_max_f32 v216, v216, v217\n\t" \
  "v_mov_b32 v217, v216\n\t" \
  "s_nop 1\n\t" \
  "v_permlane16_swap_b32 v216, v217\n\t" \
  "s_nop 1\n\t" \
  "v_max_f32 v216, v216, v217\n\t" \
  "v_mov_b32 v217, v216\n\t" \
  "s_nop 1\n\t" \
  "v_permlane32_swap_b32 v216, v217\n\t" \
  "s_nop 1\n\t" \
  "v_max_f32 v216, v216, v217\n\t" \
  "v_add_f32 v218, 0x40c00000, v216\n\t" \
  "v_sub_f32 v184, v184, v218\n\t" \
  "v_sub_f32 v185, v185, v218\n\t" \
  "v_sub_f32 v186, v186, v218\n\t" \
  "v_sub_f32 v187, v187, v218\n\t" \
  "v_sub_f32 v188, v188, v218\n\t" \
  "v_sub_f32 v189, v189, v218\n\t" \
  "v_sub_f32 v190, v190, v218\n\t" \
  "v_sub_f32 v191, v191, v218\n\t" \
  "v_sub_f32 v204, v204, v218\n\t" \
  "v_sub_f32 v205, v205, v218\n\t" \
  "v_sub_f32 v206, v206, v218\n\t" \
  "v_sub_f32 v207, v207, v218\n\t" \
  "v_exp_f32 v208, v184\n\t" \
  "v_exp_f32 v209, v185\n\t" \
  "v_exp_f32 v210, v186\n\t" \
  "v_exp_f32 v211, v187\n\t" \
  "v_exp_f32 v212, v188\n\t" \
  "v_exp_f32 v213, v189\n\t" \
  "v_exp_f32 v214, v190\n\t" \
  "v_exp_f32 v215, v191\n\t" \
  "s_nop 0\n\t" \
  "v_cvt_pk_bf16_f32 v196, v208, v209\n\t" \
  "v_cvt_pk_bf16_f32 v197, v210, v211\n\t" \
  "v_cvt_pk_bf16_f32 v198, v212, v213\n\t" \
  "v_cvt_pk_bf16_f32 v199, v214, v215\n\t" \
  "s_nop 3\n\t" \
  "s_mov_b32 %[cnt], 7\n\t" \
  ".Lloop_%=:\n\t" \
  "s_waitcnt vmcnt(16) lgkmcnt(0)\n\t" \
  "s_add_u32 m0, %[mkv], 16384\n\t" \
  "s_add_u32 %[sk], %[sk], 0x2000\n\t" \
  "s_barrier\n\t" \
  "v_mfma_f32_16x16x32_bf16 v[176:179], a[96:99], v[136:139], v[200:203]\n\t" \
  "buffer_load_dwordx4 %[kv0], %[rk], %[sk] offen offset:0 nt lds\n\t" \
  "v_cvt_pk_bf16_f32 v196, v208, v209\n\t" \
  "v_mfma_f32_16x16x32_bf16 v[180:183], a[112:115], v[136:139], v[200:203]\n\t" \
  "ds_read_b128 a[64:67], %[ka0] offset:32768\n\t" \
  "v_cvt_pk_bf16_f32 v197, v210, v211\n\t" \
  "v_mfma_f32_16x16x32_bf16 v[176:179], a[100:103], v[140:143], v[176:179]\n\t" \
  "ds_read_b128 a[80:83], %[ka0] offset:33792\n\t" \
  "v_cvt_pk_bf16_f32 v198, v212, v213\n\t" \
  "v_mfma_f32_16x16x32_bf16 v[180:183], a[116:119], v[140:143], v[180:183]\n\t" \
  "ds_read_b128 a[68:71], %[ka1] offset:32768\n\t" \
  "v_cvt_pk_bf16_f32 v199, v214, v215\n\t" \
  "v_mfma_f32_16x16x32_bf16 v[176:179], a[104:107], v[144:147], v[176:179]\n\t" \
  "buffer_load_dwordx4 %[kv0], %[rk], %[sk] offen offset:1024 nt lds\n\t" \
  "v_or3_b32 v224, v196, v197, v224\n\t" \
  "v_mfma_f32_16x16x32_bf16 v[180:183], a[120:123], v[144:147], v[180:183]\n\t" \
  "ds_read_b128 a[84:87], %[ka1] offset:33792\n\t" \
  "v_or3_b32 v224, v198, v199, v224\n\t" \
  "v_mfma_f32_16x16x32_bf16 v[176:179], a[108:111], v[148:151], v[176:179]\n\t" \
  "ds_read_b128 a[72:75], %[ka2] offset:32768\n\t" \
  "v_and_b32 v216, 0x40004000, v224\n\t" \
  "v_mfma_f32_16x16x32_bf16 v[180:183], a[124:127], v[148:151], v[180:183]\n\t" \
  "v_cmp_ne_u32 vcc, 0, v216\n\t" \
  "s_cbranch_vccnz .Lrare0_%=\n\t" \
  ".Lback0_%=:\n\t" \
  "v_mfma_f32_16x16x32_bf16 v[64:67], a[0:3], v[192:195], v[64:67]\n\t" \
  "ds_read_b128 a[88:91], %[ka2] offset:33792\n\t" \
  "buffer_load_dwordx4 %[kv1], %[rk], %[sk] offen offset:2048 nt lds\n\t" \
  "v_mfma_f32_16x16x32_bf16 v[72:75], a[4:7], v[192:195], v[72:75]\n\t" \
  "ds_read_b128 a[76:79], %[ka3] offset:32768\n\t" \
  "v_exp_f32 v208, v176\n\t" \
  "v_mfma_f32_16x16x32_bf16 v[80:83], a[8:11], v[192:195], v[80:83]\n\t" \
  "ds_read_b128 a[92:95], %[ka3] offset:33792\n\t" \
  "v_exp_f32 v209, v177\n\t" \
  "v_mfma_f32_16x16x32_bf16 v[88:91], a[12:15], v[192:195], v[88:91]\n\t" \
  "ds_read_b64_tr_b16 a[32:33], %[va0] offset:40960\n\t" \
  "v_exp_f32 v210, v178\n\t" \
  "v_mfma_f32_16x16x32_bf16 v[96:99], a[16:19], v[192:195], v[96:99]\n\t" \
  "buffer_load_dwordx4 %[kv1], %[rk], %[sk] offen offset:3072 nt lds\n\t" \
  "v_exp_f32 v211, v179\n\t" \
  "v_mfma_f32_16x16x32_bf16 v[104:107], a[20:23], v[192:195], v[104:107]\n\t" \
  "ds_read_b64_tr_b16 a[34:35], %[va0] offset:41984\n\t" \
  "v_exp_f32 v212, v180\n\t" \
  "v_mfma_f32_16x16x32_bf16 v[112:115], a[24:27], v[192:195], v[112:115]\n\t" \
  "ds_read_b64_tr_b16 a[36:37], %[va1] offset:40960\n\t" \
  "v_exp_f32 v213, v181\n\t" \
  "v_mfma_f32_16x16x32_bf16 v[120:123], a[28:31], v[192:195], v[120:123]\n\t" \
  "s_add_u32 m0, %[mkv], 24576\n\t" \
  "v_exp_f32 v214, v182\n\t" \
  "v_mfma_f32_16x16x32_bf16 v[128:131], a[128:131], v[192:195], v[128:131]\n\t" \
  "s_add_u32 %[sv], %[sv], 0x2000\n\t" \
  "v_exp_f32 v215, v183\n\t" \
  "v_mfma_f32_16x16x32_bf16 v[184:187], a[96:99], v[152:155], v[204:207]\n\t" \
  "buffer_load_dwordx4 %[vv0], %[rv], %[sv] offen offset:0 nt lds\n\t" \
  "v_cvt_pk_bf16_f32 v192, v208, v209\n\t" \
  "v_mfma_f32_16x16x32_bf16 v[188:191], a[112:115], v[152:155], v[204:207]\n\t" \
  "ds_read_b64_tr_b16 a[38:39], %[va1] offset:41984\n\t" \
  "v_cvt_pk_bf16_f32 v193, v210, v211\n\t" \
  "v_mfma_f32_16x16x32_bf16 v[184:187], a[100:103], v[156:159], v[184:187]\n\t" \
  "ds_read_b64_tr_b16 a[40:41], %[va2] offset:40960\n\t" \
  "v_cvt_pk_bf16_f32 v194, v212, v213\n\t" \
  "v_mfma_f32_16x16x32_bf16 v[188:191], a[116:119], v[156:159], v[188:191]\n\t" \
  "ds_read_b64_tr_b16 a[42:43], %[va2] offset:41984\n\t" \
  "v_cvt_pk_bf16_f32 v195, v214, v215\n\t" \
  "v_mfma_f32_16x16x32_bf16 v[184:187], a[104:107], v[160:163], v[184:187]\n\t" \
  "buffer_load_dwordx4 %[vv0], %[rv], %[sv] offen offset:1024 nt lds\n\t" \
  "v_or_b32 v224, v192, v193\n\t" \
  "v_mfma_f32_16x16x32_bf16 v[188:191], a[120:123], v[160:163], v[188:191]\n\t" \
  "ds_read_b64_tr_b16 a[44:45], %[va3] offset:40960\n\t" \
  "v_or3_b32 v224, v194, v195, v224\n\t" \
  "v_mfma_f32_16x16x32_bf16 v[184:187], a[108:111], v[164:167], v[184:187]\n\t" \
  "ds_read_b64_tr_b16 a[46:47], %[va3] offset:41984\n\t" \
  "ds_read_b64_tr_b16 a[48:49], %[va4] offset:40960\n\t" \
  "v_mfma_f32_16x16x32_bf16 v[188:191], a[124:127], v[164:167], v[188:191]\n\t" \
  "ds_read_b64_tr_b16 a[50:51], %[va4] offset:41984\n\t" \
  "ds_read_b64_tr_b16 a[52:53], %[va5] offset:40960\n\t" \
  "v_mfma_f32_16x16x32_bf16 v[68:71], a[0:3], v[196:199], v[68:71]\n\t" \
  "ds_read_b64_tr_b16 a[54:55], %[va5] offset:41984\n\t" \
  "buffer_load_dwordx4 %[vv1], %[rv], %[sv] offen offset:2048 nt lds\n\t" \
  "v_mfma_f32_16x16x32_bf16 v[76:79], a[4:7], v[196:199], v[76:79]\n\t" \
  "ds_read_b64_tr_b16 a[56:57], %[va6] offset:40960\n\t" \
  "v_exp_f32 v208, v184\n\t" \
  "v_mfma_f32_16x16x32_bf16 v[84:87], a[8:11], v[196:199], v[84:87]\n\t" \
  "ds_read_b64_tr_b16 a[58:59], %[va6] offset:41984\n\t" \
  "v_exp_f32 v209, v185\n\t" \
  "v_mfma_f32_16x16x32_bf16 v[92:95], a[12:15], v[196:199], v[92:95]\n\t" \
  "ds_read_b64_tr_b16 a[60:61], %[va7] offset:40960\n\t" \
  "v_exp_f32 v210, v186\n\t" \
  "v_mfma_f32_16x16x32_bf16 v[100:103], a[16:19], v[196:199], v[100:103]\n\t" \
  "buffer_load_dwordx4 %[vv1], %[rv], %[sv] offen offset:3072 nt lds\n\t" \
  "v_exp_f32 v211, v187\n\t" \
  "v_mfma_f32_16x16x32_bf16 v[108:111], a[20:23], v[196:199], v[108:111]\n\t" \
  "ds_read_b64_tr_b16 a[62:63], %[va7] offset:41984\n\t" \
  "v_exp_f32 v212, v188\n\t" \
  "v_mfma_f32_16x16x32_bf16 v[116:119], a[24:27], v[196:199], v[116:119]\n\t" \
  "v_exp_f32 v213, v189\n\t" \
  "v_mfma_f32_16x16x32_bf16 v[124:127], a[28:31], v[196:199], v[124:127]\n\t" \
  "v_exp_f32 v214, v190\n\t" \
  "v_mfma_f32_16x16x32_bf16 v[132:135], a[128:131], v[196:199], v[132:135]\n\t" \
  "v_exp_f32 v215, v191\n\t" \
  "s_waitcnt vmcnt(16) lgkmcnt(0)\n\t" \
  "s_add_u32 m0, %[mkv], 32768\n\t" \
  "s_add_u32 %[sk], %[sk], 0x2000\n\t" \
  "s_barrier\n\t" \
  "v_mfma_f32_16x16x32_bf16 v[168:171], a[64:67], v[136:139], v[200:203]\n\t" \
  "buffer_load_dwordx4 %[kv0], %[rk], %[sk] offen offset:0 nt lds\n\t" \
  "v_cvt_pk_bf16_f32 v196, v208, v209\n\t" \
  "v_mfma_f32_16x16x32_bf16 v[172:175], a[80:83], v[136:139], v[200:203]\n\t" \
  "ds_read_b128 a[96:99], %[ka0] offset:49152\n\t" \
  "v_cvt_pk_bf16_f32 v197, v210, v211\n\t" \
  "v_mfma_f32_16x16x32_bf16 v[168:171], a[68:71], v[140:143], v[168:171]\n\t" \
  "ds_read_b128 a[112:115], %[ka0] offset:50176\n\t" \
  "v_cvt_pk_bf16_f32 v198, v212, v213\n\t" \
  "v_mfma_f32_16x16x32_bf16 v[172:175], a[84:87], v[140:143], v[172:175]\n\t" \
  "ds_read_b128 a[100:103], %[ka1] offset:49152\n\t" \
  "v_cvt_pk_bf16_f32 v199, v214, v215\n\t" \
  "v_mfma_f32_16x16x32_bf16 v[168:171], a[72:75], v[144:147], v[168:171]\n\t" \
  "buffer_load_dwordx4 %[kv0], %[rk], %[sk] offen offset:1024 nt lds\n\t" \
  "v_or3_b32 v224, v196, v197, v224\n\t" \
  "v_mfma_f32_16x16x32_bf16 v[172:175], a[88:91], v[144:147], v[172:175]\n\t" \
  "ds_read_b128 a[116:119], %[ka1] offset:50176\n\t" \
  "v_or3_b32 v224, v198, v199, v224\n\t" \
  "v_mfma_f32_16x16x32_bf16 v[168:171], a[76:79], v[148:151], v[168:171]\n\t" \
  "ds_read_b128 a[104:107], %[ka2] offset:49152\n\t" \
  "v_and_b32 v216, 0x40004000, v224\n\t" \
  "v_mfma_f32_16x16x32_bf16 v[172:175], a[92:95], v[148:151], v[172:175]\n\t" \
  "v_cmp_ne_u32 vcc, 0, v216\n\t" \
  "s_cbranch_vccnz .Lrare1_%=\n\t" \
  ".Lback1_%=:\n\t" \
  "v_mfma_f32_16x16x32_bf16 v[64:67], a[32:35], v[192:195], v[64:67]\n\t" \
  "ds_read_b128 a[120:123], %[ka2] offset:50176\n\t" \
  "buffer_load_dwordx4 %[kv1], %[rk], %[sk] offen offset:2048 nt lds\n\t" \
  "v_mfma_f32_16x16x32_bf16 v[72:75], a[36:39], v[192:195], v[72:75]\n\t" \
  "ds_read_b128 a[108:111], %[ka3] offset:49152\n\t" \
  "v_exp_f32 v208, v168\n\t" \
  "v_mfma_f32_16x16x32_bf16 v[80:83], a[40:43], v[192:195], v[80:83]\n\t" \
  "ds_read_b128 a[124:127], %[ka3] offset:50176\n\t" \
  "v_exp_f32 v209, v169\n\t" \
  "v_mfma_f32_16x16x32_bf16 v[88:91], a[44:47], v[192:195], v[88:91]\n\t" \
  "ds_read_b64_tr_b16 a[0:1], %[va0] offset:57344\n\t" \
  "v_exp_f32 v210, v170\n\t" \
  "v_mfma_f32_16x16x32_bf16 v[96:99], a[48:51], v[192:195], v[96:99]\n\t" \
  "buffer_load_dwordx4 %[kv1], %[rk], %[sk] offen offset:3072 nt lds\n\t" \
  "v_exp_f32 v211, v171\n\t" \
  "v_mfma_f32_16x16x32_bf16 v[104:107], a[52:55], v[192:195], v[104:107]\n\t" \
  "ds_read_b64_tr_b16 a[2:3], %[va0] offset:58368\n\t" \
  "v_exp_f32 v212, v172\n\t" \
  "v_mfma_f32_16x16x32_bf16 v[112:115], a[56:59], v[192:195], v[112:115]\n\t" \
  "ds_read_b64_tr_b16 a[4:5], %[va1] offset:57344\n\t" \
  "v_exp_f32 v213, v173\n\t" \
  "v_mfma_f32_16x16x32_bf16 v[120:123], a[60:63], v[192:195], v[120:123]\n\t" \
  "s_add_u32 m0, %[mkv], 40960\n\t" \
  "v_exp_f32 v214, v174\n\t" \
  "v_mfma_f32_16x16x32_bf16 v[128:131], a[128:131], v[192:195], v[128:131]\n\t" \
  "s_add_u32 %[sv], %[sv], 0x2000\n\t" \
  "v_exp_f32 v215, v175\n\t" \
  "v_mfma_f32_16x16x32_bf16 v[184:187], a[64:67], v[152:155], v[204:207]\n\t" \
  "buffer_load_dwordx4 %[vv0], %[rv], %[sv] offen offset:0 nt lds\n\t" \
  "v_cvt_pk_bf16_f32 v192, v208, v209\n\t" \
  "v_mfma_f32_16x16x32_bf16 v[188:191], a[80:83], v[152:155], v[204:207]\n\t" \
  "ds_read_b64_tr_b16 a[6:7], %[va1] offset:58368\n\t" \
  "v_cvt_pk_bf16_f32 v193, v210, v211\n\t" \
  "v_mfma_f32_16x16x32_bf16 v[184:187], a[68:71], v[156:159], v[184:187]\n\t" \
  "ds_read_b64_tr_b16 a[8:9], %[va2] offset:57344\n\t" \
  "v_cvt_pk_bf16_f32 v194, v212, v213\n\t" \
  "v_mfma_f32_16x16x32_bf16 v[188:191], a[84:87], v[156:159], v[188:191]\n\t" \
  "ds_read_b64_tr_b16 a[10:11], %[va2] offset:58368\n\t" \
  "v_cvt_pk_bf16_f32 v195, v214, v215\n\t" \
  "v_mfma_f32_16x16x32_bf16 v[184:187], a[72:75], v[160:163], v[184:187]\n\t" \
  "buffer_load_dwordx4 %[vv0], %[rv], %[sv] offen offset:1024 nt lds\n\t" \
  "v_or_b32 v224, v192, v193\n\t" \
  "v_mfma_f32_16x16x32_bf16 v[188:191], a[88:91], v[160:163], v[188:191]\n\t" \
  "ds_read_b64_tr_b16 a[12:13], %[va3] offset:57344\n\t" \
  "v_or3_b32 v224, v194, v195, v224\n\t" \
  "v_mfma_f32_16x16x32_bf16 v[184:187], a[76:79], v[164:167], v[184:187]\n\t" \
  "ds_read_b64_tr_b16 a[14:15], %[va3] offset:58368\n\t" \
  "ds_read_b64_tr_b16 a[16:17], %[va4] offset:57344\n\t" \
  "v_mfma_f32_16x16x32_bf16 v[188:191], a[92:95], v[164:167], v[188:191]\n\t" \
  "ds_read_b64_tr_b16 a[18:19], %[va4] offset:58368\n\t" \
  "ds_read_b64_tr_b16 a[20:21], %[va5] offset:57344\n\t" \
  "v_mfma_f32_16x16x32_bf16 v[68:71], a[32:35], v[196:199], v[68:71]\n\t" \
  "ds_read_b64_tr_b16 a[22:23], %[va5] offset:58368\n\t" \
  "buffer_load_dwordx4 %[vv1], %[rv], %[sv] offen offset:2048 nt lds\n\t" \
  "v_mfma_f32_16x16x32_bf16 v[76:79], a[36:39], v[196:199], v[76:79]\n\t" \
  "ds_read_b64_tr_b16 a[24:25], %[va6] offset:57344\n\t" \
  "v_exp_f32 v208, v184\n\t" \
  "v_mfma_f32_16x16x32_bf16 v[84:87], a[40:43], v[196:199], v[84:87]\n\t" \
  "ds_read_b64_tr_b16 a[26:27], %[va6] offset:58368\n\t" \
  "v_exp_f32 v209, v185\n\t" \
  "v_mfma_f32_16x16x32_bf16 v[92:95], a[44:47], v[196:199], v[92:95]\n\t" \
  "ds_read_b64_tr_b16 a[28:29], %[va7] offset:57344\n\t" \
  "v_exp_f32 v210, v186\n\t" \
  "v_mfma_f32_16x16x32_bf16 v[100:103], a[48:51], v[196:199], v[100:103]\n\t" \
  "buffer_load_dwordx4 %[vv1], %[rv], %[sv] offen offset:3072 nt lds\n\t" \
  "v_exp_f32 v211, v187\n\t" \
  "v_mfma_f32_16x16x32_bf16 v[108:111], a[52:55], v[196:199], v[108:111]\n\t" \
  "ds_read_b64_tr_b16 a[30:31], %[va7] offset:58368\n\t" \
  "v_exp_f32 v212, v188\n\t" \
  "v_mfma_f32_16x16x32_bf16 v[116:119], a[56:59], v[196:199], v[116:119]\n\t" \
  "v_exp_f32 v213, v189\n\t" \
  "v_mfma_f32_16x16x32_bf16 v[124:127], a[60:63], v[196:199], v[124:127]\n\t" \
  "v_exp_f32 v214, v190\n\t" \
  "v_mfma_f32_16x16x32_bf16 v[132:135], a[128:131], v[196:199], v[132:135]\n\t" \
  "v_exp_f32 v215, v191\n\t" \
  "s_waitcnt vmcnt(16) lgkmcnt(0)\n\t" \
  "s_add_u32 m0, %[mkv], 49152\n\t" \
  "s_add_u32 %[sk], %[sk], 0x2000\n\t" \
  "s_barrier\n\t" \
  "v_mfma_f32_16x16x32_bf16 v[176:179], a[96:99], v[136:139], v[200:203]\n\t" \
  "buffer_load_dwordx4 %[kv0], %[rk], %[sk] offen offset:0 nt lds\n\t" \
  "v_cvt_pk_bf16_f32 v196, v208, v209\n\t" \
  "v_mfma_f32_16x16x32_bf16 v[180:183], a[112:115], v[136:139], v[200:203]\n\t" \
  "ds_read_b128 a[64:67], %[ka0] offset:0\n\t" \
  "v_cvt_pk_bf16_f32 v197, v210, v211\n\t" \
  "v_mfma_f32_16x16x32_bf16 v[176:179], a[100:103], v[140:143], v[176:179]\n\t" \
  "ds_read_b128 a[80:83], %[ka0] offset:1024\n\t" \
  "v_cvt_pk_bf16_f32 v198, v212, v213\n\t" \
  "v_mfma_f32_16x16x32_bf16 v[180:183], a[116:119], v[140:143], v[180:183]\n\t" \
  "ds_read_b128 a[68:71], %[ka1] offset:0\n\t" \
  "v_cvt_pk_bf16_f32 v199, v214, v215\n\t" \
  "v_mfma_f32_16x16x32_bf16 v[176:179], a[104:107], v[144:147], v[176:179]\n\t" \
  "buffer_load_dwordx4 %[kv0], %[rk], %[sk] offen offset:1024 nt lds\n\t" \
  "v_or3_b32 v224, v196, v197, v224\n\t" \
  "v_mfma_f32_16x16x32_bf16 v[180:183], a[120:123], v[144:147], v[180:183]\n\t" \
  "ds_read_b128 a[84:87], %[ka1] offset:1024\n\t" \
  "v_or3_b32 v224, v198, v199, v224\n\t" \
  "v_mfma_f32_16x16x32_bf16 v[176:179], a[108:111], v[148:151], v[176:179]\n\t" \
  "ds_read_b128 a[72:75], %[ka2] offset:0\n\t" \
  "v_and_b32 v216, 0x40004000, v224\n\t" \
  "v_mfma_f32_16x16x32_bf16 v[180:183], a[124:127], v[148:151], v[180:183]\n\t" \
  "v_cmp_ne_u32 vcc, 0, v216\n\t" \
  "s_cbranch_vccnz .Lrare2_%=\n\t" \
  ".Lback2_%=:\n\t" \
  "v_mfma_f32_16x16x32_bf16 v[64:67], a[0:3], v[192:195], v[64:67]\n\t" \
  "ds_read_b128 a[88:91], %[ka2] offset:1024\n\t" \
  "buffer_load_dwordx4 %[kv1], %[rk], %[sk] offen offset:2048 nt lds\n\t" \
  "v_mfma_f32_16x16x32_bf16 v[72:75], a[4:7], v[192:195], v[72:75]\n\t" \
  "ds_read_b128 a[76:79], %[ka3] offset:0\n\t" \
  "v_exp_f32 v208, v176\n\t" \
  "v_mfma_f32_16x16x32_bf16 v[80:83], a[8:11], v[192:195], v[80:83]\n\t" \
  "ds_read_b128 a[92:95], %[ka3] offset:1024\n\t" \
  "v_exp_f32 v209, v177\n\t" \
  "v_mfma_f32_16x16x32_bf16 v[88:91], a[12:15], v[192:195], v[88:91]\n\t" \
  "ds_read_b64_tr_b16 a[32:33], %[va0] offset:8192\n\t" \
  "v_exp_f32 v210, v178\n\t" \
  "v_mfma_f32_16x16x32_bf16 v[96:99], a[16:19], v[192:195], v[96:99]\n\t" \
  "buffer_load_dwordx4 %[kv1], %[rk], %[sk] offen offset:3072 nt lds\n\t" \
  "v_exp_f32 v211, v179\n\t" \
  "v_mfma_f32_16x16x32_bf16 v[104:107], a[20:23], v[192:195], v[104:107]\n\t" \
  "ds_read_b64_tr_b16 a[34:35], %[va0] offset:9216\n\t" \
  "v_exp_f32 v212, v180\n\t" \
  "v_mfma_f32_16x16x32_bf16 v[112:115], a[24:27], v[192:195], v[112:115]\n\t" \
  "ds_read_b64_tr_b16 a[36:37], %[va1] offset:8192\n\t" \
  "v_exp_f32 v213, v181\n\t" \
  "v_mfma_f32_16x16x32_bf16 v[120:123], a[28:31], v[192:195], v[120:123]\n\t" \
  "s_add_u32 m0, %[mkv], 57344\n\t" \
  "v_exp_f32 v214, v182\n\t" \
  "v_mfma_f32_16x16x32_bf16 v[128:131], a[128:131], v[192:195], v[128:131]\n\t" \
  "s_add_u32 %[sv], %[sv], 0x2000\n\t" \
  "v_exp_f32 v215, v183\n\t" \
  "v_mfma_f32_16x16x32_bf16 v[184:187], a[96:99], v[152:155], v[204:207]\n\t" \
  "buffer_load_dwordx4 %[vv0], %[rv], %[sv] offen offset:0 nt lds\n\t" \
  "v_cvt_pk_bf16_f32 v192, v208, v209\n\t" \
  "v_mfma_f32_16x16x32_bf16 v[188:191], a[112:115], v[152:155], v[204:207]\n\t" \
  "ds_read_b64_tr_b16 a[38:39], %[va1] offset:9216\n\t" \
  "v_cvt_pk_bf16_f32 v193, v210, v211\n\t" \
  "v_mfma_f32_16x16x32_bf16 v[184:187], a[100:103], v[156:159], v[184:187]\n\t" \
  "ds_read_b64_tr_b16 a[40:41], %[va2] offset:8192\n\t" \
  "v_cvt_pk_bf16_f32 v194, v212, v213\n\t" \
  "v_mfma_f32_16x16x32_bf16 v[188:191], a[116:119], v[156:159], v[188:191]\n\t" \
  "ds_read_b64_tr_b16 a[42:43], %[va2] offset:9216\n\t" \
  "v_cvt_pk_bf16_f32 v195, v214, v215\n\t" \
  "v_mfma_f32_16x16x32_bf16 v[184:187], a[104:107], v[160:163], v[184:187]\n\t" \
  "buffer_load_dwordx4 %[vv0], %[rv], %[sv] offen offset:1024 nt lds\n\t" \
  "v_or_b32 v224, v192, v193\n\t" \
  "v_mfma_f32_16x16x32_bf16 v[188:191], a[120:123], v[160:163], v[188:191]\n\t" \
  "ds_read_b64_tr_b16 a[44:45], %[va3] offset:8192\n\t" \
  "v_or3_b32 v224, v194, v195, v224\n\t" \
  "v_mfma_f32_16x16x32_bf16 v[184:187], a[108:111], v[164:167], v[184:187]\n\t" \
  "ds_read_b64_tr_b16 a[46:47], %[va3] offset:9216\n\t" \
  "ds_read_b64_tr_b16 a[48:49], %[va4] offset:8192\n\t" \
  "v_mfma_f32_16x16x32_bf16 v[188:191], a[124:127], v[164:167], v[188:191]\n\t" \
  "ds_read_b64_tr_b16 a[50:51], %[va4] offset:9216\n\t" \
  "ds_read_b64_tr_b16 a[52:53], %[va5] offset:8192\n\t" \
  "v_mfma_f32_16x16x32_bf16 v[68:71], a[0:3], v[196:199], v[68:71]\n\t" \
  "ds_read_b64_tr_b16 a[54:55], %[va5] offset:9216\n\t" \
  "buffer_load_dwordx4 %[vv1], %[rv], %[sv] offen offset:2048 nt lds\n\t" \
  "v_mfma_f32_16x16x32_bf16 v[76:79], a[4:7], v[196:199], v[76:79]\n\t" \
  "ds_read_b64_tr_b16 a[56:57], %[va6] offset:8192\n\t" \
  "v_exp_f32 v208, v184\n\t" \
  "v_mfma_f32_16x16x32_bf16 v[84:87], a[8:11], v[196:199], v[84:87]\n\t" \
  "ds_read_b64_tr_b16 a[58:59], %[va6] offset:9216\n\t" \
  "v_exp_f32 v209, v185\n\t" \
  "v_mfma_f32_16x16x32_bf16 v[92:95], a[12:15], v[196:199], v[92:95]\n\t" \
  "ds_read_b64_tr_b16 a[60:61], %[va7] offset:8192\n\t" \
  "v_exp_f32 v210, v186\n\t" \
  "v_mfma_f32_16x16x32_bf16 v[100:103], a[16:19], v[196:199], v[100:103]\n\t" \
  "buffer_load_dwordx4 %[vv1], %[rv], %[sv] offen offset:3072 nt lds\n\t" \
  "v_exp_f32 v211, v187\n\t" \
  "v_mfma_f32_16x16x32_bf16 v[108:111], a[20:23], v[196:199], v[108:111]\n\t" \
  "ds_read_b64_tr_b16 a[62:63], %[va7] offset:9216\n\t" \
  "v_exp_f32 v212, v188\n\t" \
  "v_mfma_f32_16x16x32_bf16 v[116:119], a[24:27], v[196:199], v[116:119]\n\t" \
  "v_exp_f32 v213, v189\n\t" \
  "v_mfma_f32_16x16x32_bf16 v[124:127], a[28:31], v[196:199], v[124:127]\n\t" \
  "v_exp_f32 v214, v190\n\t" \
  "v_mfma_f32_16x16x32_bf16 v[132:135], a[128:131], v[196:199], v[132:135]\n\t" \
  "v_exp_f32 v215, v191\n\t" \
  "s_waitcnt vmcnt(16) lgkmcnt(0)\n\t" \
  "s_add_u32 m0, %[mkv], 0\n\t" \
  "s_add_u32 %[sk], %[sk], 0x2000\n\t" \
  "s_barrier\n\t" \
  "v_mfma_f32_16x16x32_bf16 v[168:171], a[64:67], v[136:139], v[200:203]\n\t" \
  "buffer_load_dwordx4 %[kv0], %[rk], %[sk] offen offset:0 nt lds\n\t" \
  "v_cvt_pk_bf16_f32 v196, v208, v209\n\t" \
  "v_mfma_f32_16x16x32_bf16 v[172:175], a[80:83], v[136:139], v[200:203]\n\t" \
  "ds_read_b128 a[96:99], %[ka0] offset:16384\n\t" \
  "v_cvt_pk_bf16_f32 v197, v210, v211\n\t" \
  "v_mfma_f32_16x16x32_bf16 v[168:171], a[68:71], v[140:143], v[168:171]\n\t" \
  "ds_read_b128 a[112:115], %[ka0] offset:17408\n\t" \
  "v_cvt_pk_bf16_f32 v198, v212, v213\n\t" \
  "v_mfma_f32_16x16x32_bf16 v[172:175], a[84:87], v[140:143], v[172:175]\n\t" \
  "ds_read_b128 a[100:103], %[ka1] offset:16384\n\t" \
  "v_cvt_pk_bf16_f32 v199, v214, v215\n\t" \
  "v_mfma_f32_16x16x32_bf16 v[168:171], a[72:75], v[144:147], v[168:171]\n\t" \
  "buffer_load_dwordx4 %[kv0], %[rk], %[sk] offen offset:1024 nt lds\n\t" \
  "v_or3_b32 v224, v196, v197, v224\n\t" \
  "v_mfma_f32_16x16x32_bf16 v[172:175], a[88:91], v[144:147], v[172:175]\n\t" \
  "ds_read_b128 a[116:119], %[ka1] offset:17408\n\t" \
  "v_or3_b32 v224, v198, v199, v224\n\t" \
  "v_mfma_f32_16x16x32_bf16 v[168:171], a[76:79], v[148:151], v[168:171]\n\t" \
  "ds_read_b128 a[104:107], %[ka2] offset:16384\n\t" \
  "v_and_b32 v216, 0x40004000, v224\n\t" \
  "v_mfma_f32_16x16x32_bf16 v[172:175], a[92:95], v[148:151], v[172:175]\n\t" \
  "v_cmp_ne_u32 vcc, 0, v216\n\t" \
  "s_cbranch_vccnz .Lrare3_%=\n\t" \
  ".Lback3_%=:\n\t" \
  "v_mfma_f32_16x16x32_bf16 v[64:67], a[32:35], v[192:195], v[64:67]\n\t" \
  "ds_read_b128 a[120:123], %[ka2] offset:17408\n\t" \
  "buffer_load_dwordx4 %[kv1], %[rk], %[sk] offen offset:2048 nt lds\n\t" \
  "v_mfma_f32_16x16x32_bf16 v[72:75], a[36:39], v[192:195], v[72:75]\n\t" \
  "ds_read_b128 a[108:111], %[ka3] offset:16384\n\t" \
  "v_exp_f32 v208, v168\n\t" \
  "v_mfma_f32_16x16x32_bf16 v[80:83], a[40:43], v[192:195], v[80:83]\n\t" \
  "ds_read_b128 a[124:127], %[ka3] offset:17408\n\t" \
  "v_exp_f32 v209, v169\n\t" \
  "v_mfma_f32_16x16x32_bf16 v[88:91], a[44:47], v[192:195], v[88:91]\n\t" \
  "ds_read_b64_tr_b16 a[0:1], %[va0] offset:24576\n\t" \
  "v_exp_f32 v210, v170\n\t" \
  "v_mfma_f32_16x16x32_bf16 v[96:99], a[48:51], v[192:195], v[96:99]\n\t" \
  "buffer_load_dwordx4 %[kv1], %[rk], %[sk] offen offset:3072 nt lds\n\t" \
  "v_exp_f32 v211, v171\n\t" \
  "v_mfma_f32_16x16x32_bf16 v[104:107], a[52:55], v[192:195], v[104:107]\n\t" \
  "ds_read_b64_tr_b16 a[2:3], %[va0] offset:25600\n\t" \
  "v_exp_f32 v212, v172\n\t" \
  "v_mfma_f32_16x16x32_bf16 v[112:115], a[56:59], v[192:195], v[112:115]\n\t" \
  "ds_read_b64_tr_b16 a[4:5], %[va1] offset:24576\n\t" \
  "v_exp_f32 v213, v173\n\t" \
  "v_mfma_f32_16x16x32_bf16 v[120:123], a[60:63], v[192:195], v[120:123]\n\t" \
  "s_add_u32 m0, %[mkv], 8192\n\t" \
  "v_exp_f32 v214, v174\n\t" \
  "v_mfma_f32_16x16x32_bf16 v[128:131], a[128:131], v[192:195], v[128:131]\n\t" \
  "s_add_u32 %[sv], %[sv], 0x2000\n\t" \
  "v_exp_f32 v215, v175\n\t" \
  "v_mfma_f32_16x16x32_bf16 v[184:187], a[64:67], v[152:155], v[204:207]\n\t" \
  "buffer_load_dwordx4 %[vv0], %[rv], %[sv] offen offset:0 nt lds\n\t" \
  "v_cvt_pk_bf16_f32 v192, v208, v209\n\t" \
  "v_mfma_f32_16x16x32_bf16 v[188:191], a[80:83], v[152:155], v[204:207]\n\t" \
  "ds_read_b64_tr_b16 a[6:7], %[va1] offset:25600\n\t" \
  "v_cvt_pk_bf16_f32 v193, v210, v211\n\t" \
  "v_mfma_f32_16x16x32_bf16 v[184:187], a[68:71], v[156:159], v[184:187]\n\t" \
  "ds_read_b64_tr_b16 a[8:9], %[va2] offset:24576\n\t" \
  "v_cvt_pk_bf16_f32 v194, v212, v213\n\t" \
  "v_mfma_f32_16x16x32_bf16 v[188:191], a[84:87], v[156:159], v[188:191]\n\t" \
  "ds_read_b64_tr_b16 a[10:11], %[va2] offset:25600\n\t" \
  "v_cvt_pk_bf16_f32 v195, v214, v215\n\t" \
  "v_mfma_f32_16x16x32_bf16 v[184:187], a[72:75], v[160:163], v[184:187]\n\t" \
  "buffer_load_dwordx4 %[vv0], %[rv], %[sv] offen offset:1024 nt lds\n\t" \
  "v_or_b32 v224, v192, v193\n\t" \
  "v_mfma_f32_16x16x32_bf16 v[188:191], a[88:91], v[160:163], v[188:191]\n\t" \
  "ds_read_b64_tr_b16 a[12:13], %[va3] offset:24576\n\t" \
  "v_or3_b32 v224, v194, v195, v224\n\t" \
  "v_mfma_f32_16x16x32_bf16 v[184:187], a[76:79], v[164:167], v[184:187]\n\t" \
  "ds_read_b64_tr_b16 a[14:15], %[va3] offset:25600\n\t" \
  "ds_read_b64_tr_b16 a[16:17], %[va4] offset:24576\n\t" \
  "v_mfma_f32_16x16x32_bf16 v[188:191], a[92:95], v[164:167], v[188:191]\n\t" \
  "ds_read_b64_tr_b16 a[18:19], %[va4] offset:25600\n\t" \
  "ds_read_b64_tr_b16 a[20:21], %[va5] offset:24576\n\t" \
  "v_mfma_f32_16x16x32_bf16 v[68:71], a[32:35], v[196:199], v[68:71]\n\t" \
  "ds_read_b64_tr_b16 a[22:23], %[va5] offset:25600\n\t" \
  "buffer_load_dwordx4 %[vv1], %[rv], %[sv] offen offset:2048 nt lds\n\t" \
  "v_mfma_f32_16x16x32_bf16 v[76:79], a[36:39], v[196:199], v[76:79]\n\t" \
  "ds_read_b64_tr_b16 a[24:25], %[va6] offset:24576\n\t" \
  "v_exp_f32 v208, v184\n\t" \
  "v_mfma_f32_16x16x32_bf16 v[84:87], a[40:43], v[196:199], v[84:87]\n\t" \
  "ds_read_b64_tr_b16 a[26:27], %[va6] offset:25600\n\t" \
  "v_exp_f32 v209, v185\n\t" \
  "v_mfma_f32_16x16x32_bf16 v[92:95], a[44:47], v[196:199], v[92:95]\n\t" \
  "ds_read_b64_tr_b16 a[28:29], %[va7] offset:24576\n\t" \
  "v_exp_f32 v210, v186\n\t" \
  "v_mfma_f32_16x16x32_bf16 v[100:103], a[48:51], v[196:199], v[100:103]\n\t" \
  "buffer_load_dwordx4 %[vv1], %[rv], %[sv] offen offset:3072 nt lds\n\t" \
  "v_exp_f32 v211, v187\n\t" \
  "v_mfma_f32_16x16x32_bf16 v[108:111], a[52:55], v[196:199], v[108:111]\n\t" \
  "ds_read_b64_tr_b16 a[30:31], %[va7] offset:25600\n\t" \
  "v_exp_f32 v212, v188\n\t" \
  "v_mfma_f32_16x16x32_bf16 v[116:119], a[56:59], v[196:199], v[116:119]\n\t" \
  "v_exp_f32 v213, v189\n\t" \
  "v_mfma_f32_16x16x32_bf16 v[124:127], a[60:63], v[196:199], v[124:127]\n\t" \
  "v_exp_f32 v214, v190\n\t" \
  "v_mfma_f32_16x16x32_bf16 v[132:135], a[128:131], v[196:199], v[132:135]\n\t" \
  "s_sub_u32 %[cnt], %[cnt], 1\n\t" \
  "v_exp_f32 v215, v191\n\t" \
  "s_cmp_lg_u32 %[cnt], 0\n\t" \
  "s_cbranch_scc1 .Lloop_%=\n\t" \
  "s_waitcnt vmcnt(16) lgkmcnt(0)\n\t" \
  "s_add_u32 m0, %[mw], 16384\n\t" \
  "s_add_u32 %[st], %[so0], 0\n\t" \
  "s_barrier\n\t" \
  "v_mfma_f32_16x16x32_bf16 v[176:179], a[96:99], v[136:139], v[200:203]\n\t" \
  "buffer_load_dwordx4 %[ov0], %[ro], %[st] offen offset:0 lds\n\t" \
  "v_cvt_pk_bf16_f32 v196, v208, v209\n\t" \
  "v_mfma_f32_16x16x32_bf16 v[180:183], a[112:115], v[136:139], v[200:203]\n\t" \
  "ds_read_b128 a[64:67], %[ka0] offset:32768\n\t" \
  "v_cvt_pk_bf16_f32 v197, v210, v211\n\t" \
  "v_mfma_f32_16x16x32_bf16 v[176:179], a[100:103], v[140:143], v[176:179]\n\t" \
  "ds_read_b128 a[80:83], %[ka0] offset:33792\n\t" \
  "v_cvt_pk_bf16_f32 v198, v212, v213\n\t" \
  "v_mfma_f32_16x16x32_bf16 v[180:183], a[116:119], v[140:143], v[180:183]\n\t" \
  "ds_read_b128 a[68:71], %[ka1] offset:32768\n\t" \
  "v_cvt_pk_bf16_f32 v199, v214, v215\n\t" \
  "v_mfma_f32_16x16x32_bf16 v[176:179], a[104:107], v[144:147], v[176:179]\n\t" \
  "buffer_load_dwordx4 %[ov1], %[ro], %[st] offen offset:1024 lds\n\t" \
  "v_or3_b32 v224, v196, v197, v224\n\t" \
  "v_mfma_f32_16x16x32_bf16 v[180:183], a[120:123], v[144:147], v[180:183]\n\t" \
  "ds_read_b128 a[84:87], %[ka1] offset:33792\n\t" \
  "v_or3_b32 v224, v198, v199, v224\n\t" \
  "v_mfma_f32_16x16x32_bf16 v[176:179], a[108:111], v[148:151], v[176:179]\n\t" \
  "ds_read_b128 a[72:75], %[ka2] offset:32768\n\t" \
  "v_and_b32 v216, 0x40004000, v224\n\t" \
  "v_mfma_f32_16x16x32_bf16 v[180:183], a[124:127], v[148:151], v[180:183]\n\t" \
  "v_cmp_ne_u32 vcc, 0, v216\n\t" \
  "s_cbranch_vccnz .Lrare4_%=\n\t" \
  ".Lback4_%=:\n\t" \
  "v_mfma_f32_16x16x32_bf16 v[64:67], a[0:3], v[192:195], v[64:67]\n\t" \
  "ds_read_b128 a[88:91], %[ka2] offset:33792\n\t" \
  "buffer_load_dwordx4 %[ov2], %[ro], %[st] offen offset:2048 lds\n\t" \
  "v_mfma_f32_16x16x32_bf16 v[72:75], a[4:7], v[192:195], v[72:75]\n\t" \
  "ds_read_b128 a[76:79], %[ka3] offset:32768\n\t" \
  "v_exp_f32 v208, v176\n\t" \
  "v_mfma_f32_16x16x32_bf16 v[80:83], a[8:11], v[192:195], v[80:83]\n\t" \
  "ds_read_b128 a[92:95], %[ka3] offset:33792\n\t" \
  "v_exp_f32 v209, v177\n\t" \
  "v_mfma_f32_16x16x32_bf16 v[88:91], a[12:15], v[192:195], v[88:91]\n\t" \
  "ds_read_b64_tr_b16 a[32:33], %[va0] offset:40960\n\t" \
  "v_exp_f32 v210, v178\n\t" \
  "v_mfma_f32_16x16x32_bf16 v[96:99], a[16:19], v[192:195], v[96:99]\n\t" \
  "buffer_load_dwordx4 %[ov3], %[ro], %[st] offen offset:3072 lds\n\t" \
  "v_exp_f32 v211, v179\n\t" \
  "v_mfma_f32_16x16x32_bf16 v[104:107], a[20:23], v[192:195], v[104:107]\n\t" \
  "ds_read_b64_tr_b16 a[34:35], %[va0] offset:41984\n\t" \
  "v_exp_f32 v212, v180\n\t" \
  "v_mfma_f32_16x16x32_bf16 v[112:115], a[24:27], v[192:195], v[112:115]\n\t" \
  "ds_read_b64_tr_b16 a[36:37], %[va1] offset:40960\n\t" \
  "v_exp_f32 v213, v181\n\t" \
  "v_mfma_f32_16x16x32_bf16 v[120:123], a[28:31], v[192:195], v[120:123]\n\t" \
  "s_add_u32 m0, %[mw], 24576\n\t" \
  "v_exp_f32 v214, v182\n\t" \
  "v_mfma_f32_16x16x32_bf16 v[128:131], a[128:131], v[192:195], v[128:131]\n\t" \
  "s_add_u32 %[st], %[so0], 32768\n\t" \
  "v_exp_f32 v215, v183\n\t" \
  "v_mfma_f32_16x16x32_bf16 v[184:187], a[96:99], v[152:155], v[204:207]\n\t" \
  "buffer_load_dwordx4 %[ov0], %[ro], %[st] offen offset:0 lds\n\t" \
  "v_cvt_pk_bf16_f32 v192, v208, v209\n\t" \
  "v_mfma_f32_16x16x32_bf16 v[188:191], a[112:115], v[152:155], v[204:207]\n\t" \
  "ds_read_b64_tr_b16 a[38:39], %[va1] offset:41984\n\t" \
  "v_cvt_pk_bf16_f32 v193, v210, v211\n\t" \
  "v_mfma_f32_16x16x32_bf16 v[184:187], a[100:103], v[156:159], v[184:187]\n\t" \
  "ds_read_b64_tr_b16 a[40:41], %[va2] offset:40960\n\t" \
  "v_cvt_pk_bf16_f32 v194, v212, v213\n\t" \
  "v_mfma_f32_16x16x32_bf16 v[188:191], a[116:119], v[156:159], v[188:191]\n\t" \
  "ds_read_b64_tr_b16 a[42:43], %[va2] offset:41984\n\t" \
  "v_cvt_pk_bf16_f32 v195, v214, v215\n\t" \
  "v_mfma_f32_16x16x32_bf16 v[184:187], a[104:107], v[160:163], v[184:187]\n\t" \
  "buffer_load_dwordx4 %[ov1], %[ro], %[st] offen offset:1024 lds\n\t" \
  "v_or_b32 v224, v192, v193\n\t" \
  "v_mfma_f32_16x16x32_bf16 v[188:191], a[120:123], v[160:163], v[188:191]\n\t" \
  "ds_read_b64_tr_b16 a[44:45], %[va3] offset:40960\n\t" \
  "v_or3_b32 v224, v194, v195, v224\n\t" \
  "v_mfma_f32_16x16x32_bf16 v[184:187], a[108:111], v[164:167], v[184:187]\n\t" \
  "ds_read_b64_tr_b16 a[46:47], %[va3] offset:41984\n\t" \
  "ds_read_b64_tr_b16 a[48:49], %[va4] offset:40960\n\t" \
  "v_mfma_f32_16x16x32_bf16 v[188:191], a[124:127], v[164:167], v[188:191]\n\t" \
  "ds_read_b64_tr_b16 a[50:51], %[va4] offset:41984\n\t" \
  "ds_read_b64_tr_b16 a[52:53], %[va5] offset:40960\n\t" \
  "v_mfma_f32_16x16x32_bf16 v[68:71], a[0:3], v[196:199], v[68:71]\n\t" \
  "ds_read_b64_tr_b16 a[54:55], %[va5] offset:41984\n\t" \
  "buffer_load_dwordx4 %[ov2], %[ro], %[st] offen offset:2048 lds\n\t" \
  "v_mfma_f32_16x16x32_bf16 v[76:79], a[4:7], v[196:199], v[76:79]\n\t" \
  "ds_read_b64_tr_b16 a[56:57], %[va6] offset:40960\n\t" \
  "v_exp_f32 v208, v184\n\t" \
  "v_mfma_f32_16x16x32_bf16 v[84:87], a[8:11], v[196:199], v[84:87]\n\t" \
  "ds_read_b64_tr_b16 a[58:59], %[va6] offset:41984\n\t" \
  "v_exp_f32 v209, v185\n\t" \
  "v_mfma_f32_16x16x32_bf16 v[92:95], a[12:15], v[196:199], v[92:95]\n\t" \
  "ds_read_b64_tr_b16 a[60:61], %[va7] offset:40960\n\t" \
  "v_exp_f32 v210, v186\n\t" \
  "v_mfma_f32_16x16x32_bf16 v[100:103], a[16:19], v[196:199], v[100:103]\n\t" \
  "buffer_load_dwordx4 %[ov3], %[ro], %[st] offen offset:3072 lds\n\t" \
  "v_exp_f32 v211, v187\n\t" \
  "v_mfma_f32_16x16x32_bf16 v[108:111], a[20:23], v[196:199], v[108:111]\n\t" \
  "ds_read_b64_tr_b16 a[62:63], %[va7] offset:41984\n\t" \
  "v_exp_f32 v212, v188\n\t" \
  "v_mfma_f32_16x16x32_bf16 v[116:119], a[24:27], v[196:199], v[116:119]\n\t" \
  "v_exp_f32 v213, v189\n\t" \
  "v_mfma_f32_16x16x32_bf16 v[124:127], a[28:31], v[196:199], v[124:127]\n\t" \
  "v_exp_f32 v214, v190\n\t" \
  "v_mfma_f32_16x16x32_bf16 v[132:135], a[128:131], v[196:199], v[132:135]\n\t" \
  "v_exp_f32 v215, v191\n\t" \
  "s_waitcnt vmcnt(16) lgkmcnt(0)\n\t" \
  "s_add_u32 m0, %[mw], 32768\n\t" \
  "s_add_u32 %[st], %[so0], 65536\n\t" \
  "s_barrier\n\t" \
  "v_mfma_f32_16x16x32_bf16 v[168:171], a[64:67], v[136:139], v[200:203]\n\t" \
  "buffer_load_dwordx4 %[ov0], %[ro], %[st] offen offset:0 lds\n\t" \
  "v_cvt_pk_bf16_f32 v196, v208, v209\n\t" \
  "v_mfma_f32_16x16x32_bf16 v[172:175], a[80:83], v[136:139], v[200:203]\n\t" \
  "ds_read_b128 a[96:99], %[ka0] offset:49152\n\t" \
  "v_cvt_pk_bf16_f32 v197, v210, v211\n\t" \
  "v_mfma_f32_16x16x32_bf16 v[168:171], a[68:71], v[140:143], v[168:171]\n\t" \
  "ds_read_b128 a[112:115], %[ka0] offset:50176\n\t" \
  "v_cvt_pk_bf16_f32 v198, v212, v213\n\t" \
  "v_mfma_f32_16x16x32_bf16 v[172:175], a[84:87], v[140:143], v[172:175]\n\t" \
  "ds_read_b128 a[100:103], %[ka1] offset:49152\n\t" \
  "v_cvt_pk_bf16_f32 v199, v214, v215\n\t" \
  "v_mfma_f32_16x16x32_bf16 v[168:171], a[72:75], v[144:147], v[168:171]\n\t" \
  "buffer_load_dwordx4 %[ov1], %[ro], %[st] offen offset:1024 lds\n\t" \
  "v_or3_b32 v224, v196, v197, v224\n\t" \
  "v_mfma_f32_16x16x32_bf16 v[172:175], a[88:91], v[144:147], v[172:175]\n\t" \
  "ds_read_b128 a[116:119], %[ka1] offset:50176\n\t" \
  "v_or3_b32 v224, v198, v199, v224\n\t" \
  "v_mfma_f32_16x16x32_bf16 v[168:171], a[76:79], v[148:151], v[168:171]\n\t" \
  "ds_read_b128 a[104:107], %[ka2] offset:49152\n\t" \
  "v_and_b32 v216, 0x40004000, v224\n\t" \
  "v_mfma_f32_16x16x32_bf16 v[172:175], a[92:95], v[148:151], v[172:175]\n\t" \
  "v_cmp_ne_u32 vcc, 0, v216\n\t" \
  "s_cbranch_vccnz .Lrare5_%=\n\t" \
  ".Lback5_%=:\n\t" \
  "v_mfma_f32_16x16x32_bf16 v[64:67], a[32:35], v[192:195], v[64:67]\n\t" \
  "ds_read_b128 a[120:123], %[ka2] offset:50176\n\t" \
  "buffer_load_dwordx4 %[ov2], %[ro], %[st] offen offset:2048 lds\n\t" \
  "v_mfma_f32_16x16x32_bf16 v[72:75], a[36:39], v[192:195], v[72:75]\n\t" \
  "ds_read_b128 a[108:111], %[ka3] offset:49152\n\t" \
  "v_exp_f32 v208, v168\n\t" \
  "v_mfma_f32_16x16x32_bf16 v[80:83], a[40:43], v[192:195], v[80:83]\n\t" \
  "ds_read_b128 a[124:127], %[ka3] offset:50176\n\t" \
  "v_exp_f32 v209, v169\n\t" \
  "v_mfma_f32_16x16x32_bf16 v[88:91], a[44:47], v[192:195], v[88:91]\n\t" \
  "ds_read_b64_tr_b16 a[0:1], %[va0] offset:57344\n\t" \
  "v_exp_f32 v210, v170\n\t" \
  "v_mfma_f32_16x16x32_bf16 v[96:99], a[48:51], v[192:195], v[96:99]\n\t" \
  "buffer_load_dwordx4 %[ov3], %[ro], %[st] offen offset:3072 lds\n\t" \
  "v_exp_f32 v211, v171\n\t" \
  "v_mfma_f32_16x16x32_bf16 v[104:107], a[52:55], v[192:195], v[104:107]\n\t" \
  "ds_read_b64_tr_b16 a[2:3], %[va0] offset:58368\n\t" \
  "v_exp_f32 v212, v172\n\t" \
  "v_mfma_f32_16x16x32_bf16 v[112:115], a[56:59], v[192:195], v[112:115]\n\t" \
  "ds_read_b64_tr_b16 a[4:5], %[va1] offset:57344\n\t" \
  "v_exp_f32 v213, v173\n\t" \
  "v_mfma_f32_16x16x32_bf16 v[120:123], a[60:63], v[192:195], v[120:123]\n\t" \
  "s_add_u32 m0, %[mw], 40960\n\t" \
  "v_exp_f32 v214, v174\n\t" \
  "v_mfma_f32_16x16x32_bf16 v[128:131], a[128:131], v[192:195], v[128:131]\n\t" \
  "s_add_u32 %[st], %[so0], 98304\n\t" \
  "v_exp_f32 v215, v175\n\t" \
  "v_mfma_f32_16x16x32_bf16 v[184:187], a[64:67], v[152:155], v[204:207]\n\t" \
  "buffer_load_dwordx4 %[ov0], %[ro], %[st] offen offset:0 lds\n\t" \
  "v_cvt_pk_bf16_f32 v192, v208, v209\n\t" \
  "v_mfma_f32_16x16x32_bf16 v[188:191], a[80:83], v[152:155], v[204:207]\n\t" \
  "ds_read_b64_tr_b16 a[6:7], %[va1] offset:58368\n\t" \
  "v_cvt_pk_bf16_f32 v193, v210, v211\n\t" \
  "v_mfma_f32_16x16x32_bf16 v[184:187], a[68:71], v[156:159], v[184:187]\n\t" \
  "ds_read_b64_tr_b16 a[8:9], %[va2] offset:57344\n\t" \
  "v_cvt_pk_bf16_f32 v194, v212, v213\n\t" \
  "v_mfma_f32_16x16x32_bf16 v[188:191], a[84:87], v[156:159], v[188:191]\n\t" \
  "ds_read_b64_tr_b16 a[10:11], %[va2] offset:58368\n\t" \
  "v_cvt_pk_bf16_f32 v195, v214, v215\n\t" \
  "v_mfma_f32_16x16x32_bf16 v[184:187], a[72:75], v[160:163], v[184:187]\n\t" \
  "buffer_load_dwordx4 %[ov1], %[ro], %[st] offen offset:1024 lds\n\t" \
  "v_or_b32 v224, v192, v193\n\t" \
  "v_mfma_f32_16x16x32_bf16 v[188:191], a[88:91], v[160:163], v[188:191]\n\t" \
  "ds_read_b64_tr_b16 a[12:13], %[va3] offset:57344\n\t" \
  "v_or3_b32 v224, v194, v195, v224\n\t" \
  "v_mfma_f32_16x16x32_bf16 v[184:187], a[76:79], v[164:167], v[184:187]\n\t" \
  "ds_read_b64_tr_b16 a[14:15], %[va3] offset:58368\n\t" \
  "ds_read_b64_tr_b16 a[16:17], %[va4] offset:57344\n\t" \
  "v_mfma_f32_16x16x32_bf16 v[188:191], a[92:95], v[164:167], v[188:191]\n\t" \
  "ds_read_b64_tr_b16 a[18:19], %[va4] offset:58368\n\t" \
  "ds_read_b64_tr_b16 a[20:21], %[va5] offset:57344\n\t" \
  "v_mfma_f32_16x16x32_bf16 v[68:71], a[32:35], v[196:199], v[68:71]\n\t" \
  "ds_read_b64_tr_b16 a[22:23], %[va5] offset:58368\n\t" \
  "buffer_load_dwordx4 %[ov2], %[ro], %[st] offen offset:2048 lds\n\t" \
  "v_mfma_f32_16x16x32_bf16 v[76:79], a[36:39], v[196:199], v[76:79]\n\t" \
  "ds_read_b64_tr_b16 a[24:25], %[va6] offset:57344\n\t" \
  "v_exp_f32 v208, v184\n\t" \
  "v_mfma_f32_16x16x32_bf16 v[84:87], a[40:43], v[196:199], v[84:87]\n\t" \
  "ds_read_b64_tr_b16 a[26:27], %[va6] offset:58368\n\t" \
  "v_exp_f32 v209, v185\n\t" \
  "v_mfma_f32_16x16x32_bf16 v[92:95], a[44:47], v[196:199], v[92:95]\n\t" \
  "ds_read_b64_tr_b16 a[28:29], %[va7] offset:57344\n\t" \
  "v_exp_f32 v210, v186\n\t" \
  "v_mfma_f32_16x16x32_bf16 v[100:103], a[48:51], v[196:199], v[100:103]\n\t" \
  "buffer_load_dwordx4 %[ov3], %[ro], %[st] offen offset:3072 lds\n\t" \
  "v_exp_f32 v211, v187\n\t" \
  "v_mfma_f32_16x16x32_bf16 v[108:111], a[52:55], v[196:199], v[108:111]\n\t" \
  "ds_read_b64_tr_b16 a[30:31], %[va7] offset:58368\n\t" \
  "v_exp_f32 v212, v188\n\t" \
  "v_mfma_f32_16x16x32_bf16 v[116:119], a[56:59], v[196:199], v[116:119]\n\t" \
  "v_exp_f32 v213, v189\n\t" \
  "v_mfma_f32_16x16x32_bf16 v[124:127], a[60:63], v[196:199], v[124:127]\n\t" \
  "v_exp_f32 v214, v190\n\t" \
  "v_mfma_f32_16x16x32_bf16 v[132:135], a[128:131], v[196:199], v[132:135]\n\t" \
  "v_exp_f32 v215, v191\n\t" \
  "s_waitcnt lgkmcnt(0)\n\t" \
  "s_add_u32 m0, %[mw], 49152\n\t" \
  "s_add_u32 %[st], %[so1], 0\n\t" \
  "s_barrier\n\t" \
  "v_mfma_f32_16x16x32_bf16 v[176:179], a[96:99], v[136:139], v[200:203]\n\t" \
  "buffer_load_dwordx4 %[ov0], %[ro], %[st] offen offset:0 lds\n\t" \
  "v_cvt_pk_bf16_f32 v196, v208, v209\n\t" \
  "v_mfma_f32_16x16x32_bf16 v[180:183], a[112:115], v[136:139], v[200:203]\n\t" \
  "v_cvt_pk_bf16_f32 v197, v210, v211\n\t" \
  "v_mfma_f32_16x16x32_bf16 v[176:179], a[100:103], v[140:143], v[176:179]\n\t" \
  "v_cvt_pk_bf16_f32 v198, v212, v213\n\t" \
  "v_mfma_f32_16x16x32_bf16 v[180:183], a[116:119], v[140:143], v[180:183]\n\t" \
  "buffer_load_dwordx4 %[ov1], %[ro], %[st] offen offset:1024 lds\n\t" \
  "v_cvt_pk_bf16_f32 v199, v214, v215\n\t" \
  "v_mfma_f32_16x16x32_bf16 v[176:179], a[104:107], v[144:147], v[176:179]\n\t" \
  "v_or3_b32 v224, v196, v197, v224\n\t" \
  "v_mfma_f32_16x16x32_bf16 v[180:183], a[120:123], v[144:147], v[180:183]\n\t" \
  "v_or3_b32 v224, v198, v199, v224\n\t" \
  "v_mfma_f32_16x16x32_bf16 v[176:179], a[108:111], v[148:151], v[176:179]\n\t" \
  "s_nop 0\n\t" \
  "buffer_load_dwordx4 %[ov2], %[ro], %[st] offen offset:2048 lds\n\t" \
  "v_and_b32 v216, 0x40004000, v224\n\t" \
  "v_mfma_f32_16x16x32_bf16 v[180:183], a[124:127], v[148:151], v[180:183]\n\t" \
  "v_cmp_ne_u32 vcc, 0, v216\n\t" \
  "s_cbranch_vccnz .Lrare6_%=\n\t" \
  ".Lback6_%=:\n\t" \
  "v_mfma_f32_16x16x32_bf16 v[64:67], a[0:3], v[192:195], v[64:67]\n\t" \
  "v_mfma_f32_16x16x32_bf16 v[72:75], a[4:7], v[192:195], v[72:75]\n\t" \
  "buffer_load_dwordx4 %[ov3], %[ro], %[st] offen offset:3072 lds\n\t" \
  "v_exp_f32 v208, v176\n\t" \
  "v_mfma_f32_16x16x32_bf16 v[80:83], a[8:11], v[192:195], v[80:83]\n\t" \
  "s_add_u32 m0, %[mw], 57344\n\t" \
  "v_exp_f32 v209, v177\n\t" \
  "v_mfma_f32_16x16x32_bf16 v[88:91], a[12:15], v[192:195], v[88:91]\n\t" \
  "s_add_u32 %[st], %[so1], 32768\n\t" \
  "v_exp_f32 v210, v178\n\t" \
  "v_mfma_f32_16x16x32_bf16 v[96:99], a[16:19], v[192:195], v[96:99]\n\t" \
  "buffer_load_dwordx4 %[ov0], %[ro], %[st] offen offset:0 lds\n\t" \
  "v_exp_f32 v211, v179\n\t" \
  "v_mfma_f32_16x16x32_bf16 v[104:107], a[20:23], v[192:195], v[104:107]\n\t" \
  "v_exp_f32 v212, v180\n\t" \
  "v_mfma_f32_16x16x32_bf16 v[112:115], a[24:27], v[192:195], v[112:115]\n\t" \
  "v_exp_f32 v213, v181\n\t" \
  "v_mfma_f32_16x16x32_bf16 v[120:123], a[28:31], v[192:195], v[120:123]\n\t" \
  "buffer_load_dwordx4 %[ov1], %[ro], %[st] offen offset:1024 lds\n\t" \
  "v_exp_f32 v214, v182\n\t" \
  "v_mfma_f32_16x16x32_bf16 v[128:131], a[128:131], v[192:195], v[128:131]\n\t" \
  "v_exp_f32 v215, v183\n\t" \
  "v_mfma_f32_16x16x32_bf16 v[184:187], a[96:99], v[152:155], v[204:207]\n\t" \
  "v_cvt_pk_bf16_f32 v192, v208, v209\n\t" \
  "v_mfma_f32_16x16x32_bf16 v[188:191], a[112:115], v[152:155], v[204:207]\n\t" \
  "buffer_load_dwordx4 %[ov2], %[ro], %[st] offen offset:2048 lds\n\t" \
  "v_cvt_pk_bf16_f32 v193, v210, v211\n\t" \
  "v_mfma_f32_16x16x32_bf16 v[184:187], a[100:103], v[156:159], v[184:187]\n\t" \
  "v_cvt_pk_bf16_f32 v194, v212, v213\n\t" \
  "v_mfma_f32_16x16x32_bf16 v[188:191], a[116:119], v[156:159], v[188:191]\n\t" \
  "v_cvt_pk_bf16_f32 v195, v214, v215\n\t" \
  "v_mfma_f32_16x16x32_bf16 v[184:187], a[104:107], v[160:163], v[184:187]\n\t" \
  "buffer_load_dwordx4 %[ov3], %[ro], %[st] offen offset:3072 lds\n\t" \
  "v_or_b32 v224, v192, v193\n\t" \
  "v_mfma_f32_16x16x32_bf16 v[188:191], a[120:123], v[160:163], v[188:191]\n\t" \
  "s_add_u32 m0, %[mw], 0\n\t" \
  "v_or3_b32 v224, v194, v195, v224\n\t" \
  "v_mfma_f32_16x16x32_bf16 v[184:187], a[108:111], v[164:167], v[184:187]\n\t" \
  "s_nop 0\n\t" \
  "s_add_u32 %[st], %[so1], 65536\n\t" \
  "v_mfma_f32_16x16x32_bf16 v[188:191], a[124:127], v[164:167], v[188:191]\n\t" \
  "s_nop 0\n\t" \
  "s_nop 0\n\t" \
  "buffer_load_dwordx4 %[ov0], %[ro], %[st] offen offset:0 lds\n\t" \
  "v_mfma_f32_16x16x32_bf16 v[68:71], a[0:3], v[196:199], v[68:71]\n\t" \
  "s_nop 0\n\t" \
  "v_mfma_f32_16x16x32_bf16 v[76:79], a[4:7], v[196:199], v[76:79]\n\t" \
  "v_exp_f32 v208, v184\n\t" \
  "v_mfma_f32_16x16x32_bf16 v[84:87], a[8:11], v[196:199], v[84:87]\n\t" \
  "buffer_load_dwordx4 %[ov1], %[ro], %[st] offen offset:1024 lds\n\t" \
  "v_exp_f32 v209, v185\n\t" \
  "v_mfma_f32_16x16x32_bf16 v[92:95], a[12:15], v[196:199], v[92:95]\n\t" \
  "v_exp_f32 v210, v186\n\t" \
  "v_mfma_f32_16x16x32_bf16 v[100:103], a[16:19], v[196:199], v[100:103]\n\t" \
  "v_exp_f32 v211, v187\n\t" \
  "v_mfma_f32_16x16x32_bf16 v[108:111], a[20:23], v[196:199], v[108:111]\n\t" \
  "buffer_load_dwordx4 %[ov2], %[ro], %[st] offen offset:2048 lds\n\t" \
  "v_exp_f32 v212, v188\n\t" \
  "v_mfma_f32_16x16x32_bf16 v[116:119], a[24:27], v[196:199], v[116:119]\n\t" \
  "v_exp_f32 v213, v189\n\t" \
  "v_mfma_f32_16x16x32_bf16 v[124:127], a[28:31], v[196:199], v[124:127]\n\t" \
  "v_exp_f32 v214, v190\n\t" \
  "v_mfma_f32_16x16x32_bf16 v[132:135], a[128:131], v[196:199], v[132:135]\n\t" \
  "buffer_load_dwordx4 %[ov3], %[ro], %[st] offen offset:3072 lds\n\t" \
  "v_exp_f32 v215, v191\n\t" \
  "s_nop 0\n\t" \
  "v_cvt_pk_bf16_f32 v196, v208, v209\n\t" \
  "v_cvt_pk_bf16_f32 v197, v210, v211\n\t" \
  "v_cvt_pk_bf16_f32 v198, v212, v213\n\t" \
  "v_cvt_pk_bf16_f32 v199, v214, v215\n\t" \
  "v_or3_b32 v224, v196, v197, v224\n\t" \
  "v_or3_b32 v224, v198, v199, v224\n\t" \
  "v_and_b32 v216, 0x40004000, v224\n\t" \
  "v_cmp_ne_u32 vcc, 0, v216\n\t" \
  "s_cbranch_vccnz .Lrare7_%=\n\t" \
  ".Lback7_%=:\n\t" \
  "v_mfma_f32_16x16x32_bf16 v[128:131], a[128:131], v[192:195], v[128:131]\n\t" \
  "v_mfma_f32_16x16x32_bf16 v[132:135], a[128:131], v[196:199], v[132:135]\n\t" \
  "s_waitcnt vmcnt(16)\n\t" \
  "s_barrier\n\t" \
  "ds_read_b64_tr_b16 a[32:33], %[va0] offset:8192\n\t" \
  "ds_read_b64_tr_b16 a[34:35], %[va0] offset:9216\n\t" \
  "ds_read_b64_tr_b16 a[36:37], %[va1] offset:8192\n\t" \
  "ds_read_b64_tr_b16 a[38:39], %[va1] offset:9216\n\t" \
  "ds_read_b64_tr_b16 a[40:41], %[va2] offset:8192\n\t" \
  "ds_read_b64_tr_b16 a[42:43], %[va2] offset:9216\n\t" \
  "ds_read_b64_tr_b16 a[44:45], %[va3] offset:8192\n\t" \
  "ds_read_b64_tr_b16 a[46:47], %[va3] offset:9216\n\t" \
  "ds_read_b64_tr_b16 a[48:49], %[va4] offset:8192\n\t" \
  "ds_read_b64_tr_b16 a[50:51], %[va4] offset:9216\n\t" \
  "ds_read_b64_tr_b16 a[52:53], %[va5] offset:8192\n\t" \
  "ds_read_b64_tr_b16 a[54:55], %[va5] offset:9216\n\t" \
  "ds_read_b64_tr_b16 a[56:57], %[va6] offset:8192\n\t" \
  "ds_read_b64_tr_b16 a[58:59], %[va6] offset:9216\n\t" \
  "ds_read_b64_tr_b16 a[60:61], %[va7] offset:8192\n\t" \
  "ds_read_b64_tr_b16 a[62:63], %[va7] offset:9216\n\t" \
  "buffer_store_dwordx2 v[226:227], %[ev], %[rs], 0 offen sc1\n\t" \
  "ds_read_b64_tr_b16 a[0:1], %[va0] offset:32768\n\t" \
  "ds_read_b64_tr_b16 a[2:3], %[va0] offset:33792\n\t" \
  "ds_read_b64_tr_b16 a[4:5], %[va1] offset:32768\n\t" \
  "ds_read_b64_tr_b16 a[6:7], %[va1] offset:33792\n\t" \
  "ds_read_b64_tr_b16 a[8:9], %[va2] offset:32768\n\t" \
  "ds_read_b64_tr_b16 a[10:11], %[va2] offset:33792\n\t" \
  "ds_read_b64_tr_b16 a[12:13], %[va3] offset:32768\n\t" \
  "ds_read_b64_tr_b16 a[14:15], %[va3] offset:33792\n\t" \
  "v_rcp_f32 v216, v128\n\t" \
  "v_rcp_f32 v218, v132\n\t" \
  "s_waitcnt lgkmcnt(8)\n\t" \
  "v_mov_b32 v217, v216\n\t" \
  "v_mov_b32 v219, v218\n\t" \
  "v_mfma_f32_16x16x32_bf16 v[64:67], a[32:35], v[192:195], v[64:67]\n\t" \
  "ds_read_b64_tr_b16 a[64:65], %[va0] offset:16384\n\t" \
  "ds_read_b64_tr_b16 a[66:67], %[va0] offset:17408\n\t" \
  "v_mfma_f32_16x16x32_bf16 v[72:75], a[36:39], v[192:195], v[72:75]\n\t" \
  "ds_read_b64_tr_b16 a[68:69], %[va1] offset:16384\n\t" \
  "ds_read_b64_tr_b16 a[70:71], %[va1] offset:17408\n\t" \
  "v_mfma_f32_16x16x32_bf16 v[80:83], a[40:43], v[192:195], v[80:83]\n\t" \
  "ds_read_b64_tr_b16 a[72:73], %[va2] offset:16384\n\t" \
  "ds_read_b64_tr_b16 a[74:75], %[va2] offset:17408\n\t" \
  "v_mfma_f32_16x16x32_bf16 v[88:91], a[44:47], v[192:195], v[88:91]\n\t" \
  "ds_read_b64_tr_b16 a[76:77], %[va3] offset:16384\n\t" \
  "ds_read_b64_tr_b16 a[78:79], %[va3] offset:17408\n\t" \
  "v_mfma_f32_16x16x32_bf16 v[96:99], a[48:51], v[192:195], v[96:99]\n\t" \
  "ds_read_b64_tr_b16 a[80:81], %[va4] offset:16384\n\t" \
  "ds_read_b64_tr_b16 a[82:83], %[va4] offset:17408\n\t" \
  "v_mfma_f32_16x16x32_bf16 v[104:107], a[52:55], v[192:195], v[104:107]\n\t" \
  "ds_read_b64_tr_b16 a[84:85], %[va5] offset:16384\n\t" \
  "ds_read_b64_tr_b16 a[86:87], %[va5] offset:17408\n\t" \
  "v_mfma_f32_16x16x32_bf16 v[112:115], a[56:59], v[192:195], v[112:115]\n\t" \
  "ds_read_b64_tr_b16 a[88:89], %[va6] offset:16384\n\t" \
  "ds_read_b64_tr_b16 a[90:91], %[va6] offset:17408\n\t" \
  "v_mfma_f32_16x16x32_bf16 v[120:123], a[60:63], v[192:195], v[120:123]\n\t" \
  "ds_read_b64_tr_b16 a[92:93], %[va7] offset:16384\n\t" \
  "ds_read_b64_tr_b16 a[94:95], %[va7] offset:17408\n\t" \
  "v_mfma_f32_16x16x32_bf16 v[68:71], a[32:35], v[196:199], v[68:71]\n\t" \
  "ds_read_b64_tr_b16 a[96:97], %[va0] offset:24576\n\t" \
  "ds_read_b64_tr_b16 a[98:99], %[va0] offset:25600\n\t" \
  "v_mfma_f32_16x16x32_bf16 v[76:79], a[36:39], v[196:199], v[76:79]\n\t" \
  "ds_read_b64_tr_b16 a[100:101], %[va1] offset:24576\n\t" \
  "ds_read_b64_tr_b16 a[102:103], %[va1] offset:25600\n\t" \
  "v_mfma_f32_16x16x32_bf16 v[84:87], a[40:43], v[196:199], v[84:87]\n\t" \
  "ds_read_b64_tr_b16 a[104:105], %[va2] offset:24576\n\t" \
  "ds_read_b64_tr_b16 a[106:107], %[va2] offset:25600\n\t" \
  "v_mfma_f32_16x16x32_bf16 v[92:95], a[44:47], v[196:199], v[92:95]\n\t" \
  "ds_read_b64_tr_b16 a[108:109], %[va3] offset:24576\n\t" \
  "ds_read_b64_tr_b16 a[110:111], %[va3] offset:25600\n\t" \
  "v_mfma_f32_16x16x32_bf16 v[100:103], a[48:51], v[196:199], v[100:103]\n\t" \
  "ds_read_b64_tr_b16 a[112:113], %[va4] offset:24576\n\t" \
  "ds_read_b64_tr_b16 a[114:115], %[va4] offset:25600\n\t" \
  "v_mfma_f32_16x16x32_bf16 v[108:111], a[52:55], v[196:199], v[108:111]\n\t" \
  "ds_read_b64_tr_b16 a[116:117], %[va5] offset:24576\n\t" \
  "ds_read_b64_tr_b16 a[118:119], %[va5] offset:25600\n\t" \
  "v_mfma_f32_16x16x32_bf16 v[116:119], a[56:59], v[196:199], v[116:119]\n\t" \
  "ds_read_b64_tr_b16 a[120:121], %[va6] offset:24576\n\t" \
  "ds_read_b64_tr_b16 a[122:123], %[va6] offset:25600\n\t" \
  "v_mfma_f32_16x16x32_bf16 v[124:127], a[60:63], v[196:199], v[124:127]\n\t" \
  "ds_read_b64_tr_b16 a[124:125], %[va7] offset:24576\n\t" \
  "ds_read_b64_tr_b16 a[126:127], %[va7] offset:25600\n\t" \
  "ds_read_b64_tr_b16 a[16:17], %[va4] offset:32768\n\t" \
  "ds_read_b64_tr_b16 a[18:19], %[va4] offset:33792\n\t" \
  "ds_read_b64_tr_b16 a[20:21], %[va5] offset:32768\n\t" \
  "ds_read_b64_tr_b16 a[22:23], %[va5] offset:33792\n\t" \
  "ds_read_b64_tr_b16 a[24:25], %[va6] offset:32768\n\t" \
  "ds_read_b64_tr_b16 a[26:27], %[va6] offset:33792\n\t" \
  "ds_read_b64_tr_b16 a[28:29], %[va7] offset:32768\n\t" \
  "ds_read_b64_tr_b16 a[30:31], %[va7] offset:33792\n\t" \
  "v_pk_mul_f32 v[64:65], v[64:65], v[216:217]\n\t" \
  "v_pk_mul_f32 v[66:67], v[66:67], v[216:217]\n\t" \
  "v_pk_mul_f32 v[72:73], v[72:73], v[216:217]\n\t" \
  "v_pk_mul_f32 v[74:75], v[74:75], v[216:217]\n\t" \
  "v_cvt_pk_bf16_f32 v136, v64, v65\n\t" \
  "v_cvt_pk_bf16_f32 v137, v66, v67\n\t" \
  "v_cvt_pk_bf16_f32 v138, v72, v73\n\t" \
  "v_cvt_pk_bf16_f32 v139, v74, v75\n\t" \
  "ds_write_b128 %[oa], v[136:139] offset:0\n\t" \
  "v_pk_mul_f32 v[80:81], v[80:81], v[216:217]\n\t" \
  "v_pk_mul_f32 v[82:83], v[82:83], v[216:217]\n\t" \
  "v_pk_mul_f32 v[88:89], v[88:89], v[216:217]\n\t" \
  "v_pk_mul_f32 v[90:91], v[90:91], v[216:217]\n\t" \
  "v_cvt_pk_bf16_f32 v140, v80, v81\n\t" \
  "v_cvt_pk_bf16_f32 v141, v82, v83\n\t" \
  "v_cvt_pk_bf16_f32 v142, v88, v89\n\t" \
  "v_cvt_pk_bf16_f32 v143, v90, v91\n\t" \
  "ds_write_b128 %[oa], v[140:143] offset:1024\n\t" \
  "v_pk_mul_f32 v[96:97], v[96:97], v[216:217]\n\t" \
  "v_pk_mul_f32 v[98:99], v[98:99], v[216:217]\n\t" \
  "v_pk_mul_f32 v[104:105], v[104:105], v[216:217]\n\t" \
  "v_pk_mul_f32 v[106:107], v[106:107], v[216:217]\n\t" \
  "v_cvt_pk_bf16_f32 v144, v96, v97\n\t" \
  "v_cvt_pk_bf16_f32 v145, v98, v99\n\t" \
  "v_cvt_pk_bf16_f32 v146, v104, v105\n\t" \
  "v_cvt_pk_bf16_f32 v147, v106, v107\n\t" \
  "ds_write_b128 %[oa], v[144:147] offset:2048\n\t" \
  "v_pk_mul_f32 v[112:113], v[112:113], v[216:217]\n\t" \
  "v_pk_mul_f32 v[114:115], v[114:115], v[216:217]\n\t" \
  "v_pk_mul_f32 v[120:121], v[120:121], v[216:217]\n\t" \
  "v_pk_mul_f32 v[122:123], v[122:123], v[216:217]\n\t" \
  "v_cvt_pk_bf16_f32 v148, v112, v113\n\t" \
  "v_cvt_pk_bf16_f32 v149, v114, v115\n\t" \
  "v_cvt_pk_bf16_f32 v150, v120, v121\n\t" \
  "v_cvt_pk_bf16_f32 v151, v122, v123\n\t" \
  "ds_write_b128 %[oa], v[148:151] offset:3072\n\t" \
  "s_waitcnt lgkmcnt(4)\n\t" \
  "v_mfma_f32_16x16x32_bf16 v[64:67], a[64:67], v[136:139], 0\n\t" \
  "v_pk_mul_f32 v[68:69], v[68:69], v[218:219]\n\t" \
  "v_pk_mul_f32 v[70:71], v[70:71], v[218:219]\n\t" \
  "v_mfma_f32_16x16x32_bf16 v[72:75], a[68:71], v[136:139], 0\n\t" \
  "v_pk_mul_f32 v[76:77], v[76:77], v[218:219]\n\t" \
  "v_pk_mul_f32 v[78:79], v[78:79], v[218:219]\n\t" \
  "v_mfma_f32_16x16x32_bf16 v[80:83], a[72:75], v[136:139], 0\n\t" \
  "v_cvt_pk_bf16_f32 v152, v68, v69\n\t" \
  "v_cvt_pk_bf16_f32 v153, v70, v71\n\t" \
  "v_mfma_f32_16x16x32_bf16 v[88:91], a[76:79], v[136:139], 0\n\t" \
  "v_cvt_pk_bf16_f32 v154, v76, v77\n\t" \
  "v_cvt_pk_bf16_f32 v155, v78, v79\n\t" \
  "ds_write_b128 %[oa], v[152:155] offset:4096\n\t" \
  "v_mfma_f32_16x16x32_bf16 v[96:99], a[80:83], v[136:139], 0\n\t" \
  "v_pk_mul_f32 v[84:85], v[84:85], v[218:219]\n\t" \
  "v_pk_mul_f32 v[86:87], v[86:87], v[218:219]\n\t" \
  "v_mfma_f32_16x16x32_bf16 v[104:107], a[84:87], v[136:139], 0\n\t" \
  "v_pk_mul_f32 v[92:93], v[92:93], v[218:219]\n\t" \
  "v_pk_mul_f32 v[94:95], v[94:95], v[218:219]\n\t" \
  "v_mfma_f32_16x16x32_bf16 v[112:115], a[88:91], v[136:139], 0\n\t" \
  "v_cvt_pk_bf16_f32 v156, v84, v85\n\t" \
  "v_cvt_pk_bf16_f32 v157, v86, v87\n\t" \
  "v_mfma_f32_16x16x32_bf16 v[120:123], a[92:95], v[136:139], 0\n\t" \
  "v_cvt_pk_bf16_f32 v158, v92, v93\n\t" \
  "v_cvt_pk_bf16_f32 v159, v94, v95\n\t" \
  "ds_write_b128 %[oa], v[156:159] offset:5120\n\t" \
  "v_mfma_f32_16x16x32_bf16 v[64:67], a[96:99], v[140:143], v[64:67]\n\t" \
  "v_pk_mul_f32 v[100:101], v[100:101], v[218:219]\n\t" \
  "v_pk_mul_f32 v[102:103], v[102:103], v[218:219]\n\t" \
  "v_mfma_f32_16x16x32_bf16 v[72:75], a[100:103], v[140:143], v[72:75]\n\t" \
  "v_pk_mul_f32 v[108:109], v[108:109], v[218:219]\n\t" \
  "v_pk_mul_f32 v[110:111], v[110:111], v[218:219]\n\t" \
  "v_mfma_f32_16x16x32_bf16 v[80:83], a[104:107], v[140:143], v[80:83]\n\t" \
  "v_cvt_pk_bf16_f32 v160, v100, v101\n\t" \
  "v_cvt_pk_bf16_f32 v161, v102, v103\n\t" \
  "v_mfma_f32_16x16x32_bf16 v[88:91], a[108:111], v[140:143], v[88:91]\n\t" \
  "v_cvt_pk_bf16_f32 v162, v108, v109\n\t" \
  "v_cvt_pk_bf16_f32 v163, v110, v111\n\t" \
  "ds_write_b128 %[oa], v[160:163] offset:6144\n\t" \
  "v_mfma_f32_16x16x32_bf16 v[96:99], a[112:115], v[140:143], v[96:99]\n\t" \
  "v_pk_mul_f32 v[116:117], v[116:117], v[218:219]\n\t" \
  "v_pk_mul_f32 v[118:119], v[118:119], v[218:219]\n\t" \
  "v_mfma_f32_16x16x32_bf16 v[104:107], a[116:119], v[140:143], v[104:107]\n\t" \
  "v_pk_mul_f32 v[124:125], v[124:125], v[218:219]\n\t" \
  "v_pk_mul_f32 v[126:127], v[126:127], v[218:219]\n\t" \
  "v_mfma_f32_16x16x32_bf16 v[112:115], a[120:123], v[140:143], v[112:115]\n\t" \
  "v_cvt_pk_bf16_f32 v164, v116, v117\n\t" \
  "v_cvt_pk_bf16_f32 v165, v118, v119\n\t" \
  "v_mfma_f32_16x16x32_bf16 v[120:123], a[124:127], v[140:143], v[120:123]\n\t" \
  "v_cvt_pk_bf16_f32 v166, v124, v125\n\t" \
  "v_cvt_pk_bf16_f32 v167, v126, v127\n\t" \
  "ds_write_b128 %[oa], v[164:167] offset:7168\n\t" \
  "s_waitcnt vmcnt(12) lgkmcnt(0)\n\t" \
  "s_barrier\n\t" \
  "v_mfma_f32_16x16x32_bf16 v[68:71], a[64:67], v[152:155], 0\n\t" \
  "ds_read_b64_tr_b16 a[32:33], %[va0] offset:40960\n\t" \
  "ds_read_b64_tr_b16 a[34:35], %[va0] offset:41984\n\t" \
  "s_add_u32 m0, %[mw], 8192\n\t" \
  "v_mfma_f32_16x16x32_bf16 v[76:79], a[68:71], v[152:155], 0\n\t" \
  "ds_read_b64_tr_b16 a[36:37], %[va1] offset:40960\n\t" \
  "ds_read_b64_tr_b16 a[38:39], %[va1] offset:41984\n\t" \
  "s_add_u32 %[st], %[so1], 98304\n\t" \
  "v_mfma_f32_16x16x32_bf16 v[84:87], a[72:75], v[152:155], 0\n\t" \
  "ds_read_b64_tr_b16 a[40:41], %[va2] offset:40960\n\t" \
  "buffer_load_dwordx4 %[ov0], %[ro], %[st] offen offset:0 lds\n\t" \
  "v_mfma_f32_16x16x32_bf16 v[92:95], a[76:79], v[152:155], 0\n\t" \
  "ds_read_b64_tr_b16 a[42:43], %[va2] offset:41984\n\t" \
  "buffer_load_dwordx4 %[ov1], %[ro], %[st] offen offset:1024 lds\n\t" \
  "v_mfma_f32_16x16x32_bf16 v[100:103], a[80:83], v[152:155], 0\n\t" \
  "ds_read_b64_tr_b16 a[44:45], %[va3] offset:40960\n\t" \
  "ds_read_b64_tr_b16 a[46:47], %[va3] offset:41984\n\t" \
  "v_mfma_f32_16x16x32_bf16 v[108:111], a[84:87], v[152:155], 0\n\t" \
  "ds_read_b64_tr_b16 a[48:49], %[va4] offset:40960\n\t" \
  "buffer_load_dwordx4 %[ov2], %[ro], %[st] offen offset:2048 lds\n\t" \
  "v_mfma_f32_16x16x32_bf16 v[116:119], a[88:91], v[152:155], 0\n\t" \
  "ds_read_b64_tr_b16 a[50:51], %[va4] offset:41984\n\t" \
  "buffer_load_dwordx4 %[ov3], %[ro], %[st] offen offset:3072 lds\n\t" \
  "v_mfma_f32_16x16x32_bf16 v[124:127], a[92:95], v[152:155], 0\n\t" \
  "ds_read_b64_tr_b16 a[52:53], %[va5] offset:40960\n\t" \
  "s_add_u32 m0, %[mw], 16384\n\t" \
  "v_mfma_f32_16x16x32_bf16 v[68:71], a[96:99], v[156:159], v[68:71]\n\t" \
  "ds_read_b64_tr_b16 a[54:55], %[va5] offset:41984\n\t" \
  "s_add_u32 %[st], %[so0], 256\n\t" \
  "v_mfma_f32_16x16x32_bf16 v[76:79], a[100:103], v[156:159], v[76:79]\n\t" \
  "ds_read_b64_tr_b16 a[56:57], %[va6] offset:40960\n\t" \
  "ds_read_b64_tr_b16 a[58:59], %[va6] offset:41984\n\t" \
  "v_mfma_f32_16x16x32_bf16 v[84:87], a[104:107], v[156:159], v[84:87]\n\t" \
  "ds_read_b64_tr_b16 a[60:61], %[va7] offset:40960\n\t" \
  "buffer_load_dwordx4 %[ov0], %[ro], %[st] offen offset:0 lds\n\t" \
  "v_mfma_f32_16x16x32_bf16 v[92:95], a[108:111], v[156:159], v[92:95]\n\t" \
  "ds_read_b64_tr_b16 a[62:63], %[va7] offset:41984\n\t" \
  "buffer_load_dwordx4 %[ov1], %[ro], %[st] offen offset:1024 lds\n\t" \
  "v_mfma_f32_16x16x32_bf16 v[100:103], a[112:115], v[156:159], v[100:103]\n\t" \
  "buffer_load_dwordx4 %[ov2], %[ro], %[st] offen offset:2048 lds\n\t" \
  "v_mfma_f32_16x16x32_bf16 v[108:111], a[116:119], v[156:159], v[108:111]\n\t" \
  "buffer_load_dwordx4 %[ov3], %[ro], %[st] offen offset:3072 lds\n\t" \
  "v_mfma_f32_16x16x32_bf16 v[116:119], a[120:123], v[156:159], v[116:119]\n\t" \
  "v_mfma_f32_16x16x32_bf16 v[124:127], a[124:127], v[156:159], v[124:127]\n\t" \
  "s_waitcnt vmcnt(12) lgkmcnt(0)\n\t" \
  "s_barrier\n\t" \
  "v_mfma_f32_16x16x32_bf16 v[64:67], a[0:3], v[144:147], v[64:67]\n\t" \
  "ds_read_b64_tr_b16 a[64:65], %[va0] offset:49152\n\t" \
  "s_add_u32 m0, %[mw], 24576\n\t" \
  "v_mfma_f32_16x16x32_bf16 v[72:75], a[4:7], v[144:147], v[72:75]\n\t" \
  "ds_read_b64_tr_b16 a[66:67], %[va0] offset:50176\n\t" \
  "ds_read_b64_tr_b16 a[68:69], %[va1] offset:49152\n\t" \
  "v_mfma_f32_16x16x32_bf16 v[80:83], a[8:11], v[144:147], v[80:83]\n\t" \
  "ds_read_b64_tr_b16 a[70:71], %[va1] offset:50176\n\t" \
  "s_add_u32 %[st], %[so0], 33024\n\t" \
  "v_mfma_f32_16x16x32_bf16 v[88:91], a[12:15], v[144:147], v[88:91]\n\t" \
  "ds_read_b64_tr_b16 a[72:73], %[va2] offset:49152\n\t" \
  "ds_read_b64_tr_b16 a[74:75], %[va2] offset:50176\n\t" \
  "v_mfma_f32_16x16x32_bf16 v[96:99], a[16:19], v[144:147], v[96:99]\n\t" \
  "ds_read_b64_tr_b16 a[76:77], %[va3] offset:49152\n\t" \
  "ds_read_b64_tr_b16 a[78:79], %[va3] offset:50176\n\t" \
  "v_mfma_f32_16x16x32_bf16 v[104:107], a[20:23], v[144:147], v[104:107]\n\t" \
  "ds_read_b64_tr_b16 a[80:81], %[va4] offset:49152\n\t" \
  "buffer_load_dwordx4 %[ov0], %[ro], %[st] offen offset:0 lds\n\t" \
  "v_mfma_f32_16x16x32_bf16 v[112:115], a[24:27], v[144:147], v[112:115]\n\t" \
  "ds_read_b64_tr_b16 a[82:83], %[va4] offset:50176\n\t" \
  "ds_read_b64_tr_b16 a[84:85], %[va5] offset:49152\n\t" \
  "v_mfma_f32_16x16x32_bf16 v[120:123], a[28:31], v[144:147], v[120:123]\n\t" \
  "ds_read_b64_tr_b16 a[86:87], %[va5] offset:50176\n\t" \
  "buffer_load_dwordx4 %[ov1], %[ro], %[st] offen offset:1024 lds\n\t" \
  "v_mfma_f32_16x16x32_bf16 v[68:71], a[0:3], v[160:163], v[68:71]\n\t" \
  "ds_read_b64_tr_b16 a[88:89], %[va6] offset:49152\n\t" \
  "ds_read_b64_tr_b16 a[90:91], %[va6] offset:50176\n\t" \
  "v_mfma_f32_16x16x32_bf16 v[76:79], a[4:7], v[160:163], v[76:79]\n\t" \
  "ds_read_b64_tr_b16 a[92:93], %[va7] offset:49152\n\t" \
  "ds_read_b64_tr_b16 a[94:95], %[va7] offset:50176\n\t" \
  "v_mfma_f32_16x16x32_bf16 v[84:87], a[8:11], v[160:163], v[84:87]\n\t" \
  "ds_read_b64_tr_b16 a[96:97], %[va0] offset:57344\n\t" \
  "buffer_load_dwordx4 %[ov2], %[ro], %[st] offen offset:2048 lds\n\t" \
  "v_mfma_f32_16x16x32_bf16 v[92:95], a[12:15], v[160:163], v[92:95]\n\t" \
  "ds_read_b64_tr_b16 a[98:99], %[va0] offset:58368\n\t" \
  "ds_read_b64_tr_b16 a[100:101], %[va1] offset:57344\n\t" \
  "v_mfma_f32_16x16x32_bf16 v[100:103], a[16:19], v[160:163], v[100:103]\n\t" \
  "ds_read_b64_tr_b16 a[102:103], %[va1] offset:58368\n\t" \
  "buffer_load_dwordx4 %[ov3], %[ro], %[st] offen offset:3072 lds\n\t" \
  "v_mfma_f32_16x16x32_bf16 v[108:111], a[20:23], v[160:163], v[108:111]\n\t" \
  "ds_read_b64_tr_b16 a[104:105], %[va2] offset:57344\n\t" \
  "ds_read_b64_tr_b16 a[106:107], %[va2] offset:58368\n\t" \
  "v_mfma_f32_16x16x32_bf16 v[116:119], a[24:27], v[160:163], v[116:119]\n\t" \
  "ds_read_b64_tr_b16 a[108:109], %[va3] offset:57344\n\t" \
  "ds_read_b64_tr_b16 a[110:111], %[va3] offset:58368\n\t" \
  "v_mfma_f32_16x16x32_bf16 v[124:127], a[28:31], v[160:163], v[124:127]\n\t" \
  "ds_read_b64_tr_b16 a[112:113], %[va4] offset:57344\n\t" \
  "s_add_u32 m0, %[mw], 32768\n\t" \
  "v_mfma_f32_16x16x32_bf16 v[64:67], a[32:35], v[148:151], v[64:67]\n\t" \
  "ds_read_b64_tr_b16 a[114:115], %[va4] offset:58368\n\t" \
  "ds_read_b64_tr_b16 a[116:117], %[va5] offset:57344\n\t" \
  "v_mfma_f32_16x16x32_bf16 v[72:75], a[36:39], v[148:151], v[72:75]\n\t" \
  "ds_read_b64_tr_b16 a[118:119], %[va5] offset:58368\n\t" \
  "ds_read_b64_tr_b16 a[120:121], %[va6] offset:57344\n\t" \
  "v_mfma_f32_16x16x32_bf16 v[80:83], a[40:43], v[148:151], v[80:83]\n\t" \
  "ds_read_b64_tr_b16 a[122:123], %[va6] offset:58368\n\t" \
  "s_add_u32 %[st], %[so0], 65792\n\t" \
  "v_mfma_f32_16x16x32_bf16 v[88:91], a[44:47], v[148:151], v[88:91]\n\t" \
  "ds_read_b64_tr_b16 a[124:125], %[va7] offset:57344\n\t" \
  "v_mfma_f32_16x16x32_bf16 v[96:99], a[48:51], v[148:151], v[96:99]\n\t" \
  "ds_read_b64_tr_b16 a[126:127], %[va7] offset:58368\n\t" \
  "buffer_load_dwordx4 %[ov0], %[ro], %[st] offen offset:0 lds\n\t" \
  "v_mfma_f32_16x16x32_bf16 v[104:107], a[52:55], v[148:151], v[104:107]\n\t" \
  "ds_read_b128 a[160:163], %[ob] offset:0\n\t" \
  "ds_read_b128 a[164:167], %[ob] offset:1024\n\t" \
  "v_mfma_f32_16x16x32_bf16 v[112:115], a[56:59], v[148:151], v[112:115]\n\t" \
  "ds_read_b128 a[168:171], %[ob] offset:2048\n\t" \
  "v_mfma_f32_16x16x32_bf16 v[120:123], a[60:63], v[148:151], v[120:123]\n\t" \
  "ds_read_b128 a[172:175], %[ob] offset:3072\n\t" \
  "buffer_load_dwordx4 %[ov1], %[ro], %[st] offen offset:1024 lds\n\t" \
  "v_mfma_f32_16x16x32_bf16 v[68:71], a[32:35], v[164:167], v[68:71]\n\t" \
  "ds_read_b128 a[176:179], %[ob] offset:4096\n\t" \
  "ds_read_b128 a[180:183], %[ob] offset:5120\n\t" \
  "v_mfma_f32_16x16x32_bf16 v[76:79], a[36:39], v[164:167], v[76:79]\n\t" \
  "ds_read_b128 a[184:187], %[ob] offset:6144\n\t" \
  "buffer_load_dwordx4 %[ov2], %[ro], %[st] offen offset:2048 lds\n\t" \
  "v_mfma_f32_16x16x32_bf16 v[84:87], a[40:43], v[164:167], v[84:87]\n\t" \
  "ds_read_b128 a[188:191], %[ob] offset:7168\n\t" \
  "v_mfma_f32_16x16x32_bf16 v[92:95], a[44:47], v[164:167], v[92:95]\n\t" \
  "v_mfma_f32_16x16x32_bf16 v[100:103], a[48:51], v[164:167], v[100:103]\n\t" \
  "buffer_load_dwordx4 %[ov3], %[ro], %[st] offen offset:3072 lds\n\t" \
  "v_mfma_f32_16x16x32_bf16 v[108:111], a[52:55], v[164:167], v[108:111]\n\t" \
  "v_mfma_f32_16x16x32_bf16 v[116:119], a[56:59], v[164:167], v[116:119]\n\t" \
  "v_mfma_f32_16x16x32_bf16 v[124:127], a[60:63], v[164:167], v[124:127]\n\t" \
  "s_waitcnt vmcnt(12) lgkmcnt(0)\n\t" \
  "s_barrier\n\t" \
  "v_mfma_f32_16x16x32_bf16 v[64:67], a[64:67], a[160:163], v[64:67]\n\t" \
  "ds_read_b64_tr_b16 a[0:1], %[va0] offset:0\n\t" \
  "s_add_u32 m0, %[mw], 40960\n\t" \
  "v_mfma_f32_16x16x32_bf16 v[72:75], a[68:71], a[160:163], v[72:75]\n\t" \
  "ds_read_b64_tr_b16 a[2:3], %[va0] offset:1024\n\t" \
  "ds_read_b64_tr_b16 a[4:5], %[va1] offset:0\n\t" \
  "v_mfma_f32_16x16x32_bf16 v[80:83], a[72:75], a[160:163], v[80:83]\n\t" \
  "ds_read_b64_tr_b16 a[6:7], %[va1] offset:1024\n\t" \
  "s_add_u32 %[st], %[so0], 98560\n\t" \
  "v_mfma_f32_16x16x32_bf16 v[88:91], a[76:79], a[160:163], v[88:91]\n\t" \
  "ds_read_b64_tr_b16 a[8:9], %[va2] offset:0\n\t" \
  "v_mfma_f32_16x16x32_bf16 v[96:99], a[80:83], a[160:163], v[96:99]\n\t" \
  "ds_read_b64_tr_b16 a[10:11], %[va2] offset:1024\n\t" \
  "ds_read_b64_tr_b16 a[12:13], %[va3] offset:0\n\t" \
  "v_mfma_f32_16x16x32_bf16 v[104:107], a[84:87], a[160:163], v[104:107]\n\t" \
  "ds_read_b64_tr_b16 a[14:15], %[va3] offset:1024\n\t" \
  "buffer_load_dwordx4 %[ov0], %[ro], %[st] offen offset:0 lds\n\t" \
  "v_mfma_f32_16x16x32_bf16 v[112:115], a[88:91], a[160:163], v[112:115]\n\t" \
  "ds_read_b64_tr_b16 a[16:17], %[va4] offset:0\n\t" \
  "v_mfma_f32_16x16x32_bf16 v[120:123], a[92:95], a[160:163], v[120:123]\n\t" \
  "ds_read_b64_tr_b16 a[18:19], %[va4] offset:1024\n\t" \
  "buffer_load_dwordx4 %[ov1], %[ro], %[st] offen offset:1024 lds\n\t" \
  "v_mfma_f32_16x16x32_bf16 v[68:71], a[64:67], a[176:179], v[68:71]\n\t" \
  "ds_read_b64_tr_b16 a[20:21], %[va5] offset:0\n\t" \
  "ds_read_b64_tr_b16 a[22:23], %[va5] offset:1024\n\t" \
  "v_mfma_f32_16x16x32_bf16 v[76:79], a[68:71], a[176:179], v[76:79]\n\t" \
  "ds_read_b64_tr_b16 a[24:25], %[va6] offset:0\n\t" \
  "v_mfma_f32_16x16x32_bf16 v[84:87], a[72:75], a[176:179], v[84:87]\n\t" \
  "ds_read_b64_tr_b16 a[26:27], %[va6] offset:1024\n\t" \
  "buffer_load_dwordx4 %[ov2], %[ro], %[st] offen offset:2048 lds\n\t" \
  "v_mfma_f32_16x16x32_bf16 v[92:95], a[76:79], a[176:179], v[92:95]\n\t" \
  "ds_read_b64_tr_b16 a[28:29], %[va7] offset:0\n\t" \
  "ds_read_b64_tr_b16 a[30:31], %[va7] offset:1024\n\t" \
  "v_mfma_f32_16x16x32_bf16 v[100:103], a[80:83], a[176:179], v[100:103]\n\t" \
  "ds_read_b64_tr_b16 a[32:33], %[va0] offset:8192\n\t" \
  "buffer_load_dwordx4 %[ov3], %[ro], %[st] offen offset:3072 lds\n\t" \
  "v_mfma_f32_16x16x32_bf16 v[108:111], a[84:87], a[176:179], v[108:111]\n\t" \
  "ds_read_b64_tr_b16 a[34:35], %[va0] offset:9216\n\t" \
  "v_mfma_f32_16x16x32_bf16 v[116:119], a[88:91], a[176:179], v[116:119]\n\t" \
  "ds_read_b64_tr_b16 a[36:37], %[va1] offset:8192\n\t" \
  "ds_read_b64_tr_b16 a[38:39], %[va1] offset:9216\n\t" \
  "v_mfma_f32_16x16x32_bf16 v[124:127], a[92:95], a[176:179], v[124:127]\n\t" \
  "ds_read_b64_tr_b16 a[40:41], %[va2] offset:8192\n\t" \
  "s_add_u32 m0, %[mw], 49152\n\t" \
  "v_mfma_f32_16x16x32_bf16 v[64:67], a[96:99], a[164:167], v[64:67]\n\t" \
  "ds_read_b64_tr_b16 a[42:43], %[va2] offset:9216\n\t" \
  "v_mfma_f32_16x16x32_bf16 v[72:75], a[100:103], a[164:167], v[72:75]\n\t" \
  "ds_read_b64_tr_b16 a[44:45], %[va3] offset:8192\n\t" \
  "v_mfma_f32_16x16x32_bf16 v[80:83], a[104:107], a[164:167], v[80:83]\n\t" \
  "ds_read_b64_tr_b16 a[46:47], %[va3] offset:9216\n\t" \
  "s_add_u32 %[st], %[so1], 256\n\t" \
  "v_mfma_f32_16x16x32_bf16 v[88:91], a[108:111], a[164:167], v[88:91]\n\t" \
  "ds_read_b64_tr_b16 a[48:49], %[va4] offset:8192\n\t" \
  "v_mfma_f32_16x16x32_bf16 v[96:99], a[112:115], a[164:167], v[96:99]\n\t" \
  "ds_read_b64_tr_b16 a[50:51], %[va4] offset:9216\n\t" \
  "buffer_load_dwordx4 %[ov0], %[ro], %[st] offen offset:0 lds\n\t" \
  "v_mfma_f32_16x16x32_bf16 v[104:107], a[116:119], a[164:167], v[104:107]\n\t" \
  "ds_read_b64_tr_b16 a[52:53], %[va5] offset:8192\n\t" \
  "v_mfma_f32_16x16x32_bf16 v[112:115], a[120:123], a[164:167], v[112:115]\n\t" \
  "ds_read_b64_tr_b16 a[54:55], %[va5] offset:9216\n\t" \
  "v_mfma_f32_16x16x32_bf16 v[120:123], a[124:127], a[164:167], v[120:123]\n\t" \
  "ds_read_b64_tr_b16 a[56:57], %[va6] offset:8192\n\t" \
  "buffer_load_dwordx4 %[ov1], %[ro], %[st] offen offset:1024 lds\n\t" \
  "v_mfma_f32_16x16x32_bf16 v[68:71], a[96:99], a[180:183], v[68:71]\n\t" \
  "ds_read_b64_tr_b16 a[58:59], %[va6] offset:9216\n\t" \
  "v_mfma_f32_16x16x32_bf16 v[76:79], a[100:103], a[180:183], v[76:79]\n\t" \
  "ds_read_b64_tr_b16 a[60:61], %[va7] offset:8192\n\t" \
  "buffer_load_dwordx4 %[ov2], %[ro], %[st] offen offset:2048 lds\n\t" \
  "v_mfma_f32_16x16x32_bf16 v[84:87], a[104:107], a[180:183], v[84:87]\n\t" \
  "ds_read_b64_tr_b16 a[62:63], %[va7] offset:9216\n\t" \
  "v_mfma_f32_16x16x32_bf16 v[92:95], a[108:111], a[180:183], v[92:95]\n\t" \
  "v_mfma_f32_16x16x32_bf16 v[100:103], a[112:115], a[180:183], v[100:103]\n\t" \
  "buffer_load_dwordx4 %[ov3], %[ro], %[st] offen offset:3072 lds\n\t" \
  "v_mfma_f32_16x16x32_bf16 v[108:111], a[116:119], a[180:183], v[108:111]\n\t" \
  "v_mfma_f32_16x16x32_bf16 v[116:119], a[120:123], a[180:183], v[116:119]\n\t" \
  "v_mfma_f32_16x16x32_bf16 v[124:127], a[124:127], a[180:183], v[124:127]\n\t" \
  "s_waitcnt vmcnt(12) lgkmcnt(0)\n\t" \
  "s_barrier\n\t" \
  "v_mfma_f32_16x16x32_bf16 v[64:67], a[0:3], a[168:171], v[64:67]\n\t" \
  "ds_read_b64_tr_b16 a[64:65], %[va0] offset:16384\n\t" \
  "s_add_u32 m0, %[mw], 57344\n\t" \
  "v_mfma_f32_16x16x32_bf16 v[72:75], a[4:7], a[168:171], v[72:75]\n\t" \
  "ds_read_b64_tr_b16 a[66:67], %[va0] offset:17408\n\t" \
  "ds_read_b64_tr_b16 a[68:69], %[va1] offset:16384\n\t" \
  "v_mfma_f32_16x16x32_bf16 v[80:83], a[8:11], a[168:171], v[80:83]\n\t" \
  "ds_read_b64_tr_b16 a[70:71], %[va1] offset:17408\n\t" \
  "s_add_u32 %[st], %[so1], 33024\n\t" \
  "v_mfma_f32_16x16x32_bf16 v[88:91], a[12:15], a[168:171], v[88:91]\n\t" \
  "ds_read_b64_tr_b16 a[72:73], %[va2] offset:16384\n\t" \
  "v_mfma_f32_16x16x32_bf16 v[96:99], a[16:19], a[168:171], v[96:99]\n\t" \
  "ds_read_b64_tr_b16 a[74:75], %[va2] offset:17408\n\t" \
  "ds_read_b64_tr_b16 a[76:77], %[va3] offset:16384\n\t" \
  "v_mfma_f32_16x16x32_bf16 v[104:107], a[20:23], a[168:171], v[104:107]\n\t" \
  "ds_read_b64_tr_b16 a[78:79], %[va3] offset:17408\n\t" \
  "buffer_load_dwordx4 %[ov0], %[ro], %[st] offen offset:0 lds\n\t" \
  "v_mfma_f32_16x16x32_bf16 v[112:115], a[24:27], a[168:171], v[112:115]\n\t" \
  "ds_read_b64_tr_b16 a[80:81], %[va4] offset:16384\n\t" \
  "v_mfma_f32_16x16x32_bf16 v[120:123], a[28:31], a[168:171], v[120:123]\n\t" \
  "ds_read_b64_tr_b16 a[82:83], %[va4] offset:17408\n\t" \
  "buffer_load_dwordx4 %[ov1], %[ro], %[st] offen offset:1024 lds\n\t" \
  "v_mfma_f32_16x16x32_bf16 v[68:71], a[0:3], a[184:187], v[68:71]\n\t" \
  "ds_read_b64_tr_b16 a[84:85], %[va5] offset:16384\n\t" \
  "ds_read_b64_tr_b16 a[86:87], %[va5] offset:17408\n\t" \
  "v_mfma_f32_16x16x32_bf16 v[76:79], a[4:7], a[184:187], v[76:79]\n\t" \
  "ds_read_b64_tr_b16 a[88:89], %[va6] offset:16384\n\t" \
  "v_mfma_f32_16x16x32_bf16 v[84:87], a[8:11], a[184:187], v[84:87]\n\t" \
  "ds_read_b64_tr_b16 a[90:91], %[va6] offset:17408\n\t" \
  "buffer_load_dwordx4 %[ov2], %[ro], %[st] offen offset:2048 lds\n\t" \
  "v_mfma_f32_16x16x32_bf16 v[92:95], a[12:15], a[184:187], v[92:95]\n\t" \
  "ds_read_b64_tr_b16 a[92:93], %[va7] offset:16384\n\t" \
  "ds_read_b64_tr_b16 a[94:95], %[va7] offset:17408\n\t" \
  "v_mfma_f32_16x16x32_bf16 v[100:103], a[16:19], a[184:187], v[100:103]\n\t" \
  "ds_read_b64_tr_b16 a[96:97], %[va0] offset:24576\n\t" \
  "buffer_load_dwordx4 %[ov3], %[ro], %[st] offen offset:3072 lds\n\t" \
  "v_mfma_f32_16x16x32_bf16 v[108:111], a[20:23], a[184:187], v[108:111]\n\t" \
  "ds_read_b64_tr_b16 a[98:99], %[va0] offset:25600\n\t" \
  "v_mfma_f32_16x16x32_bf16 v[116:119], a[24:27], a[184:187], v[116:119]\n\t" \
  "ds_read_b64_tr_b16 a[100:101], %[va1] offset:24576\n\t" \
  "ds_read_b64_tr_b16 a[102:103], %[va1] offset:25600\n\t" \
  "v_mfma_f32_16x16x32_bf16 v[124:127], a[28:31], a[184:187], v[124:127]\n\t" \
  "ds_read_b64_tr_b16 a[104:105], %[va2] offset:24576\n\t" \
  "s_add_u32 m0, %[mw], 0\n\t" \
  "v_mfma_f32_16x16x32_bf16 v[64:67], a[32:35], a[172:175], v[64:67]\n\t" \
  "ds_read_b64_tr_b16 a[106:107], %[va2] offset:25600\n\t" \
  "v_mfma_f32_16x16x32_bf16 v[72:75], a[36:39], a[172:175], v[72:75]\n\t" \
  "ds_read_b64_tr_b16 a[108:109], %[va3] offset:24576\n\t" \
  "v_mfma_f32_16x16x32_bf16 v[80:83], a[40:43], a[172:175], v[80:83]\n\t" \
  "ds_read_b64_tr_b16 a[110:111], %[va3] offset:25600\n\t" \
  "s_add_u32 %[st], %[so1], 65792\n\t" \
  "v_mfma_f32_16x16x32_bf16 v[88:91], a[44:47], a[172:175], v[88:91]\n\t" \
  "ds_read_b64_tr_b16 a[112:113], %[va4] offset:24576\n\t" \
  "v_mfma_f32_16x16x32_bf16 v[96:99], a[48:51], a[172:175], v[96:99]\n\t" \
  "ds_read_b64_tr_b16 a[114:115], %[va4] offset:25600\n\t" \
  "buffer_load_dwordx4 %[ov0], %[ro], %[st] offen offset:0 lds\n\t" \
  "v_mfma_f32_16x16x32_bf16 v[104:107], a[52:55], a[172:175], v[104:107]\n\t" \
  "ds_read_b64_tr_b16 a[116:117], %[va5] offset:24576\n\t" \
  "v_mfma_f32_16x16x32_bf16 v[112:115], a[56:59], a[172:175], v[112:115]\n\t" \
  "ds_read_b64_tr_b16 a[118:119], %[va5] offset:25600\n\t" \
  "v_mfma_f32_16x16x32_bf16 v[120:123], a[60:63], a[172:175], v[120:123]\n\t" \
  "ds_read_b64_tr_b16 a[120:121], %[va6] offset:24576\n\t" \
  "buffer_load_dwordx4 %[ov1], %[ro], %[st] offen offset:1024 lds\n\t" \
  "v_mfma_f32_16x16x32_bf16 v[68:71], a[32:35], a[188:191], v[68:71]\n\t" \
  "ds_read_b64_tr_b16 a[122:123], %[va6] offset:25600\n\t" \
  "v_mfma_f32_16x16x32_bf16 v[76:79], a[36:39], a[188:191], v[76:79]\n\t" \
  "ds_read_b64_tr_b16 a[124:125], %[va7] offset:24576\n\t" \
  "buffer_load_dwordx4 %[ov2], %[ro], %[st] offen offset:2048 lds\n\t" \
  "v_mfma_f32_16x16x32_bf16 v[84:87], a[40:43], a[188:191], v[84:87]\n\t" \
  "ds_read_b64_tr_b16 a[126:127], %[va7] offset:25600\n\t" \
  "v_mfma_f32_16x16x32_bf16 v[92:95], a[44:47], a[188:191], v[92:95]\n\t" \
  "v_mfma_f32_16x16x32_bf16 v[100:103], a[48:51], a[188:191], v[100:103]\n\t" \
  "buffer_load_dwordx4 %[ov3], %[ro], %[st] offen offset:3072 lds\n\t" \
  "v_mfma_f32_16x16x32_bf16 v[108:111], a[52:55], a[188:191], v[108:111]\n\t" \
  "v_mfma_f32_16x16x32_bf16 v[116:119], a[56:59], a[188:191], v[116:119]\n\t" \
  "v_mfma_f32_16x16x32_bf16 v[124:127], a[60:63], a[188:191], v[124:127]\n\t" \
  "s_waitcnt vmcnt(12) lgkmcnt(0)\n\t" \
  "s_barrier\n\t" \
  "v_mfma_f32_16x16x32_bf16 v[192:195], a[64:67], v[136:139], 0\n\t" \
  "ds_read_b64_tr_b16 a[0:1], %[va0] offset:32768\n\t" \
  "ds_read_b64_tr_b16 a[2:3], %[va0] offset:33792\n\t" \
  "s_add_u32 m0, %[mw], 8192\n\t" \
  "v_mfma_f32_16x16x32_bf16 v[200:203], a[68:71], v[136:139], 0\n\t" \
  "ds_read_b64_tr_b16 a[4:5], %[va1] offset:32768\n\t" \
  "ds_read_b64_tr_b16 a[6:7], %[va1] offset:33792\n\t" \
  "s_add_u32 %[st], %[so1], 98560\n\t" \
  "v_mfma_f32_16x16x32_bf16 v[208:211], a[72:75], v[136:139], 0\n\t" \
  "ds_read_b64_tr_b16 a[8:9], %[va2] offset:32768\n\t" \
  "buffer_load_dwordx4 %[ov0], %[ro], %[st] offen offset:0 lds\n\t" \
  "v_mfma_f32_16x16x32_bf16 v[216:219], a[76:79], v[136:139], 0\n\t" \
  "ds_read_b64_tr_b16 a[10:11], %[va2] offset:33792\n\t" \
  "buffer_load_dwordx4 %[ov1], %[ro], %[st] offen offset:1024 lds\n\t" \
  "v_mfma_f32_16x16x32_bf16 v[224:227], a[80:83], v[136:139], 0\n\t" \
  "ds_read_b64_tr_b16 a[12:13], %[va3] offset:32768\n\t" \
  "buffer_load_dwordx4 %[ov2], %[ro], %[st] offen offset:2048 lds\n\t" \
  "v_mfma_f32_16x16x32_bf16 v[232:235], a[84:87], v[136:139], 0\n\t" \
  "ds_read_b64_tr_b16 a[14:15], %[va3] offset:33792\n\t" \
  "buffer_load_dwordx4 %[ov3], %[ro], %[st] offen offset:3072 lds\n\t" \
  "v_mfma_f32_16x16x32_bf16 v[240:243], a[88:91], v[136:139], 0\n\t" \
  "ds_read_b64_tr_b16 a[16:17], %[va4] offset:32768\n\t" \
  "s_add_u32 %[st], %[sp], 0\n\t" \
  "v_mfma_f32_16x16x32_bf16 v[248:251], a[92:95], v[136:139], 0\n\t" \
  "ds_read_b64_tr_b16 a[18:19], %[va4] offset:33792\n\t" \
  "ds_read_b64_tr_b16 a[20:21], %[va5] offset:32768\n\t" \
  "v_mfma_f32_16x16x32_bf16 v[196:199], a[64:67], v[152:155], 0\n\t" \
  "ds_read_b64_tr_b16 a[22:23], %[va5] offset:33792\n\t" \
  "v_cvt_pk_bf16_f32 v168, v64, v65\n\t" \
  "v_mfma_f32_16x16x32_bf16 v[204:207], a[68:71], v[152:155], 0\n\t" \
  "ds_read_b64_tr_b16 a[24:25], %[va6] offset:32768\n\t" \
  "v_cvt_pk_bf16_f32 v169, v66, v67\n\t" \
  "v_mfma_f32_16x16x32_bf16 v[212:215], a[72:75], v[152:155], 0\n\t" \
  "ds_read_b64_tr_b16 a[26:27], %[va6] offset:33792\n\t" \
  "v_cvt_pk_bf16_f32 v170, v72, v73\n\t" \
  "v_mfma_f32_16x16x32_bf16 v[220:223], a[76:79], v[152:155], 0\n\t" \
  "ds_read_b64_tr_b16 a[28:29], %[va7] offset:32768\n\t" \
  "v_cvt_pk_bf16_f32 v171, v74, v75\n\t" \
  "v_mfma_f32_16x16x32_bf16 v[228:231], a[80:83], v[152:155], 0\n\t" \
  "ds_read_b64_tr_b16 a[30:31], %[va7] offset:33792\n\t" \
  "buffer_store_dwordx4 v[168:171], %[zv], %[rp], %[st] offen offset:0 sc1\n\t" \
  "v_mfma_f32_16x16x32_bf16 v[236:239], a[84:87], v[152:155], 0\n\t" \
  "ds_read_b64_tr_b16 a[32:33], %[va0] offset:40960\n\t" \
  "v_cvt_pk_bf16_f32 v172, v80, v81\n\t" \
  "v_mfma_f32_16x16x32_bf16 v[244:247], a[88:91], v[152:155], 0\n\t" \
  "ds_read_b64_tr_b16 a[34:35], %[va0] offset:41984\n\t" \
  "v_cvt_pk_bf16_f32 v173, v82, v83\n\t" \
  "v_mfma_f32_16x16x32_bf16 v[252:255], a[92:95], v[152:155], 0\n\t" \
  "ds_read_b64_tr_b16 a[36:37], %[va1] offset:40960\n\t" \
  "ds_read_b64_tr_b16 a[38:39], %[va1] offset:41984\n\t" \
  "v_mfma_f32_16x16x32_bf16 v[192:195], a[96:99], v[140:143], v[192:195]\n\t" \
  "ds_read_b64_tr_b16 a[40:41], %[va2] offset:40960\n\t" \
  "v_cvt_pk_bf16_f32 v174, v88, v89\n\t" \
  "v_mfma_f32_16x16x32_bf16 v[200:203], a[100:103], v[140:143], v[200:203]\n\t" \
  "ds_read_b64_tr_b16 a[42:43], %[va2] offset:41984\n\t" \
  "v_cvt_pk_bf16_f32 v175, v90, v91\n\t" \
  "v_mfma_f32_16x16x32_bf16 v[208:211], a[104:107], v[140:143], v[208:211]\n\t" \
  "ds_read_b64_tr_b16 a[44:45], %[va3] offset:40960\n\t" \
  "buffer_store_dwordx4 v[172:175], %[zv], %[rp], %[st] offen offset:1024 sc1\n\t" \
  "v_mfma_f32_16x16x32_bf16 v[216:219], a[108:111], v[140:143], v[216:219]\n\t" \
  "ds_read_b64_tr_b16 a[46:47], %[va3] offset:41984\n\t" \
  "v_cvt_pk_bf16_f32 v176, v96, v97\n\t" \
  "v_mfma_f32_16x16x32_bf16 v[224:227], a[112:115], v[140:143], v[224:227]\n\t" \
  "ds_read_b64_tr_b16 a[48:49], %[va4] offset:40960\n\t" \
  "v_cvt_pk_bf16_f32 v177, v98, v99\n\t" \
  "v_mfma_f32_16x16x32_bf16 v[232:235], a[116:119], v[140:143], v[232:235]\n\t" \
  "ds_read_b64_tr_b16 a[50:51], %[va4] offset:41984\n\t" \
  "v_cvt_pk_bf16_f32 v178, v104, v105\n\t" \
  "v_mfma_f32_16x16x32_bf16 v[240:243], a[120:123], v[140:143], v[240:243]\n\t" \
  "ds_read_b64_tr_b16 a[52:53], %[va5] offset:40960\n\t" \
  "v_cvt_pk_bf16_f32 v179, v106, v107\n\t" \
  "v_mfma_f32_16x16x32_bf16 v[248:251], a[124:127], v[140:143], v[248:251]\n\t" \
  "ds_read_b64_tr_b16 a[54:55], %[va5] offset:41984\n\t" \
  "ds_read_b64_tr_b16 a[56:57], %[va6] offset:40960\n\t" \
  "v_mfma_f32_16x16x32_bf16 v[196:199], a[96:99], v[156:159], v[196:199]\n\t" \
  "ds_read_b64_tr_b16 a[58:59], %[va6] offset:41984\n\t" \
  "buffer_store_dwordx4 v[176:179], %[zv], %[rp], %[st] offen offset:2048 sc1\n\t" \
  "v_mfma_f32_16x16x32_bf16 v[204:207], a[100:103], v[156:159], v[204:207]\n\t" \
  "ds_read_b64_tr_b16 a[60:61], %[va7] offset:40960\n\t" \
  "v_cvt_pk_bf16_f32 v180, v112, v113\n\t" \
  "v_mfma_f32_16x16x32_bf16 v[212:215], a[104:107], v[156:159], v[212:215]\n\t" \
  "ds_read_b64_tr_b16 a[62:63], %[va7] offset:41984\n\t" \
  "v_cvt_pk_bf16_f32 v181, v114, v115\n\t" \
  "v_mfma_f32_16x16x32_bf16 v[220:223], a[108:111], v[156:159], v[220:223]\n\t" \
  "v_cvt_pk_bf16_f32 v182, v120, v121\n\t" \
  "v_mfma_f32_16x16x32_bf16 v[228:231], a[112:115], v[156:159], v[228:231]\n\t" \
  "v_cvt_pk_bf16_f32 v183, v122, v123\n\t" \
  "v_mfma_f32_16x16x32_bf16 v[236:239], a[116:119], v[156:159], v[236:239]\n\t" \
  "buffer_store_dwordx4 v[180:183], %[zv], %[rp], %[st] offen offset:3072 sc1\n\t" \
  "v_mfma_f32_16x16x32_bf16 v[244:247], a[120:123], v[156:159], v[244:247]\n\t" \
  "v_mfma_f32_16x16x32_bf16 v[252:255], a[124:127], v[156:159], v[252:255]\n\t" \
  "s_waitcnt vmcnt(12) lgkmcnt(0)\n\t" \
  "s_barrier\n\t" \
  "v_mfma_f32_16x16x32_bf16 v[192:195], a[0:3], v[144:147], v[192:195]\n\t" \
  "ds_read_b64_tr_b16 a[64:65], %[va0] offset:49152\n\t" \
  "s_add_u32 %[st], %[sp], 4096\n\t" \
  "v_mfma_f32_16x16x32_bf16 v[200:203], a[4:7], v[144:147], v[200:203]\n\t" \
  "ds_read_b64_tr_b16 a[66:67], %[va0] offset:50176\n\t" \
  "v_cvt_pk_bf16_f32 v184, v68, v69\n\t" \
  "v_mfma_f32_16x16x32_bf16 v[208:211], a[8:11], v[144:147], v[208:211]\n\t" \
  "ds_read_b64_tr_b16 a[68:69], %[va1] offset:49152\n\t" \
  "v_cvt_pk_bf16_f32 v185, v70, v71\n\t" \
  "v_mfma_f32_16x16x32_bf16 v[216:219], a[12:15], v[144:147], v[216:219]\n\t" \
  "ds_read_b64_tr_b16 a[70:71], %[va1] offset:50176\n\t" \
  "ds_read_b64_tr_b16 a[72:73], %[va2] offset:49152\n\t" \
  "v_mfma_f32_16x16x32_bf16 v[224:227], a[16:19], v[144:147], v[224:227]\n\t" \
  "ds_read_b64_tr_b16 a[74:75], %[va2] offset:50176\n\t" \
  "v_cvt_pk_bf16_f32 v186, v76, v77\n\t" \
  "v_mfma_f32_16x16x32_bf16 v[232:235], a[20:23], v[144:147], v[232:235]\n\t" \
  "ds_read_b64_tr_b16 a[76:77], %[va3] offset:49152\n\t" \
  "v_cvt_pk_bf16_f32 v187, v78, v79\n\t" \
  "v_mfma_f32_16x16x32_bf16 v[240:243], a[24:27], v[144:147], v[240:243]\n\t" \
  "ds_read_b64_tr_b16 a[78:79], %[va3] offset:50176\n\t" \
  "ds_read_b64_tr_b16 a[80:81], %[va4] offset:49152\n\t" \
  "v_mfma_f32_16x16x32_bf16 v[248:251], a[28:31], v[144:147], v[248:251]\n\t" \
  "ds_read_b64_tr_b16 a[82:83], %[va4] offset:50176\n\t" \
  "buffer_store_dwordx4 v[184:187], %[zv], %[rp], %[st] offen offset:0 sc1\n\t" \
  "v_mfma_f32_16x16x32_bf16 v[196:199], a[0:3], v[160:163], v[196:199]\n\t" \
  "ds_read_b64_tr_b16 a[84:85], %[va5] offset:49152\n\t" \
  "v_cvt_pk_bf16_f32 v188, v84, v85\n\t" \
  "v_mfma_f32_16x16x32_bf16 v[204:207], a[4:7], v[160:163], v[204:207]\n\t" \
  "ds_read_b64_tr_b16 a[86:87], %[va5] offset:50176\n\t" \
  "ds_read_b64_tr_b16 a[88:89], %[va6] offset:49152\n\t" \
  "v_mfma_f32_16x16x32_bf16 v[212:215], a[8:11], v[160:163], v[212:215]\n\t" \
  "ds_read_b64_tr_b16 a[90:91], %[va6] offset:50176\n\t" \
  "v_cvt_pk_bf16_f32 v189, v86, v87\n\t" \
  "v_mfma_f32_16x16x32_bf16 v[220:223], a[12:15], v[160:163], v[220:223]\n\t" \
  "ds_read_b64_tr_b16 a[92:93], %[va7] offset:49152\n\t" \
  "v_cvt_pk_bf16_f32 v190, v92, v93\n\t" \
  "v_mfma_f32_16x16x32_bf16 v[228:231], a[16:19], v[160:163], v[228:231]\n\t" \
  "ds_read_b64_tr_b16 a[94:95], %[va7] offset:50176\n\t" \
  "v_mfma_f32_16x16x32_bf16 v[236:239], a[20:23], v[160:163], v[236:239]\n\t" \
  "ds_read_b64_tr_b16 a[96:97], %[va0] offset:57344\n\t" \
  "v_cvt_pk_bf16_f32 v191, v94, v95\n\t" \
  "v_mfma_f32_16x16x32_bf16 v[244:247], a[24:27], v[160:163], v[244:247]\n\t" \
  "ds_read_b64_tr_b16 a[98:99], %[va0] offset:58368\n\t" \
  "buffer_store_dwordx4 v[188:191], %[zv], %[rp], %[st] offen offset:1024 sc1\n\t" \
  "v_mfma_f32_16x16x32_bf16 v[252:255], a[28:31], v[160:163], v[252:255]\n\t" \
  "ds_read_b64_tr_b16 a[100:101], %[va1] offset:57344\n\t" \
  "ds_read_b64_tr_b16 a[102:103], %[va1] offset:58368\n\t" \
  "v_mfma_f32_16x16x32_bf16 v[192:195], a[32:35], v[148:151], v[192:195]\n\t" \
  "ds_read_b64_tr_b16 a[104:105], %[va2] offset:57344\n\t" \
  "v_cvt_pk_bf16_f32 v128, v100, v101\n\t" \
  "v_mfma_f32_16x16x32_bf16 v[200:203], a[36:39], v[148:151], v[200:203]\n\t" \
  "ds_read_b64_tr_b16 a[106:107], %[va2] offset:58368\n\t" \
  "v_cvt_pk_bf16_f32 v129, v102, v103\n\t" \
  "v_mfma_f32_16x16x32_bf16 v[208:211], a[40:43], v[148:151], v[208:211]\n\t" \
  "ds_read_b64_tr_b16 a[108:109], %[va3] offset:57344\n\t" \
  "v_mfma_f32_16x16x32_bf16 v[216:219], a[44:47], v[148:151], v[216:219]\n\t" \
  "ds_read_b64_tr_b16 a[110:111], %[va3] offset:58368\n\t" \
  "v_cvt_pk_bf16_f32 v130, v108, v109\n\t" \
  "v_mfma_f32_16x16x32_bf16 v[224:227], a[48:51], v[148:151], v[224:227]\n\t" \
  "ds_read_b64_tr_b16 a[112:113], %[va4] offset:57344\n\t" \
  "v_cvt_pk_bf16_f32 v131, v110, v111\n\t" \
  "v_mfma_f32_16x16x32_bf16 v[232:235], a[52:55], v[148:151], v[232:235]\n\t" \
  "ds_read_b64_tr_b16 a[114:115], %[va4] offset:58368\n\t" \
  "ds_read_b64_tr_b16 a[116:117], %[va5] offset:57344\n\t" \
  "v_mfma_f32_16x16x32_bf16 v[240:243], a[56:59], v[148:151], v[240:243]\n\t" \
  "ds_read_b64_tr_b16 a[118:119], %[va5] offset:58368\n\t" \
  "buffer_store_dwordx4 v[128:131], %[zv], %[rp], %[st] offen offset:2048 sc1\n\t" \
  "v_mfma_f32_16x16x32_bf16 v[248:251], a[60:63], v[148:151], v[248:251]\n\t" \
  "ds_read_b64_tr_b16 a[120:121], %[va6] offset:57344\n\t" \
  "v_cvt_pk_bf16_f32 v132, v116, v117\n\t" \
  "v_mfma_f32_16x16x32_bf16 v[196:199], a[32:35], v[164:167], v[196:199]\n\t" \
  "ds_read_b64_tr_b16 a[122:123], %[va6] offset:58368\n\t" \
  "v_mfma_f32_16x16x32_bf16 v[204:207], a[36:39], v[164:167], v[204:207]\n\t" \
  "ds_read_b64_tr_b16 a[124:125], %[va7] offset:57344\n\t" \
  "v_cvt_pk_bf16_f32 v133, v118, v119\n\t" \
  "v_mfma_f32_16x16x32_bf16 v[212:215], a[40:43], v[164:167], v[212:215]\n\t" \
  "ds_read_b64_tr_b16 a[126:127], %[va7] offset:58368\n\t" \
  "v_cvt_pk_bf16_f32 v134, v124, v125\n\t" \
  "v_mfma_f32_16x16x32_bf16 v[220:223], a[44:47], v[164:167], v[220:223]\n\t" \
  "v_mfma_f32_16x16x32_bf16 v[228:231], a[48:51], v[164:167], v[228:231]\n\t" \
  "v_cvt_pk_bf16_f32 v135, v126, v127\n\t" \
  "v_mfma_f32_16x16x32_bf16 v[236:239], a[52:55], v[164:167], v[236:239]\n\t" \
  "buffer_store_dwordx4 v[132:135], %[zv], %[rp], %[st] offen offset:3072 sc1\n\t" \
  "v_mfma_f32_16x16x32_bf16 v[244:247], a[56:59], v[164:167], v[244:247]\n\t" \
  "v_mfma_f32_16x16x32_bf16 v[252:255], a[60:63], v[164:167], v[252:255]\n\t" \
  "s_waitcnt vmcnt(8) lgkmcnt(0)\n\t" \
  "s_barrier\n\t" \
  "v_mfma_f32_16x16x32_bf16 v[192:195], a[64:67], a[160:163], v[192:195]\n\t" \
  "ds_read_b64_tr_b16 a[0:1], %[va0] offset:0\n\t" \
  "ds_read_b64_tr_b16 a[2:3], %[va0] offset:1024\n\t" \
  "v_mfma_f32_16x16x32_bf16 v[200:203], a[68:71], a[160:163], v[200:203]\n\t" \
  "ds_read_b64_tr_b16 a[4:5], %[va1] offset:0\n\t" \
  "v_mfma_f32_16x16x32_bf16 v[208:211], a[72:75], a[160:163], v[208:211]\n\t" \
  "ds_read_b64_tr_b16 a[6:7], %[va1] offset:1024\n\t" \
  "ds_read_b64_tr_b16 a[8:9], %[va2] offset:0\n\t" \
  "v_mfma_f32_16x16x32_bf16 v[216:219], a[76:79], a[160:163], v[216:219]\n\t" \
  "ds_read_b64_tr_b16 a[10:11], %[va2] offset:1024\n\t" \
  "v_mfma_f32_16x16x32_bf16 v[224:227], a[80:83], a[160:163], v[224:227]\n\t" \
  "ds_read_b64_tr_b16 a[12:13], %[va3] offset:0\n\t" \
  "ds_read_b64_tr_b16 a[14:15], %[va3] offset:1024\n\t" \
  "v_mfma_f32_16x16x32_bf16 v[232:235], a[84:87], a[160:163], v[232:235]\n\t" \
  "ds_read_b64_tr_b16 a[16:17], %[va4] offset:0\n\t" \
  "v_mfma_f32_16x16x32_bf16 v[240:243], a[88:91], a[160:163], v[240:243]\n\t" \
  "ds_read_b64_tr_b16 a[18:19], %[va4] offset:1024\n\t" \
  "ds_read_b64_tr_b16 a[20:21], %[va5] offset:0\n\t" \
  "v_mfma_f32_16x16x32_bf16 v[248:251], a[92:95], a[160:163], v[248:251]\n\t" \
  "ds_read_b64_tr_b16 a[22:23], %[va5] offset:1024\n\t" \
  "v_mfma_f32_16x16x32_bf16 v[196:199], a[64:67], a[176:179], v[196:199]\n\t" \
  "ds_read_b64_tr_b16 a[24:25], %[va6] offset:0\n\t" \
  "ds_read_b64_tr_b16 a[26:27], %[va6] offset:1024\n\t" \
  "v_mfma_f32_16x16x32_bf16 v[204:207], a[68:71], a[176:179], v[204:207]\n\t" \
  "ds_read_b64_tr_b16 a[28:29], %[va7] offset:0\n\t" \
  "v_mfma_f32_16x16x32_bf16 v[212:215], a[72:75], a[176:179], v[212:215]\n\t" \
  "ds_read_b64_tr_b16 a[30:31], %[va7] offset:1024\n\t" \
  "v_mfma_f32_16x16x32_bf16 v[220:223], a[76:79], a[176:179], v[220:223]\n\t" \
  "ds_read_b64_tr_b16 a[32:33], %[va0] offset:8192\n\t" \
  "v_mfma_f32_16x16x32_bf16 v[228:231], a[80:83], a[176:179], v[228:231]\n\t" \
  "ds_read_b64_tr_b16 a[34:35], %[va0] offset:9216\n\t" \
  "v_mfma_f32_16x16x32_bf16 v[236:239], a[84:87], a[176:179], v[236:239]\n\t" \
  "ds_read_b64_tr_b16 a[36:37], %[va1] offset:8192\n\t" \
  "v_mfma_f32_16x16x32_bf16 v[244:247], a[88:91], a[176:179], v[244:247]\n\t" \
  "ds_read_b64_tr_b16 a[38:39], %[va1] offset:9216\n\t" \
  "v_mfma_f32_16x16x32_bf16 v[252:255], a[92:95], a[176:179], v[252:255]\n\t" \
  "ds_read_b64_tr_b16 a[40:41], %[va2] offset:8192\n\t" \
  "v_mfma_f32_16x16x32_bf16 v[192:195], a[96:99], a[164:167], v[192:195]\n\t" \
  "ds_read_b64_tr_b16 a[42:43], %[va2] offset:9216\n\t" \
  "v_mfma_f32_16x16x32_bf16 v[200:203], a[100:103], a[164:167], v[200:203]\n\t" \
  "ds_read_b64_tr_b16 a[44:45], %[va3] offset:8192\n\t" \
  "v_mfma_f32_16x16x32_bf16 v[208:211], a[104:107], a[164:167], v[208:211]\n\t" \
  "ds_read_b64_tr_b16 a[46:47], %[va3] offset:9216\n\t" \
  "v_mfma_f32_16x16x32_bf16 v[216:219], a[108:111], a[164:167], v[216:219]\n\t" \
  "ds_read_b64_tr_b16 a[48:49], %[va4] offset:8192\n\t" \
  "v_mfma_f32_16x16x32_bf16 v[224:227], a[112:115], a[164:167], v[224:227]\n\t" \
  "ds_read_b64_tr_b16 a[50:51], %[va4] offset:9216\n\t" \
  "v_mfma_f32_16x16x32_bf16 v[232:235], a[116:119], a[164:167], v[232:235]\n\t" \
  "ds_read_b64_tr_b16 a[52:53], %[va5] offset:8192\n\t" \
  "v_mfma_f32_16x16x32_bf16 v[240:243], a[120:123], a[164:167], v[240:243]\n\t" \
  "ds_read_b64_tr_b16 a[54:55], %[va5] offset:9216\n\t" \
  "v_mfma_f32_16x16x32_bf16 v[248:251], a[124:127], a[164:167], v[248:251]\n\t" \
  "ds_read_b64_tr_b16 a[56:57], %[va6] offset:8192\n\t" \
  "v_mfma_f32_16x16x32_bf16 v[196:199], a[96:99], a[180:183], v[196:199]\n\t" \
  "ds_read_b64_tr_b16 a[58:59], %[va6] offset:9216\n\t" \
  "v_mfma_f32_16x16x32_bf16 v[204:207], a[100:103], a[180:183], v[204:207]\n\t" \
  "ds_read_b64_tr_b16 a[60:61], %[va7] offset:8192\n\t" \
  "v_mfma_f32_16x16x32_bf16 v[212:215], a[104:107], a[180:183], v[212:215]\n\t" \
  "ds_read_b64_tr_b16 a[62:63], %[va7] offset:9216\n\t" \
  "v_mfma_f32_16x16x32_bf16 v[220:223], a[108:111], a[180:183], v[220:223]\n\t" \
  "v_mfma_f32_16x16x32_bf16 v[228:231], a[112:115], a[180:183], v[228:231]\n\t" \
  "v_mfma_f32_16x16x32_bf16 v[236:239], a[116:119], a[180:183], v[236:239]\n\t" \
  "v_mfma_f32_16x16x32_bf16 v[244:247], a[120:123], a[180:183], v[244:247]\n\t" \
  "v_mfma_f32_16x16x32_bf16 v[252:255], a[124:127], a[180:183], v[252:255]\n\t" \
  "s_waitcnt lgkmcnt(0)\n\t" \
  "v_mfma_f32_16x16x32_bf16 v[192:195], a[0:3], a[168:171], v[192:195]\n\t" \
  "v_mfma_f32_16x16x32_bf16 v[200:203], a[4:7], a[168:171], v[200:203]\n\t" \
  "v_mfma_f32_16x16x32_bf16 v[208:211], a[8:11], a[168:171], v[208:211]\n\t" \
  "v_mfma_f32_16x16x32_bf16 v[216:219], a[12:15], a[168:171], v[216:219]\n\t" \
  "v_mfma_f32_16x16x32_bf16 v[224:227], a[16:19], a[168:171], v[224:227]\n\t" \
  "v_mfma_f32_16x16x32_bf16 v[232:235], a[20:23], a[168:171], v[232:235]\n\t" \
  "v_mfma_f32_16x16x32_bf16 v[240:243], a[24:27], a[168:171], v[240:243]\n\t" \
  "v_mfma_f32_16x16x32_bf16 v[248:251], a[28:31], a[168:171], v[248:251]\n\t" \
  "v_mfma_f32_16x16x32_bf16 v[192:195], a[32:35], a[172:175], v[192:195]\n\t" \
  "v_mfma_f32_16x16x32_bf16 v[200:203], a[36:39], a[172:175], v[200:203]\n\t" \
  "v_mfma_f32_16x16x32_bf16 v[208:211], a[40:43], a[172:175], v[208:211]\n\t" \
  "v_mfma_f32_16x16x32_bf16 v[216:219], a[44:47], a[172:175], v[216:219]\n\t" \
  "v_mfma_f32_16x16x32_bf16 v[224:227], a[48:51], a[172:175], v[224:227]\n\t" \
  "v_mfma_f32_16x16x32_bf16 v[232:235], a[52:55], a[172:175], v[232:235]\n\t" \
  "v_mfma_f32_16x16x32_bf16 v[240:243], a[56:59], a[172:175], v[240:243]\n\t" \
  "v_mfma_f32_16x16x32_bf16 v[248:251], a[60:63], a[172:175], v[248:251]\n\t" \
  "v_mfma_f32_16x16x32_bf16 v[196:199], a[0:3], a[184:187], v[196:199]\n\t" \
  "v_mfma_f32_16x16x32_bf16 v[204:207], a[4:7], a[184:187], v[204:207]\n\t" \
  "v_mfma_f32_16x16x32_bf16 v[212:215], a[8:11], a[184:187], v[212:215]\n\t" \
  "v_mfma_f32_16x16x32_bf16 v[220:223], a[12:15], a[184:187], v[220:223]\n\t" \
  "s_add_u32 %[st], %[sp], 16384\n\t" \
  "v_cvt_pk_bf16_f32 v168, v192, v193\n\t" \
  "v_mfma_f32_16x16x32_bf16 v[228:231], a[16:19], a[184:187], v[228:231]\n\t" \
  "v_cvt_pk_bf16_f32 v169, v194, v195\n\t" \
  "v_cvt_pk_bf16_f32 v170, v200, v201\n\t" \
  "v_mfma_f32_16x16x32_bf16 v[236:239], a[20:23], a[184:187], v[236:239]\n\t" \
  "v_cvt_pk_bf16_f32 v171, v202, v203\n\t" \
  "v_mfma_f32_16x16x32_bf16 v[244:247], a[24:27], a[184:187], v[244:247]\n\t" \
  "buffer_store_dwordx4 v[168:171], %[zv], %[rp], %[st] offen offset:0 sc1\n\t" \
  "v_cvt_pk_bf16_f32 v172, v208, v209\n\t" \
  "v_mfma_f32_16x16x32_bf16 v[252:255], a[28:31], a[184:187], v[252:255]\n\t" \
  "v_cvt_pk_bf16_f32 v173, v210, v211\n\t" \
  "v_cvt_pk_bf16_f32 v174, v216, v217\n\t" \
  "v_mfma_f32_16x16x32_bf16 v[196:199], a[32:35], a[188:191], v[196:199]\n\t" \
  "v_cvt_pk_bf16_f32 v175, v218, v219\n\t" \
  "v_mfma_f32_16x16x32_bf16 v[204:207], a[36:39], a[188:191], v[204:207]\n\t" \
  "buffer_store_dwordx4 v[172:175], %[zv], %[rp], %[st] offen offset:1024 sc1\n\t" \
  "v_cvt_pk_bf16_f32 v176, v224, v225\n\t" \
  "v_mfma_f32_16x16x32_bf16 v[212:215], a[40:43], a[188:191], v[212:215]\n\t" \
  "v_cvt_pk_bf16_f32 v177, v226, v227\n\t" \
  "v_cvt_pk_bf16_f32 v178, v232, v233\n\t" \
  "v_mfma_f32_16x16x32_bf16 v[220:223], a[44:47], a[188:191], v[220:223]\n\t" \
  "v_cvt_pk_bf16_f32 v179, v234, v235\n\t" \
  "v_mfma_f32_16x16x32_bf16 v[228:231], a[48:51], a[188:191], v[228:231]\n\t" \
  "buffer_store_dwordx4 v[176:179], %[zv], %[rp], %[st] offen offset:2048 sc1\n\t" \
  "v_cvt_pk_bf16_f32 v180, v240, v241\n\t" \
  "v_mfma_f32_16x16x32_bf16 v[236:239], a[52:55], a[188:191], v[236:239]\n\t" \
  "v_cvt_pk_bf16_f32 v181, v242, v243\n\t" \
  "v_cvt_pk_bf16_f32 v182, v248, v249\n\t" \
  "v_mfma_f32_16x16x32_bf16 v[244:247], a[56:59], a[188:191], v[244:247]\n\t" \
  "v_cvt_pk_bf16_f32 v183, v250, v251\n\t" \
  "v_mfma_f32_16x16x32_bf16 v[252:255], a[60:63], a[188:191], v[252:255]\n\t" \
  "buffer_store_dwordx4 v[180:183], %[zv], %[rp], %[st] offen offset:3072 sc1\n\t" \
  "s_nop 7\n\t" \
  "s_nop 3\n\t" \
  "s_add_u32 %[st], %[sp], 20480\n\t" \
  "v_cvt_pk_bf16_f32 v184, v196, v197\n\t" \
  "v_cvt_pk_bf16_f32 v185, v198, v199\n\t" \
  "v_cvt_pk_bf16_f32 v186, v204, v205\n\t" \
  "v_cvt_pk_bf16_f32 v187, v206, v207\n\t" \
  "buffer_store_dwordx4 v[184:187], %[zv], %[rp], %[st] offen offset:0 sc1\n\t" \
  "v_cvt_pk_bf16_f32 v188, v212, v213\n\t" \
  "v_cvt_pk_bf16_f32 v189, v214, v215\n\t" \
  "v_cvt_pk_bf16_f32 v190, v220, v221\n\t" \
  "v_cvt_pk_bf16_f32 v191, v222, v223\n\t" \
  "buffer_store_dwordx4 v[188:191], %[zv], %[rp], %[st] offen offset:1024 sc1\n\t" \
  "v_cvt_pk_bf16_f32 v128, v228, v229\n\t" \
  "v_cvt_pk_bf16_f32 v129, v230, v231\n\t" \
  "v_cvt_pk_bf16_f32 v130, v236, v237\n\t" \
  "v_cvt_pk_bf16_f32 v131, v238, v239\n\t" \
  "buffer_store_dwordx4 v[128:131], %[zv], %[rp], %[st] offen offset:2048 sc1\n\t" \
  "v_cvt_pk_bf16_f32 v132, v244, v245\n\t" \
  "v_cvt_pk_bf16_f32 v133, v246, v247\n\t" \
  "v_cvt_pk_bf16_f32 v134, v252, v253\n\t" \
  "v_cvt_pk_bf16_f32 v135, v254, v255\n\t" \
  "buffer_store_dwordx4 v[132:135], %[zv], %[rp], %[st] offen offset:3072 sc1\n\t" \
  "s_branch .Lend_%=\n\t" \
  ".Lrare0_%=:\n\t" \
  "s_nop 7\n\t" \
  "s_nop 7\n\t" \
  "v_max3_f32 v216, v168, v169, v170\n\t" \
  "v_max3_f32 v217, v171, v172, v173\n\t" \
  "v_max3_f32 v216, v216, v174, v175\n\t" \
  "v_max_f32 v216, v216, v217\n\t" \
  "v_mov_b32 v217, v216\n\t" \
  "s_nop 1\n\t" \
  "v_permlane16_swap_b32 v216, v217\n\t" \
  "s_nop 1\n\t" \
  "v_max_f32 v216, v216, v217\n\t" \
  "v_mov_b32 v217, v216\n\t" \
  "s_nop 1\n\t" \
  "v_permlane32_swap_b32 v216, v217\n\t" \
  "s_nop 1\n\t" \
  "v_max_f32 v216, v216, v217\n\t" \
  "v_add_f32 v218, 0x40c00000, v216\n\t" \
  "v_max_f32 v218, 0, v218\n\t" \
  "v_sub_f32 v168, v168, v218\n\t" \
  "v_sub_f32 v169, v169, v218\n\t" \
  "v_sub_f32 v170, v170, v218\n\t" \
  "v_sub_f32 v171, v171, v218\n\t" \
  "v_sub_f32 v172, v172, v218\n\t" \
  "v_sub_f32 v173, v173, v218\n\t" \
  "v_sub_f32 v174, v174, v218\n\t" \
  "v_sub_f32 v175, v175, v218\n\t" \
  "v_sub_f32 v200, v200, v218\n\t" \
  "v_sub_f32 v201, v201, v218\n\t" \
  "v_sub_f32 v202, v202, v218\n\t" \
  "v_sub_f32 v203, v203, v218\n\t" \
  "v_sub_f32 v176, v176, v218\n\t" \
  "v_sub_f32 v177, v177, v218\n\t" \
  "v_sub_f32 v178, v178, v218\n\t" \
  "v_sub_f32 v179, v179, v218\n\t" \
  "v_sub_f32 v180, v180, v218\n\t" \
  "v_sub_f32 v181, v181, v218\n\t" \
  "v_sub_f32 v182, v182, v218\n\t" \
  "v_sub_f32 v183, v183, v218\n\t" \
  "v_exp_f32 v208, v168\n\t" \
  "v_exp_f32 v209, v169\n\t" \
  "v_exp_f32 v210, v170\n\t" \
  "v_exp_f32 v211, v171\n\t" \
  "v_exp_f32 v212, v172\n\t" \
  "v_exp_f32 v213, v173\n\t" \
  "v_exp_f32 v214, v174\n\t" \
  "v_exp_f32 v215, v175\n\t" \
  "v_exp_f32 v219, -v218\n\t" \
  "s_nop 0\n\t" \
  "v_cvt_pk_bf16_f32 v192, v208, v209\n\t" \
  "v_cvt_pk_bf16_f32 v193, v210, v211\n\t" \
  "v_cvt_pk_bf16_f32 v194, v212, v213\n\t" \
  "v_cvt_pk_bf16_f32 v195, v214, v215\n\t" \
  "v_mul_f32 v128, v128, v219\n\t" \
  "v_mul_f32 v129, v129, v219\n\t" \
  "v_mul_f32 v130, v130, v219\n\t" \
  "v_mul_f32 v131, v131, v219\n\t" \
  "v_mul_f32 v64, v64, v219\n\t" \
  "v_mul_f32 v65, v65, v219\n\t" \
  "v_mul_f32 v66, v66, v219\n\t" \
  "v_mul_f32 v67, v67, v219\n\t" \
  "v_mul_f32 v72, v72, v219\n\t" \
  "v_mul_f32 v73, v73, v219\n\t" \
  "v_mul_f32 v74, v74, v219\n\t" \
  "v_mul_f32 v75, v75, v219\n\t" \
  "v_mul_f32 v80, v80, v219\n\t" \
  "v_mul_f32 v81, v81, v219\n\t" \
  "v_mul_f32 v82, v82, v219\n\t" \
  "v_mul_f32 v83, v83, v219\n\t" \
  "v_mul_f32 v88, v88, v219\n\t" \
  "v_mul_f32 v89, v89, v219\n\t" \
  "v_mul_f32 v90, v90, v219\n\t" \
  "v_mul_f32 v91, v91, v219\n\t" \
  "v_mul_f32 v96, v96, v219\n\t" \
  "v_mul_f32 v97, v97, v219\n\t" \
  "v_mul_f32 v98, v98, v219\n\t" \
  "v_mul_f32 v99, v99, v219\n\t" \
  "v_mul_f32 v104, v104, v219\n\t" \
  "v_mul_f32 v105, v105, v219\n\t" \
  "v_mul_f32 v106, v106, v219\n\t" \
  "v_mul_f32 v107, v107, v219\n\t" \
  "v_mul_f32 v112, v112, v219\n\t" \
  "v_mul_f32 v113, v113, v219\n\t" \
  "v_mul_f32 v114, v114, v219\n\t" \
  "v_mul_f32 v115, v115, v219\n\t" \
  "v_mul_f32 v120, v120, v219\n\t" \
  "v_mul_f32 v121, v121, v219\n\t" \
  "v_mul_f32 v122, v122, v219\n\t" \
  "v_mul_f32 v123, v123, v219\n\t" \
  "v_max3_f32 v216, v184, v185, v186\n\t" \
  "v_max3_f32 v217, v187, v188, v189\n\t" \
  "v_max3_f32 v216, v216, v190, v191\n\t" \
  "v_max_f32 v216, v216, v217\n\t" \
  "v_mov_b32 v217, v216\n\t" \
  "s_nop 1\n\t" \
  "v_permlane16_swap_b32 v216, v217\n\t" \
  "s_nop 1\n\t" \
  "v_max_f32 v216, v216, v217\n\t" \
  "v_mov_b32 v217, v216\n\t" \
  "s_nop 1\n\t" \
  "v_permlane32_swap_b32 v216, v217\n\t" \
  "s_nop 1\n\t" \
  "v_max_f32 v216, v216, v217\n\t" \
  "v_add_f32 v218, 0x40c00000, v216\n\t" \
  "v_max_f32 v218, 0, v218\n\t" \
  "v_sub_f32 v184, v184, v218\n\t" \
  "v_sub_f32 v185, v185, v218\n\t" \
  "v_sub_f32 v186, v186, v218\n\t" \
  "v_sub_f32 v187, v187, v218\n\t" \
  "v_sub_f32 v188, v188, v218\n\t" \
  "v_sub_f32 v189, v189, v218\n\t" \
  "v_sub_f32 v190, v190, v218\n\t" \
  "v_sub_f32 v191, v191, v218\n\t" \
  "v_sub_f32 v204, v204, v218\n\t" \
  "v_sub_f32 v205, v205, v218\n\t" \
  "v_sub_f32 v206, v206, v218\n\t" \
  "v_sub_f32 v207, v207, v218\n\t" \
  "v_exp_f32 v208, v184\n\t" \
  "v_exp_f32 v209, v185\n\t" \
  "v_exp_f32 v210, v186\n\t" \
  "v_exp_f32 v211, v187\n\t" \
  "v_exp_f32 v212, v188\n\t" \
  "v_exp_f32 v213, v189\n\t" \
  "v_exp_f32 v214, v190\n\t" \
  "v_exp_f32 v215, v191\n\t" \
  "v_exp_f32 v219, -v218\n\t" \
  "s_nop 0\n\t" \
  "v_cvt_pk_bf16_f32 v196, v208, v209\n\t" \
  "v_cvt_pk_bf16_f32 v197, v210, v211\n\t" \
  "v_cvt_pk_bf16_f32 v198, v212, v213\n\t" \
  "v_cvt_pk_bf16_f32 v199, v214, v215\n\t" \
  "v_mul_f32 v132, v132, v219\n\t" \
  "v_mul_f32 v133, v133, v219\n\t" \
  "v_mul_f32 v134, v134, v219\n\t" \
  "v_mul_f32 v135, v135, v219\n\t" \
  "v_mul_f32 v68, v68, v219\n\t" \
  "v_mul_f32 v69, v69, v219\n\t" \
  "v_mul_f32 v70, v70, v219\n\t" \
  "v_mul_f32 v71, v71, v219\n\t" \
  "v_mul_f32 v76, v76, v219\n\t" \
  "v_mul_f32 v77, v77, v219\n\t" \
  "v_mul_f32 v78, v78, v219\n\t" \
  "v_mul_f32 v79, v79, v219\n\t" \
  "v_mul_f32 v84, v84, v219\n\t" \
  "v_mul_f32 v85, v85, v219\n\t" \
  "v_mul_f32 v86, v86, v219\n\t" \
  "v_mul_f32 v87, v87, v219\n\t" \
  "v_mul_f32 v92, v92, v219\n\t" \
  "v_mul_f32 v93, v93, v219\n\t" \
  "v_mul_f32 v94, v94, v219\n\t" \
  "v_mul_f32 v95, v95, v219\n\t" \
  "v_mul_f32 v100, v100, v219\n\t" \
  "v_mul_f32 v101, v101, v219\n\t" \
  "v_mul_f32 v102, v102, v219\n\t" \
  "v_mul_f32 v103, v103, v219\n\t" \
  "v_mul_f32 v108, v108, v219\n\t" \
  "v_mul_f32 v109, v109, v219\n\t" \
  "v_mul_f32 v110, v110, v219\n\t" \
  "v_mul_f32 v111, v111, v219\n\t" \
  "v_mul_f32 v116, v116, v219\n\t" \
  "v_mul_f32 v117, v117, v219\n\t" \
  "v_mul_f32 v118, v118, v219\n\t" \
  "v_mul_f32 v119, v119, v219\n\t" \
  "v_mul_f32 v124, v124, v219\n\t" \
  "v_mul_f32 v125, v125, v219\n\t" \
  "v_mul_f32 v126, v126, v219\n\t" \
  "v_mul_f32 v127, v127, v219\n\t" \
  "s_nop 3\n\t" \
  "s_branch .Lback0_%=\n\t" \
  ".Lrare1_%=:\n\t" \
  "s_nop 7\n\t" \
  "s_nop 7\n\t" \
  "v_max3_f32 v216, v176, v177, v178\n\t" \
  "v_max3_f32 v217, v179, v180, v181\n\t" \
  "v_max3_f32 v216, v216, v182, v183\n\t" \
  "v_max_f32 v216, v216, v217\n\t" \
  "v_mov_b32 v217, v216\n\t" \
  "s_nop 1\n\t" \
  "v_permlane16_swap_b32 v216, v217\n\t" \
  "s_nop 1\n\t" \
  "v_max_f32 v216, v216, v217\n\t" \
  "v_mov_b32 v217, v216\n\t" \
  "s_nop 1\n\t" \
  "v_permlane32_swap_b32 v216, v217\n\t" \
  "s_nop 1\n\t" \
  "v_max_f32 v216, v216, v217\n\t" \
  "v_add_f32 v218, 0x40c00000, v216\n\t" \
  "v_max_f32 v218, 0, v218\n\t" \
  "v_sub_f32 v176, v176, v218\n\t" \
  "v_sub_f32 v177, v177, v218\n\t" \
  "v_sub_f32 v178, v178, v218\n\t" \
  "v_sub_f32 v179, v179, v218\n\t" \
  "v_sub_f32 v180, v180, v218\n\t" \
  "v_sub_f32 v181, v181, v218\n\t" \
  "v_sub_f32 v182, v182, v218\n\t" \
  "v_sub_f32 v183, v183, v218\n\t" \
  "v_sub_f32 v200, v200, v218\n\t" \
  "v_sub_f32 v201, v201, v218\n\t" \
  "v_sub_f32 v202, v202, v218\n\t" \
  "v_sub_f32 v203, v203, v218\n\t" \
  "v_sub_f32 v168, v168, v218\n\t" \
  "v_sub_f32 v169, v169, v218\n\t" \
  "v_sub_f32 v170, v170, v218\n\t" \
  "v_sub_f32 v171, v171, v218\n\t" \
  "v_sub_f32 v172, v172, v218\n\t" \
  "v_sub_f32 v173, v173, v218\n\t" \
  "v_sub_f32 v174, v174, v218\n\t" \
  "v_sub_f32 v175, v175, v218\n\t" \
  "v_exp_f32 v208, v176\n\t" \
  "v_exp_f32 v209, v177\n\t" \
  "v_exp_f32 v210, v178\n\t" \
  "v_exp_f32 v211, v179\n\t" \
  "v_exp_f32 v212, v180\n\t" \
  "v_exp_f32 v213, v181\n\t" \
  "v_exp_f32 v214, v182\n\t" \
  "v_exp_f32 v215, v183\n\t" \
  "v_exp_f32 v219, -v218\n\t" \
  "s_nop 0\n\t" \
  "v_cvt_pk_bf16_f32 v192, v208, v209\n\t" \
  "v_cvt_pk_bf16_f32 v193, v210, v211\n\t" \
  "v_cvt_pk_bf16_f32 v194, v212, v213\n\t" \
  "v_cvt_pk_bf16_f32 v195, v214, v215\n\t" \
  "v_mul_f32 v128, v128, v219\n\t" \
  "v_mul_f32 v129, v129, v219\n\t" \
  "v_mul_f32 v130, v130, v219\n\t" \
  "v_mul_f32 v131, v131, v219\n\t" \
  "v_mul_f32 v64, v64, v219\n\t" \
  "v_mul_f32 v65, v65, v219\n\t" \
  "v_mul_f32 v66, v66, v219\n\t" \
  "v_mul_f32 v67, v67, v219\n\t" \
  "v_mul_f32 v72, v72, v219\n\t" \
  "v_mul_f32 v73, v73, v219\n\t" \
  "v_mul_f32 v74, v74, v219\n\t" \
  "v_mul_f32 v75, v75, v219\n\t" \
  "v_mul_f32 v80, v80, v219\n\t" \
  "v_mul_f32 v81, v81, v219\n\t" \
  "v_mul_f32 v82, v82, v219\n\t" \
  "v_mul_f32 v83, v83, v219\n\t" \
  "v_mul_f32 v88, v88, v219\n\t" \
  "v_mul_f32 v89, v89, v219\n\t" \
  "v_mul_f32 v90, v90, v219\n\t" \
  "v_mul_f32 v91, v91, v219\n\t" \
  "v_mul_f32 v96, v96, v219\n\t" \
  "v_mul_f32 v97, v97, v219\n\t" \
  "v_mul_f32 v98, v98, v219\n\t" \
  "v_mul_f32 v99, v99, v219\n\t" \
  "v_mul_f32 v104, v104, v219\n\t" \
  "v_mul_f32 v105, v105, v219\n\t" \
  "v_mul_f32 v106, v106, v219\n\t" \
  "v_mul_f32 v107, v107, v219\n\t" \
  "v_mul_f32 v112, v112, v219\n\t" \
  "v_mul_f32 v113, v113, v219\n\t" \
  "v_mul_f32 v114, v114, v219\n\t" \
  "v_mul_f32 v115, v115, v219\n\t" \
  "v_mul_f32 v120, v120, v219\n\t" \
  "v_mul_f32 v121, v121, v219\n\t" \
  "v_mul_f32 v122, v122, v219\n\t" \
  "v_mul_f32 v123, v123, v219\n\t" \
  "v_max3_f32 v216, v184, v185, v186\n\t" \
  "v_max3_f32 v217, v187, v188, v189\n\t" \
  "v_max3_f32 v216, v216, v190, v191\n\t" \
  "v_max_f32 v216, v216, v217\n\t" \
  "v_mov_b32 v217, v216\n\t" \
  "s_nop 1\n\t" \
  "v_permlane16_swap_b32 v216, v217\n\t" \
  "s_nop 1\n\t" \
  "v_max_f32 v216, v216, v217\n\t" \
  "v_mov_b32 v217, v216\n\t" \
  "s_nop 1\n\t" \
  "v_permlane32_swap_b32 v216, v217\n\t" \
  "s_nop 1\n\t" \
  "v_max_f32 v216, v216, v217\n\t" \
  "v_add_f32 v218, 0x40c00000, v216\n\t" \
  "v_max_f32 v218, 0, v218\n\t" \
  "v_sub_f32 v184, v184, v218\n\t" \
  "v_sub_f32 v185, v185, v218\n\t" \
  "v_sub_f32 v186, v186, v218\n\t" \
  "v_sub_f32 v187, v187, v218\n\t" \
  "v_sub_f32 v188, v188, v218\n\t" \
  "v_sub_f32 v189, v189, v218\n\t" \
  "v_sub_f32 v190, v190, v218\n\t" \
  "v_sub_f32 v191, v191, v218\n\t" \
  "v_sub_f32 v204, v204, v218\n\t" \
  "v_sub_f32 v205, v205, v218\n\t" \
  "v_sub_f32 v206, v206, v218\n\t" \
  "v_sub_f32 v207, v207, v218\n\t" \
  "v_exp_f32 v208, v184\n\t" \
  "v_exp_f32 v209, v185\n\t" \
  "v_exp_f32 v210, v186\n\t" \
  "v_exp_f32 v211, v187\n\t" \
  "v_exp_f32 v212, v188\n\t" \
  "v_exp_f32 v213, v189\n\t" \
  "v_exp_f32 v214, v190\n\t" \
  "v_exp_f32 v215, v191\n\t" \
  "v_exp_f32 v219, -v218\n\t" \
  "s_nop 0\n\t" \
  "v_cvt_pk_bf16_f32 v196, v208, v209\n\t" \
  "v_cvt_pk_bf16_f32 v197, v210, v211\n\t" \
  "v_cvt_pk_bf16_f32 v198, v212, v213\n\t" \
  "v_cvt_pk_bf16_f32 v199, v214, v215\n\t" \
  "v_mul_f32 v132, v132, v219\n\t" \
  "v_mul_f32 v133, v133, v219\n\t" \
  "v_mul_f32 v134, v134, v219\n\t" \
  "v_mul_f32 v135, v135, v219\n\t" \
  "v_mul_f32 v68, v68, v219\n\t" \
  "v_mul_f32 v69, v69, v219\n\t" \
  "v_mul_f32 v70, v70, v219\n\t" \
  "v_mul_f32 v71, v71, v219\n\t" \
  "v_mul_f32 v76, v76, v219\n\t" \
  "v_mul_f32 v77, v77, v219\n\t" \
  "v_mul_f32 v78, v78, v219\n\t" \
  "v_mul_f32 v79, v79, v219\n\t" \
  "v_mul_f32 v84, v84, v219\n\t" \
  "v_mul_f32 v85, v85, v219\n\t" \
  "v_mul_f32 v86, v86, v219\n\t" \
  "v_mul_f32 v87, v87, v219\n\t" \
  "v_mul_f32 v92, v92, v219\n\t" \
  "v_mul_f32 v93, v93, v219\n\t" \
  "v_mul_f32 v94, v94, v219\n\t" \
  "v_mul_f32 v95, v95, v219\n\t" \
  "v_mul_f32 v100, v100, v219\n\t" \
  "v_mul_f32 v101, v101, v219\n\t" \
  "v_mul_f32 v102, v102, v219\n\t" \
  "v_mul_f32 v103, v103, v219\n\t" \
  "v_mul_f32 v108, v108, v219\n\t" \
  "v_mul_f32 v109, v109, v219\n\t" \
  "v_mul_f32 v110, v110, v219\n\t" \
  "v_mul_f32 v111, v111, v219\n\t" \
  "v_mul_f32 v116, v116, v219\n\t" \
  "v_mul_f32 v117, v117, v219\n\t" \
  "v_mul_f32 v118, v118, v219\n\t" \
  "v_mul_f32 v119, v119, v219\n\t" \
  "v_mul_f32 v124, v124, v219\n\t" \
  "v_mul_f32 v125, v125, v219\n\t" \
  "v_mul_f32 v126, v126, v219\n\t" \
  "v_mul_f32 v127, v127, v219\n\t" \
  "s_nop 3\n\t" \
  "s_branch .Lback1_%=\n\t" \
  ".Lrare2_%=:\n\t" \
  "s_nop 7\n\t" \
  "s_nop 7\n\t" \
  "v_max3_f32 v216, v168, v169, v170\n\t" \
  "v_max3_f32 v217, v171, v172, v173\n\t" \
  "v_max3_f32 v216, v216, v174, v175\n\t" \
  "v_max_f32 v216, v216, v217\n\t" \
  "v_mov_b32 v217, v216\n\t" \
  "s_nop 1\n\t" \
  "v_permlane16_swap_b32 v216, v217\n\t" \
  "s_nop 1\n\t" \
  "v_max_f32 v216, v216, v217\n\t" \
  "v_mov_b32 v217, v216\n\t" \
  "s_nop 1\n\t" \
  "v_permlane32_swap_b32 v216, v217\n\t" \
  "s_nop 1\n\t" \
  "v_max_f32 v216, v216, v217\n\t" \
  "v_add_f32 v218, 0x40c00000, v216\n\t" \
  "v_max_f32 v218, 0, v218\n\t" \
  "v_sub_f32 v168, v168, v218\n\t" \
  "v_sub_f32 v169, v169, v218\n\t" \
  "v_sub_f32 v170, v170, v218\n\t" \
  "v_sub_f32 v171, v171, v218\n\t" \
  "v_sub_f32 v172, v172, v218\n\t" \
  "v_sub_f32 v173, v173, v218\n\t" \
  "v_sub_f32 v174, v174, v218\n\t" \
  "v_sub_f32 v175, v175, v218\n\t" \
  "v_sub_f32 v200, v200, v218\n\t" \
  "v_sub_f32 v201, v201, v218\n\t" \
  "v_sub_f32 v202, v202, v218\n\t" \
  "v_sub_f32 v203, v203, v218\n\t" \
  "v_sub_f32 v176, v176, v218\n\t" \
  "v_sub_f32 v177, v177, v218\n\t" \
  "v_sub_f32 v178, v178, v218\n\t" \
  "v_sub_f32 v179, v179, v218\n\t" \
  "v_sub_f32 v180, v180, v218\n\t" \
  "v_sub_f32 v181, v181, v218\n\t" \
  "v_sub_f32 v182, v182, v218\n\t" \
  "v_sub_f32 v183, v183, v218\n\t" \
  "v_exp_f32 v208, v168\n\t" \
  "v_exp_f32 v209, v169\n\t" \
  "v_exp_f32 v210, v170\n\t" \
  "v_exp_f32 v211, v171\n\t" \
  "v_exp_f32 v212, v172\n\t" \
  "v_exp_f32 v213, v173\n\t" \
  "v_exp_f32 v214, v174\n\t" \
  "v_exp_f32 v215, v175\n\t" \
  "v_exp_f32 v219, -v218\n\t" \
  "s_nop 0\n\t" \
  "v_cvt_pk_bf16_f32 v192, v208, v209\n\t" \
  "v_cvt_pk_bf16_f32 v193, v210, v211\n\t" \
  "v_cvt_pk_bf16_f32 v194, v212, v213\n\t" \
  "v_cvt_pk_bf16_f32 v195, v214, v215\n\t" \
  "v_mul_f32 v128, v128, v219\n\t" \
  "v_mul_f32 v129, v129, v219\n\t" \
  "v_mul_f32 v130, v130, v219\n\t" \
  "v_mul_f32 v131, v131, v219\n\t" \
  "v_mul_f32 v64, v64, v219\n\t" \
  "v_mul_f32 v65, v65, v219\n\t" \
  "v_mul_f32 v66, v66, v219\n\t" \
  "v_mul_f32 v67, v67, v219\n\t" \
  "v_mul_f32 v72, v72, v219\n\t" \
  "v_mul_f32 v73, v73, v219\n\t" \
  "v_mul_f32 v74, v74, v219\n\t" \
  "v_mul_f32 v75, v75, v219\n\t" \
  "v_mul_f32 v80, v80, v219\n\t" \
  "v_mul_f32 v81, v81, v219\n\t" \
  "v_mul_f32 v82, v82, v219\n\t" \
  "v_mul_f32 v83, v83, v219\n\t" \
  "v_mul_f32 v88, v88, v219\n\t" \
  "v_mul_f32 v89, v89, v219\n\t" \
  "v_mul_f32 v90, v90, v219\n\t" \
  "v_mul_f32 v91, v91, v219\n\t" \
  "v_mul_f32 v96, v96, v219\n\t" \
  "v_mul_f32 v97, v97, v219\n\t" \
  "v_mul_f32 v98, v98, v219\n\t" \
  "v_mul_f32 v99, v99, v219\n\t" \
  "v_mul_f32 v104, v104, v219\n\t" \
  "v_mul_f32 v105, v105, v219\n\t" \
  "v_mul_f32 v106, v106, v219\n\t" \
  "v_mul_f32 v107, v107, v219\n\t" \
  "v_mul_f32 v112, v112, v219\n\t" \
  "v_mul_f32 v113, v113, v219\n\t" \
  "v_mul_f32 v114, v114, v219\n\t" \
  "v_mul_f32 v115, v115, v219\n\t" \
  "v_mul_f32 v120, v120, v219\n\t" \
  "v_mul_f32 v121, v121, v219\n\t" \
  "v_mul_f32 v122, v122, v219\n\t" \
  "v_mul_f32 v123, v123, v219\n\t" \
  "v_max3_f32 v216, v184, v185, v186\n\t" \
  "v_max3_f32 v217, v187, v188, v189\n\t" \
  "v_max3_f32 v216, v216, v190, v191\n\t" \
  "v_max_f32 v216, v216, v217\n\t" \
  "v_mov_b32 v217, v216\n\t" \
  "s_nop 1\n\t" \
  "v_permlane16_swap_b32 v216, v217\n\t" \
  "s_nop 1\n\t" \
  "v_max_f32 v216, v216, v217\n\t" \
  "v_mov_b32 v217, v216\n\t" \
  "s_nop 1\n\t" \
  "v_permlane32_swap_b32 v216, v217\n\t" \
  "s_nop 1\n\t" \
  "v_max_f32 v216, v216, v217\n\t" \
  "v_add_f32 v218, 0x40c00000, v216\n\t" \
  "v_max_f32 v218, 0, v218\n\t" \
  "v_sub_f32 v184, v184, v218\n\t" \
  "v_sub_f32 v185, v185, v218\n\t" \
  "v_sub_f32 v186, v186, v218\n\t" \
  "v_sub_f32 v187, v187, v218\n\t" \
  "v_sub_f32 v188, v188, v218\n\t" \
  "v_sub_f32 v189, v189, v218\n\t" \
  "v_sub_f32 v190, v190, v218\n\t" \
  "v_sub_f32 v191, v191, v218\n\t" \
  "v_sub_f32 v204, v204, v218\n\t" \
  "v_sub_f32 v205, v205, v218\n\t" \
  "v_sub_f32 v206, v206, v218\n\t" \
  "v_sub_f32 v207, v207, v218\n\t" \
  "v_exp_f32 v208, v184\n\t" \
  "v_exp_f32 v209, v185\n\t" \
  "v_exp_f32 v210, v186\n\t" \
  "v_exp_f32 v211, v187\n\t" \
  "v_exp_f32 v212, v188\n\t" \
  "v_exp_f32 v213, v189\n\t" \
  "v_exp_f32 v214, v190\n\t" \
  "v_exp_f32 v215, v191\n\t" \
  "v_exp_f32 v219, -v218\n\t" \
  "s_nop 0\n\t" \
  "v_cvt_pk_bf16_f32 v196, v208, v209\n\t" \
  "v_cvt_pk_bf16_f32 v197, v210, v211\n\t" \
  "v_cvt_pk_bf16_f32 v198, v212, v213\n\t" \
  "v_cvt_pk_bf16_f32 v199, v214, v215\n\t" \
  "v_mul_f32 v132, v132, v219\n\t" \
  "v_mul_f32 v133, v133, v219\n\t" \
  "v_mul_f32 v134, v134, v219\n\t" \
  "v_mul_f32 v135, v135, v219\n\t" \
  "v_mul_f32 v68, v68, v219\n\t" \
  "v_mul_f32 v69, v69, v219\n\t" \
  "v_mul_f32 v70, v70, v219\n\t" \
  "v_mul_f32 v71, v71, v219\n\t" \
  "v_mul_f32 v76, v76, v219\n\t" \
  "v_mul_f32 v77, v77, v219\n\t" \
  "v_mul_f32 v78, v78, v219\n\t" \
  "v_mul_f32 v79, v79, v219\n\t" \
  "v_mul_f32 v84, v84, v219\n\t" \
  "v_mul_f32 v85, v85, v219\n\t" \
  "v_mul_f32 v86, v86, v219\n\t" \
  "v_mul_f32 v87, v87, v219\n\t" \
  "v_mul_f32 v92, v92, v219\n\t" \
  "v_mul_f32 v93, v93, v219\n\t" \
  "v_mul_f32 v94, v94, v219\n\t" \
  "v_mul_f32 v95, v95, v219\n\t" \
  "v_mul_f32 v100, v100, v219\n\t" \
  "v_mul_f32 v101, v101, v219\n\t" \
  "v_mul_f32 v102, v102, v219\n\t" \
  "v_mul_f32 v103, v103, v219\n\t" \
  "v_mul_f32 v108, v108, v219\n\t" \
  "v_mul_f32 v109, v109, v219\n\t" \
  "v_mul_f32 v110, v110, v219\n\t" \
  "v_mul_f32 v111, v111, v219\n\t" \
  "v_mul_f32 v116, v116, v219\n\t" \
  "v_mul_f32 v117, v117, v219\n\t" \
  "v_mul_f32 v118, v118, v219\n\t" \
  "v_mul_f32 v119, v119, v219\n\t" \
  "v_mul_f32 v124, v124, v219\n\t" \
  "v_mul_f32 v125, v125, v219\n\t" \
  "v_mul_f32 v126, v126, v219\n\t" \
  "v_mul_f32 v127, v127, v219\n\t" \
  "s_nop 3\n\t" \
  "s_branch .Lback2_%=\n\t" \
  ".Lrare3_%=:\n\t" \
  "s_nop 7\n\t" \
  "s_nop 7\n\t" \
  "v_max3_f32 v216, v176, v177, v178\n\t" \
  "v_max3_f32 v217, v179, v180, v181\n\t" \
  "v_max3_f32 v216, v216, v182, v183\n\t" \
  "v_max_f32 v216, v216, v217\n\t" \
  "v_mov_b32 v217, v216\n\t" \
  "s_nop 1\n\t" \
  "v_permlane16_swap_b32 v216, v217\n\t" \
  "s_nop 1\n\t" \
  "v_max_f32 v216, v216, v217\n\t" \
  "v_mov_b32 v217, v216\n\t" \
  "s_nop 1\n\t" \
  "v_permlane32_swap_b32 v216, v217\n\t" \
  "s_nop 1\n\t" \
  "v_max_f32 v216, v216, v217\n\t" \
  "v_add_f32 v218, 0x40c00000, v216\n\t" \
  "v_max_f32 v218, 0, v218\n\t" \
  "v_sub_f32 v176, v176, v218\n\t" \
  "v_sub_f32 v177, v177, v218\n\t" \
  "v_sub_f32 v178, v178, v218\n\t" \
  "v_sub_f32 v179, v179, v218\n\t" \
  "v_sub_f32 v180, v180, v218\n\t" \
  "v_sub_f32 v181, v181, v218\n\t" \
  "v_sub_f32 v182, v182, v218\n\t" \
  "v_sub_f32 v183, v183, v218\n\t" \
  "v_sub_f32 v200, v200, v218\n\t" \
  "v_sub_f32 v201, v201, v218\n\t" \
  "v_sub_f32 v202, v202, v218\n\t" \
  "v_sub_f32 v203, v203, v218\n\t" \
  "v_sub_f32 v168, v168, v218\n\t" \
  "v_sub_f32 v169, v169, v218\n\t" \
  "v_sub_f32 v170, v170, v218\n\t" \
  "v_sub_f32 v171, v171, v218\n\t" \
  "v_sub_f32 v172, v172, v218\n\t" \
  "v_sub_f32 v173, v173, v218\n\t" \
  "v_sub_f32 v174, v174, v218\n\t" \
  "v_sub_f32 v175, v175, v218\n\t" \
  "v_exp_f32 v208, v176\n\t" \
  "v_exp_f32 v209, v177\n\t" \
  "v_exp_f32 v210, v178\n\t" \
  "v_exp_f32 v211, v179\n\t" \
  "v_exp_f32 v212, v180\n\t" \
  "v_exp_f32 v213, v181\n\t" \
  "v_exp_f32 v214, v182\n\t" \
  "v_exp_f32 v215, v183\n\t" \
  "v_exp_f32 v219, -v218\n\t" \
  "s_nop 0\n\t" \
  "v_cvt_pk_bf16_f32 v192, v208, v209\n\t" \
  "v_cvt_pk_bf16_f32 v193, v210, v211\n\t" \
  "v_cvt_pk_bf16_f32 v194, v212, v213\n\t" \
  "v_cvt_pk_bf16_f32 v195, v214, v215\n\t" \
  "v_mul_f32 v128, v128, v219\n\t" \
  "v_mul_f32 v129, v129, v219\n\t" \
  "v_mul_f32 v130, v130, v219\n\t" \
  "v_mul_f32 v131, v131, v219\n\t" \
  "v_mul_f32 v64, v64, v219\n\t" \
  "v_mul_f32 v65, v65, v219\n\t" \
  "v_mul_f32 v66, v66, v219\n\t" \
  "v_mul_f32 v67, v67, v219\n\t" \
  "v_mul_f32 v72, v72, v219\n\t" \
  "v_mul_f32 v73, v73, v219\n\t" \
  "v_mul_f32 v74, v74, v219\n\t" \
  "v_mul_f32 v75, v75, v219\n\t" \
  "v_mul_f32 v80, v80, v219\n\t" \
  "v_mul_f32 v81, v81, v219\n\t" \
  "v_mul_f32 v82, v82, v219\n\t" \
  "v_mul_f32 v83, v83, v219\n\t" \
  "v_mul_f32 v88, v88, v219\n\t" \
  "v_mul_f32 v89, v89, v219\n\t" \
  "v_mul_f32 v90, v90, v219\n\t" \
  "v_mul_f32 v91, v91, v219\n\t" \
  "v_mul_f32 v96, v96, v219\n\t" \
  "v_mul_f32 v97, v97, v219\n\t" \
  "v_mul_f32 v98, v98, v219\n\t" \
  "v_mul_f32 v99, v99, v219\n\t" \
  "v_mul_f32 v104, v104, v219\n\t" \
  "v_mul_f32 v105, v105, v219\n\t" \
  "v_mul_f32 v106, v106, v219\n\t" \
  "v_mul_f32 v107, v107, v219\n\t" \
  "v_mul_f32 v112, v112, v219\n\t" \
  "v_mul_f32 v113, v113, v219\n\t" \
  "v_mul_f32 v114, v114, v219\n\t" \
  "v_mul_f32 v115, v115, v219\n\t" \
  "v_mul_f32 v120, v120, v219\n\t" \
  "v_mul_f32 v121, v121, v219\n\t" \
  "v_mul_f32 v122, v122, v219\n\t" \
  "v_mul_f32 v123, v123, v219\n\t" \
  "v_max3_f32 v216, v184, v185, v186\n\t" \
  "v_max3_f32 v217, v187, v188, v189\n\t" \
  "v_max3_f32 v216, v216, v190, v191\n\t" \
  "v_max_f32 v216, v216, v217\n\t" \
  "v_mov_b32 v217, v216\n\t" \
  "s_nop 1\n\t" \
  "v_permlane16_swap_b32 v216, v217\n\t" \
  "s_nop 1\n\t" \
  "v_max_f32 v216, v216, v217\n\t" \
  "v_mov_b32 v217, v216\n\t" \
  "s_nop 1\n\t" \
  "v_permlane32_swap_b32 v216, v217\n\t" \
  "s_nop 1\n\t" \
  "v_max_f32 v216, v216, v217\n\t" \
  "v_add_f32 v218, 0x40c00000, v216\n\t" \
  "v_max_f32 v218, 0, v218\n\t" \
  "v_sub_f32 v184, v184, v218\n\t" \
  "v_sub_f32 v185, v185, v218\n\t" \
  "v_sub_f32 v186, v186, v218\n\t" \
  "v_sub_f32 v187, v187, v218\n\t" \
  "v_sub_f32 v188, v188, v218\n\t" \
  "v_sub_f32 v189, v189, v218\n\t" \
  "v_sub_f32 v190, v190, v218\n\t" \
  "v_sub_f32 v191, v191, v218\n\t" \
  "v_sub_f32 v204, v204, v218\n\t" \
  "v_sub_f32 v205, v205, v218\n\t" \
  "v_sub_f32 v206, v206, v218\n\t" \
  "v_sub_f32 v207, v207, v218\n\t" \
  "v_exp_f32 v208, v184\n\t" \
  "v_exp_f32 v209, v185\n\t" \
  "v_exp_f32 v210, v186\n\t" \
  "v_exp_f32 v211, v187\n\t" \
  "v_exp_f32 v212, v188\n\t" \
  "v_exp_f32 v213, v189\n\t" \
  "v_exp_f32 v214, v190\n\t" \
  "v_exp_f32 v215, v191\n\t" \
  "v_exp_f32 v219, -v218\n\t" \
  "s_nop 0\n\t" \
  "v_cvt_pk_bf16_f32 v196, v208, v209\n\t" \
  "v_cvt_pk_bf16_f32 v197, v210, v211\n\t" \
  "v_cvt_pk_bf16_f32 v198, v212, v213\n\t" \
  "v_cvt_pk_bf16_f32 v199, v214, v215\n\t" \
  "v_mul_f32 v132, v132, v219\n\t" \
  "v_mul_f32 v133, v133, v219\n\t" \
  "v_mul_f32 v134, v134, v219\n\t" \
  "v_mul_f32 v135, v135, v219\n\t" \
  "v_mul_f32 v68, v68, v219\n\t" \
  "v_mul_f32 v69, v69, v219\n\t" \
  "v_mul_f32 v70, v70, v219\n\t" \
  "v_mul_f32 v71, v71, v219\n\t" \
  "v_mul_f32 v76, v76, v219\n\t" \
  "v_mul_f32 v77, v77, v219\n\t" \
  "v_mul_f32 v78, v78, v219\n\t" \
  "v_mul_f32 v79, v79, v219\n\t" \
  "v_mul_f32 v84, v84, v219\n\t" \
  "v_mul_f32 v85, v85, v219\n\t" \
  "v_mul_f32 v86, v86, v219\n\t" \
  "v_mul_f32 v87, v87, v219\n\t" \
  "v_mul_f32 v92, v92, v219\n\t" \
  "v_mul_f32 v93, v93, v219\n\t" \
  "v_mul_f32 v94, v94, v219\n\t" \
  "v_mul_f32 v95, v95, v219\n\t" \
  "v_mul_f32 v100, v100, v219\n\t" \
  "v_mul_f32 v101, v101, v219\n\t" \
  "v_mul_f32 v102, v102, v219\n\t" \
  "v_mul_f32 v103, v103, v219\n\t" \
  "v_mul_f32 v108, v108, v219\n\t" \
  "v_mul_f32 v109, v109, v219\n\t" \
  "v_mul_f32 v110, v110, v219\n\t" \
  "v_mul_f32 v111, v111, v219\n\t" \
  "v_mul_f32 v116, v116, v219\n\t" \
  "v_mul_f32 v117, v117, v219\n\t" \
  "v_mul_f32 v118, v118, v219\n\t" \
  "v_mul_f32 v119, v119, v219\n\t" \
  "v_mul_f32 v124, v124, v219\n\t" \
  "v_mul_f32 v125, v125, v219\n\t" \
  "v_mul_f32 v126, v126, v219\n\t" \
  "v_mul_f32 v127, v127, v219\n\t" \
  "s_nop 3\n\t" \
  "s_branch .Lback3_%=\n\t" \
  ".Lrare4_%=:\n\t" \
  "s_nop 7\n\t" \
  "s_nop 7\n\t" \
  "v_max3_f32 v216, v168, v169, v170\n\t" \
  "v_max3_f32 v217, v171, v172, v173\n\t" \
  "v_max3_f32 v216, v216, v174, v175\n\t" \
  "v_max_f32 v216, v216, v217\n\t" \
  "v_mov_b32 v217, v216\n\t" \
  "s_nop 1\n\t" \
  "v_permlane16_swap_b32 v216, v217\n\t" \
  "s_nop 1\n\t" \
  "v_max_f32 v216, v216, v217\n\t" \
  "v_mov_b32 v217, v216\n\t" \
  "s_nop 1\n\t" \
  "v_permlane32_swap_b32 v216, v217\n\t" \
  "s_nop 1\n\t" \
  "v_max_f32 v216, v216, v217\n\t" \
  "v_add_f32 v218, 0x40c00000, v216\n\t" \
  "v_max_f32 v218, 0, v218\n\t" \
  "v_sub_f32 v168, v168, v218\n\t" \
  "v_sub_f32 v169, v169, v218\n\t" \
  "v_sub_f32 v170, v170, v218\n\t" \
  "v_sub_f32 v171, v171, v218\n\t" \
  "v_sub_f32 v172, v172, v218\n\t" \
  "v_sub_f32 v173, v173, v218\n\t" \
  "v_sub_f32 v174, v174, v218\n\t" \
  "v_sub_f32 v175, v175, v218\n\t" \
  "v_sub_f32 v200, v200, v218\n\t" \
  "v_sub_f32 v201, v201, v218\n\t" \
  "v_sub_f32 v202, v202, v218\n\t" \
  "v_sub_f32 v203, v203, v218\n\t" \
  "v_sub_f32 v176, v176, v218\n\t" \
  "v_sub_f32 v177, v177, v218\n\t" \
  "v_sub_f32 v178, v178, v218\n\t" \
  "v_sub_f32 v179, v179, v218\n\t" \
  "v_sub_f32 v180, v180, v218\n\t" \
  "v_sub_f32 v181, v181, v218\n\t" \
  "v_sub_f32 v182, v182, v218\n\t" \
  "v_sub_f32 v183, v183, v218\n\t" \
  "v_exp_f32 v208, v168\n\t" \
  "v_exp_f32 v209, v169\n\t" \
  "v_exp_f32 v210, v170\n\t" \
  "v_exp_f32 v211, v171\n\t" \
  "v_exp_f32 v212, v172\n\t" \
  "v_exp_f32 v213, v173\n\t" \
  "v_exp_f32 v214, v174\n\t" \
  "v_exp_f32 v215, v175\n\t" \
  "v_exp_f32 v219, -v218\n\t" \
  "s_nop 0\n\t" \
  "v_cvt_pk_bf16_f32 v192, v208, v209\n\t" \
  "v_cvt_pk_bf16_f32 v193, v210, v211\n\t" \
  "v_cvt_pk_bf16_f32 v194, v212, v213\n\t" \
  "v_cvt_pk_bf16_f32 v195, v214, v215\n\t" \
  "v_mul_f32 v128, v128, v219\n\t" \
  "v_mul_f32 v129, v129, v219\n\t" \
  "v_mul_f32 v130, v130, v219\n\t" \
  "v_mul_f32 v131, v131, v219\n\t" \
  "v_mul_f32 v64, v64, v219\n\t" \
  "v_mul_f32 v65, v65, v219\n\t" \
  "v_mul_f32 v66, v66, v219\n\t" \
  "v_mul_f32 v67, v67, v219\n\t" \
  "v_mul_f32 v72, v72, v219\n\t" \
  "v_mul_f32 v73, v73, v219\n\t" \
  "v_mul_f32 v74, v74, v219\n\t" \
  "v_mul_f32 v75, v75, v219\n\t" \
  "v_mul_f32 v80, v80, v219\n\t" \
  "v_mul_f32 v81, v81, v219\n\t" \
  "v_mul_f32 v82, v82, v219\n\t" \
  "v_mul_f32 v83, v83, v219\n\t" \
  "v_mul_f32 v88, v88, v219\n\t" \
  "v_mul_f32 v89, v89, v219\n\t" \
  "v_mul_f32 v90, v90, v219\n\t" \
  "v_mul_f32 v91, v91, v219\n\t" \
  "v_mul_f32 v96, v96, v219\n\t" \
  "v_mul_f32 v97, v97, v219\n\t" \
  "v_mul_f32 v98, v98, v219\n\t" \
  "v_mul_f32 v99, v99, v219\n\t" \
  "v_mul_f32 v104, v104, v219\n\t" \
  "v_mul_f32 v105, v105, v219\n\t" \
  "v_mul_f32 v106, v106, v219\n\t" \
  "v_mul_f32 v107, v107, v219\n\t" \
  "v_mul_f32 v112, v112, v219\n\t" \
  "v_mul_f32 v113, v113, v219\n\t" \
  "v_mul_f32 v114, v114, v219\n\t" \
  "v_mul_f32 v115, v115, v219\n\t" \
  "v_mul_f32 v120, v120, v219\n\t" \
  "v_mul_f32 v121, v121, v219\n\t" \
  "v_mul_f32 v122, v122, v219\n\t" \
  "v_mul_f32 v123, v123, v219\n\t" \
  "v_max3_f32 v216, v184, v185, v186\n\t" \
  "v_max3_f32 v217, v187, v188, v189\n\t" \
  "v_max3_f32 v216, v216, v190, v191\n\t" \
  "v_max_f32 v216, v216, v217\n\t" \
  "v_mov_b32 v217, v216\n\t" \
  "s_nop 1\n\t" \
  "v_permlane16_swap_b32 v216, v217\n\t" \
  "s_nop 1\n\t" \
  "v_max_f32 v216, v216, v217\n\t" \
  "v_mov_b32 v217, v216\n\t" \
  "s_nop 1\n\t" \
  "v_permlane32_swap_b32 v216, v217\n\t" \
  "s_nop 1\n\t" \
  "v_max_f32 v216, v216, v217\n\t" \
  "v_add_f32 v218, 0x40c00000, v216\n\t" \
  "v_max_f32 v218, 0, v218\n\t" \
  "v_sub_f32 v184, v184, v218\n\t" \
  "v_sub_f32 v185, v185, v218\n\t" \
  "v_sub_f32 v186, v186, v218\n\t" \
  "v_sub_f32 v187, v187, v218\n\t" \
  "v_sub_f32 v188, v188, v218\n\t" \
  "v_sub_f32 v189, v189, v218\n\t" \
  "v_sub_f32 v190, v190, v218\n\t" \
  "v_sub_f32 v191, v191, v218\n\t" \
  "v_sub_f32 v204, v204, v218\n\t" \
  "v_sub_f32 v205, v205, v218\n\t" \
  "v_sub_f32 v206, v206, v218\n\t" \
  "v_sub_f32 v207, v207, v218\n\t" \
  "v_exp_f32 v208, v184\n\t" \
  "v_exp_f32 v209, v185\n\t" \
  "v_exp_f32 v210, v186\n\t" \
  "v_exp_f32 v211, v187\n\t" \
  "v_exp_f32 v212, v188\n\t" \
  "v_exp_f32 v213, v189\n\t" \
  "v_exp_f32 v214, v190\n\t" \
  "v_exp_f32 v215, v191\n\t" \
  "v_exp_f32 v219, -v218\n\t" \
  "s_nop 0\n\t" \
  "v_cvt_pk_bf16_f32 v196, v208, v209\n\t" \
  "v_cvt_pk_bf16_f32 v197, v210, v211\n\t" \
  "v_cvt_pk_bf16_f32 v198, v212, v213\n\t" \
  "v_cvt_pk_bf16_f32 v199, v214, v215\n\t" \
  "v_mul_f32 v132, v132, v219\n\t" \
  "v_mul_f32 v133, v133, v219\n\t" \
  "v_mul_f32 v134, v134, v219\n\t" \
  "v_mul_f32 v135, v135, v219\n\t" \
  "v_mul_f32 v68, v68, v219\n\t" \
  "v_mul_f32 v69, v69, v219\n\t" \
  "v_mul_f32 v70, v70, v219\n\t" \
  "v_mul_f32 v71, v71, v219\n\t" \
  "v_mul_f32 v76, v76, v219\n\t" \
  "v_mul_f32 v77, v77, v219\n\t" \
  "v_mul_f32 v78, v78, v219\n\t" \
  "v_mul_f32 v79, v79, v219\n\t" \
  "v_mul_f32 v84, v84, v219\n\t" \
  "v_mul_f32 v85, v85, v219\n\t" \
  "v_mul_f32 v86, v86, v219\n\t" \
  "v_mul_f32 v87, v87, v219\n\t" \
  "v_mul_f32 v92, v92, v219\n\t" \
  "v_mul_f32 v93, v93, v219\n\t" \
  "v_mul_f32 v94, v94, v219\n\t" \
  "v_mul_f32 v95, v95, v219\n\t" \
  "v_mul_f32 v100, v100, v219\n\t" \
  "v_mul_f32 v101, v101, v219\n\t" \
  "v_mul_f32 v102, v102, v219\n\t" \
  "v_mul_f32 v103, v103, v219\n\t" \
  "v_mul_f32 v108, v108, v219\n\t" \
  "v_mul_f32 v109, v109, v219\n\t" \
  "v_mul_f32 v110, v110, v219\n\t" \
  "v_mul_f32 v111, v111, v219\n\t" \
  "v_mul_f32 v116, v116, v219\n\t" \
  "v_mul_f32 v117, v117, v219\n\t" \
  "v_mul_f32 v118, v118, v219\n\t" \
  "v_mul_f32 v119, v119, v219\n\t" \
  "v_mul_f32 v124, v124, v219\n\t" \
  "v_mul_f32 v125, v125, v219\n\t" \
  "v_mul_f32 v126, v126, v219\n\t" \
  "v_mul_f32 v127, v127, v219\n\t" \
  "s_nop 3\n\t" \
  "s_branch .Lback4_%=\n\t" \
  ".Lrare5_%=:\n\t" \
  "s_nop 7\n\t" \
  "s_nop 7\n\t" \
  "v_max3_f32 v216, v176, v177, v178\n\t" \
  "v_max3_f32 v217, v179, v180, v181\n\t" \
  "v_max3_f32 v216, v216, v182, v183\n\t" \
  "v_max_f32 v216, v216, v217\n\t" \
  "v_mov_b32 v217, v216\n\t" \
  "s_nop 1\n\t" \
  "v_permlane16_swap_b32 v216, v217\n\t" \
  "s_nop 1\n\t" \
  "v_max_f32 v216, v216, v217\n\t" \
  "v_mov_b32 v217, v216\n\t" \
  "s_nop 1\n\t" \
  "v_permlane32_swap_b32 v216, v217\n\t" \
  "s_nop 1\n\t" \
  "v_max_f32 v216, v216, v217\n\t" \
  "v_add_f32 v218, 0x40c00000, v216\n\t" \
  "v_max_f32 v218, 0, v218\n\t" \
  "v_sub_f32 v176, v176, v218\n\t" \
  "v_sub_f32 v177, v177, v218\n\t" \
  "v_sub_f32 v178, v178, v218\n\t" \
  "v_sub_f32 v179, v179, v218\n\t" \
  "v_sub_f32 v180, v180, v218\n\t" \
  "v_sub_f32 v181, v181, v218\n\t" \
  "v_sub_f32 v182, v182, v218\n\t" \
  "v_sub_f32 v183, v183, v218\n\t" \
  "v_sub_f32 v200, v200, v218\n\t" \
  "v_sub_f32 v201, v201, v218\n\t" \
  "v_sub_f32 v202, v202, v218\n\t" \
  "v_sub_f32 v203, v203, v218\n\t" \
  "v_sub_f32 v168, v168, v218\n\t" \
  "v_sub_f32 v169, v169, v218\n\t" \
  "v_sub_f32 v170, v170, v218\n\t" \
  "v_sub_f32 v171, v171, v218\n\t" \
  "v_sub_f32 v172, v172, v218\n\t" \
  "v_sub_f32 v173, v173, v218\n\t" \
  "v_sub_f32 v174, v174, v218\n\t" \
  "v_sub_f32 v175, v175, v218\n\t" \
  "v_exp_f32 v208, v176\n\t" \
  "v_exp_f32 v209, v177\n\t" \
  "v_exp_f32 v210, v178\n\t" \
  "v_exp_f32 v211, v179\n\t" \
  "v_exp_f32 v212, v180\n\t" \
  "v_exp_f32 v213, v181\n\t" \
  "v_exp_f32 v214, v182\n\t" \
  "v_exp_f32 v215, v183\n\t" \
  "v_exp_f32 v219, -v218\n\t" \
  "s_nop 0\n\t" \
  "v_cvt_pk_bf16_f32 v192, v208, v209\n\t" \
  "v_cvt_pk_bf16_f32 v193, v210, v211\n\t" \
  "v_cvt_pk_bf16_f32 v194, v212, v213\n\t" \
  "v_cvt_pk_bf16_f32 v195, v214, v215\n\t" \
  "v_mul_f32 v128, v128, v219\n\t" \
  "v_mul_f32 v129, v129, v219\n\t" \
  "v_mul_f32 v130, v130, v219\n\t" \
  "v_mul_f32 v131, v131, v219\n\t" \
  "v_mul_f32 v64, v64, v219\n\t" \
  "v_mul_f32 v65, v65, v219\n\t" \
  "v_mul_f32 v66, v66, v219\n\t" \
  "v_mul_f32 v67, v67, v219\n\t" \
  "v_mul_f32 v72, v72, v219\n\t" \
  "v_mul_f32 v73, v73, v219\n\t" \
  "v_mul_f32 v74, v74, v219\n\t" \
  "v_mul_f32 v75, v75, v219\n\t" \
  "v_mul_f32 v80, v80, v219\n\t" \
  "v_mul_f32 v81, v81, v219\n\t" \
  "v_mul_f32 v82, v82, v219\n\t" \
  "v_mul_f32 v83, v83, v219\n\t" \
  "v_mul_f32 v88, v88, v219\n\t" \
  "v_mul_f32 v89, v89, v219\n\t" \
  "v_mul_f32 v90, v90, v219\n\t" \
  "v_mul_f32 v91, v91, v219\n\t" \
  "v_mul_f32 v96, v96, v219\n\t" \
  "v_mul_f32 v97, v97, v219\n\t" \
  "v_mul_f32 v98, v98, v219\n\t" \
  "v_mul_f32 v99, v99, v219\n\t" \
  "v_mul_f32 v104, v104, v219\n\t" \
  "v_mul_f32 v105, v105, v219\n\t" \
  "v_mul_f32 v106, v106, v219\n\t" \
  "v_mul_f32 v107, v107, v219\n\t" \
  "v_mul_f32 v112, v112, v219\n\t" \
  "v_mul_f32 v113, v113, v219\n\t" \
  "v_mul_f32 v114, v114, v219\n\t" \
  "v_mul_f32 v115, v115, v219\n\t" \
  "v_mul_f32 v120, v120, v219\n\t" \
  "v_mul_f32 v121, v121, v219\n\t" \
  "v_mul_f32 v122, v122, v219\n\t" \
  "v_mul_f32 v123, v123, v219\n\t" \
  "v_max3_f32 v216, v184, v185, v186\n\t" \
  "v_max3_f32 v217, v187, v188, v189\n\t" \
  "v_max3_f32 v216, v216, v190, v191\n\t" \
  "v_max_f32 v216, v216, v217\n\t" \
  "v_mov_b32 v217, v216\n\t" \
  "s_nop 1\n\t" \
  "v_permlane16_swap_b32 v216, v217\n\t" \
  "s_nop 1\n\t" \
  "v_max_f32 v216, v216, v217\n\t" \
  "v_mov_b32 v217, v216\n\t" \
  "s_nop 1\n\t" \
  "v_permlane32_swap_b32 v216, v217\n\t" \
  "s_nop 1\n\t" \
  "v_max_f32 v216, v216, v217\n\t" \
  "v_add_f32 v218, 0x40c00000, v216\n\t" \
  "v_max_f32 v218, 0, v218\n\t" \
  "v_sub_f32 v184, v184, v218\n\t" \
  "v_sub_f32 v185, v185, v218\n\t" \
  "v_sub_f32 v186, v186, v218\n\t" \
  "v_sub_f32 v187, v187, v218\n\t" \
  "v_sub_f32 v188, v188, v218\n\t" \
  "v_sub_f32 v189, v189, v218\n\t" \
  "v_sub_f32 v190, v190, v218\n\t" \
  "v_sub_f32 v191, v191, v218\n\t" \
  "v_sub_f32 v204, v204, v218\n\t" \
  "v_sub_f32 v205, v205, v218\n\t" \
  "v_sub_f32 v206, v206, v218\n\t" \
  "v_sub_f32 v207, v207, v218\n\t" \
  "v_exp_f32 v208, v184\n\t" \
  "v_exp_f32 v209, v185\n\t" \
  "v_exp_f32 v210, v186\n\t" \
  "v_exp_f32 v211, v187\n\t" \
  "v_exp_f32 v212, v188\n\t" \
  "v_exp_f32 v213, v189\n\t" \
  "v_exp_f32 v214, v190\n\t" \
  "v_exp_f32 v215, v191\n\t" \
  "v_exp_f32 v219, -v218\n\t" \
  "s_nop 0\n\t" \
  "v_cvt_pk_bf16_f32 v196, v208, v209\n\t" \
  "v_cvt_pk_bf16_f32 v197, v210, v211\n\t" \
  "v_cvt_pk_bf16_f32 v198, v212, v213\n\t" \
  "v_cvt_pk_bf16_f32 v199, v214, v215\n\t" \
  "v_mul_f32 v132, v132, v219\n\t" \
  "v_mul_f32 v133, v133, v219\n\t" \
  "v_mul_f32 v134, v134, v219\n\t" \
  "v_mul_f32 v135, v135, v219\n\t" \
  "v_mul_f32 v68, v68, v219\n\t" \
  "v_mul_f32 v69, v69, v219\n\t" \
  "v_mul_f32 v70, v70, v219\n\t" \
  "v_mul_f32 v71, v71, v219\n\t" \
  "v_mul_f32 v76, v76, v219\n\t" \
  "v_mul_f32 v77, v77, v219\n\t" \
  "v_mul_f32 v78, v78, v219\n\t" \
  "v_mul_f32 v79, v79, v219\n\t" \
  "v_mul_f32 v84, v84, v219\n\t" \
  "v_mul_f32 v85, v85, v219\n\t" \
  "v_mul_f32 v86, v86, v219\n\t" \
  "v_mul_f32 v87, v87, v219\n\t" \
  "v_mul_f32 v92, v92, v219\n\t" \
  "v_mul_f32 v93, v93, v219\n\t" \
  "v_mul_f32 v94, v94, v219\n\t" \
  "v_mul_f32 v95, v95, v219\n\t" \
  "v_mul_f32 v100, v100, v219\n\t" \
  "v_mul_f32 v101, v101, v219\n\t" \
  "v_mul_f32 v102, v102, v219\n\t" \
  "v_mul_f32 v103, v103, v219\n\t" \
  "v_mul_f32 v108, v108, v219\n\t" \
  "v_mul_f32 v109, v109, v219\n\t" \
  "v_mul_f32 v110, v110, v219\n\t" \
  "v_mul_f32 v111, v111, v219\n\t" \
  "v_mul_f32 v116, v116, v219\n\t" \
  "v_mul_f32 v117, v117, v219\n\t" \
  "v_mul_f32 v118, v118, v219\n\t" \
  "v_mul_f32 v119, v119, v219\n\t" \
  "v_mul_f32 v124, v124, v219\n\t" \
  "v_mul_f32 v125, v125, v219\n\t" \
  "v_mul_f32 v126, v126, v219\n\t" \
  "v_mul_f32 v127, v127, v219\n\t" \
  "s_nop 3\n\t" \
  "s_branch .Lback5_%=\n\t" \
  ".Lrare6_%=:\n\t" \
  "s_nop 7\n\t" \
  "s_nop 7\n\t" \
  "v_max3_f32 v216, v168, v169, v170\n\t" \
  "v_max3_f32 v217, v171, v172, v173\n\t" \
  "v_max3_f32 v216, v216, v174, v175\n\t" \
  "v_max_f32 v216, v216, v217\n\t" \
  "v_mov_b32 v217, v216\n\t" \
  "s_nop 1\n\t" \
  "v_permlane16_swap_b32 v216, v217\n\t" \
  "s_nop 1\n\t" \
  "v_max_f32 v216, v216, v217\n\t" \
  "v_mov_b32 v217, v216\n\t" \
  "s_nop 1\n\t" \
  "v_permlane32_swap_b32 v216, v217\n\t" \
  "s_nop 1\n\t" \
  "v_max_f32 v216, v216, v217\n\t" \
  "v_add_f32 v218, 0x40c00000, v216\n\t" \
  "v_max_f32 v218, 0, v218\n\t" \
  "v_sub_f32 v168, v168, v218\n\t" \
  "v_sub_f32 v169, v169, v218\n\t" \
  "v_sub_f32 v170, v170, v218\n\t" \
  "v_sub_f32 v171, v171, v218\n\t" \
  "v_sub_f32 v172, v172, v218\n\t" \
  "v_sub_f32 v173, v173, v218\n\t" \
  "v_sub_f32 v174, v174, v218\n\t" \
  "v_sub_f32 v175, v175, v218\n\t" \
  "v_sub_f32 v200, v200, v218\n\t" \
  "v_sub_f32 v201, v201, v218\n\t" \
  "v_sub_f32 v202, v202, v218\n\t" \
  "v_sub_f32 v203, v203, v218\n\t" \
  "v_sub_f32 v176, v176, v218\n\t" \
  "v_sub_f32 v177, v177, v218\n\t" \
  "v_sub_f32 v178, v178, v218\n\t" \
  "v_sub_f32 v179, v179, v218\n\t" \
  "v_sub_f32 v180, v180, v218\n\t" \
  "v_sub_f32 v181, v181, v218\n\t" \
  "v_sub_f32 v182, v182, v218\n\t" \
  "v_sub_f32 v183, v183, v218\n\t" \
  "v_exp_f32 v208, v168\n\t" \
  "v_exp_f32 v209, v169\n\t" \
  "v_exp_f32 v210, v170\n\t" \
  "v_exp_f32 v211, v171\n\t" \
  "v_exp_f32 v212, v172\n\t" \
  "v_exp_f32 v213, v173\n\t" \
  "v_exp_f32 v214, v174\n\t" \
  "v_exp_f32 v215, v175\n\t" \
  "v_exp_f32 v219, -v218\n\t" \
  "s_nop 0\n\t" \
  "v_cvt_pk_bf16_f32 v192, v208, v209\n\t" \
  "v_cvt_pk_bf16_f32 v193, v210, v211\n\t" \
  "v_cvt_pk_bf16_f32 v194, v212, v213\n\t" \
  "v_cvt_pk_bf16_f32 v195, v214, v215\n\t" \
  "v_mul_f32 v128, v128, v219\n\t" \
  "v_mul_f32 v129, v129, v219\n\t" \
  "v_mul_f32 v130, v130, v219\n\t" \
  "v_mul_f32 v131, v131, v219\n\t" \
  "v_mul_f32 v64, v64, v219\n\t" \
  "v_mul_f32 v65, v65, v219\n\t" \
  "v_mul_f32 v66, v66, v219\n\t" \
  "v_mul_f32 v67, v67, v219\n\t" \
  "v_mul_f32 v72, v72, v219\n\t" \
  "v_mul_f32 v73, v73, v219\n\t" \
  "v_mul_f32 v74, v74, v219\n\t" \
  "v_mul_f32 v75, v75, v219\n\t" \
  "v_mul_f32 v80, v80, v219\n\t" \
  "v_mul_f32 v81, v81, v219\n\t" \
  "v_mul_f32 v82, v82, v219\n\t" \
  "v_mul_f32 v83, v83, v219\n\t" \
  "v_mul_f32 v88, v88, v219\n\t" \
  "v_mul_f32 v89, v89, v219\n\t" \
  "v_mul_f32 v90, v90, v219\n\t" \
  "v_mul_f32 v91, v91, v219\n\t" \
  "v_mul_f32 v96, v96, v219\n\t" \
  "v_mul_f32 v97, v97, v219\n\t" \
  "v_mul_f32 v98, v98, v219\n\t" \
  "v_mul_f32 v99, v99, v219\n\t" \
  "v_mul_f32 v104, v104, v219\n\t" \
  "v_mul_f32 v105, v105, v219\n\t" \
  "v_mul_f32 v106, v106, v219\n\t" \
  "v_mul_f32 v107, v107, v219\n\t" \
  "v_mul_f32 v112, v112, v219\n\t" \
  "v_mul_f32 v113, v113, v219\n\t" \
  "v_mul_f32 v114, v114, v219\n\t" \
  "v_mul_f32 v115, v115, v219\n\t" \
  "v_mul_f32 v120, v120, v219\n\t" \
  "v_mul_f32 v121, v121, v219\n\t" \
  "v_mul_f32 v122, v122, v219\n\t" \
  "v_mul_f32 v123, v123, v219\n\t" \
  "v_max3_f32 v216, v184, v185, v186\n\t" \
  "v_max3_f32 v217, v187, v188, v189\n\t" \
  "v_max3_f32 v216, v216, v190, v191\n\t" \
  "v_max_f32 v216, v216, v217\n\t" \
  "v_mov_b32 v217, v216\n\t" \
  "s_nop 1\n\t" \
  "v_permlane16_swap_b32 v216, v217\n\t" \
  "s_nop 1\n\t" \
  "v_max_f32 v216, v216, v217\n\t" \
  "v_mov_b32 v217, v216\n\t" \
  "s_nop 1\n\t" \
  "v_permlane32_swap_b32 v216, v217\n\t" \
  "s_nop 1\n\t" \
  "v_max_f32 v216, v216, v217\n\t" \
  "v_add_f32 v218, 0x40c00000, v216\n\t" \
  "v_max_f32 v218, 0, v218\n\t" \
  "v_sub_f32 v184, v184, v218\n\t" \
  "v_sub_f32 v185, v185, v218\n\t" \
  "v_sub_f32 v186, v186, v218\n\t" \
  "v_sub_f32 v187, v187, v218\n\t" \
  "v_sub_f32 v188, v188, v218\n\t" \
  "v_sub_f32 v189, v189, v218\n\t" \
  "v_sub_f32 v190, v190, v218\n\t" \
  "v_sub_f32 v191, v191, v218\n\t" \
  "v_sub_f32 v204, v204, v218\n\t" \
  "v_sub_f32 v205, v205, v218\n\t" \
  "v_sub_f32 v206, v206, v218\n\t" \
  "v_sub_f32 v207, v207, v218\n\t" \
  "v_exp_f32 v208, v184\n\t" \
  "v_exp_f32 v209, v185\n\t" \
  "v_exp_f32 v210, v186\n\t" \
  "v_exp_f32 v211, v187\n\t" \
  "v_exp_f32 v212, v188\n\t" \
  "v_exp_f32 v213, v189\n\t" \
  "v_exp_f32 v214, v190\n\t" \
  "v_exp_f32 v215, v191\n\t" \
  "v_exp_f32 v219, -v218\n\t" \
  "s_nop 0\n\t" \
  "v_cvt_pk_bf16_f32 v196, v208, v209\n\t" \
  "v_cvt_pk_bf16_f32 v197, v210, v211\n\t" \
  "v_cvt_pk_bf16_f32 v198, v212, v213\n\t" \
  "v_cvt_pk_bf16_f32 v199, v214, v215\n\t" \
  "v_mul_f32 v132, v132, v219\n\t" \
  "v_mul_f32 v133, v133, v219\n\t" \
  "v_mul_f32 v134, v134, v219\n\t" \
  "v_mul_f32 v135, v135, v219\n\t" \
  "v_mul_f32 v68, v68, v219\n\t" \
  "v_mul_f32 v69, v69, v219\n\t" \
  "v_mul_f32 v70, v70, v219\n\t" \
  "v_mul_f32 v71, v71, v219\n\t" \
  "v_mul_f32 v76, v76, v219\n\t" \
  "v_mul_f32 v77, v77, v219\n\t" \
  "v_mul_f32 v78, v78, v219\n\t" \
  "v_mul_f32 v79, v79, v219\n\t" \
  "v_mul_f32 v84, v84, v219\n\t" \
  "v_mul_f32 v85, v85, v219\n\t" \
  "v_mul_f32 v86, v86, v219\n\t" \
  "v_mul_f32 v87, v87, v219\n\t" \
  "v_mul_f32 v92, v92, v219\n\t" \
  "v_mul_f32 v93, v93, v219\n\t" \
  "v_mul_f32 v94, v94, v219\n\t" \
  "v_mul_f32 v95, v95, v219\n\t" \
  "v_mul_f32 v100, v100, v219\n\t" \
  "v_mul_f32 v101, v101, v219\n\t" \
  "v_mul_f32 v102, v102, v219\n\t" \
  "v_mul_f32 v103, v103, v219\n\t" \
  "v_mul_f32 v108, v108, v219\n\t" \
  "v_mul_f32 v109, v109, v219\n\t" \
  "v_mul_f32 v110, v110, v219\n\t" \
  "v_mul_f32 v111, v111, v219\n\t" \
  "v_mul_f32 v116, v116, v219\n\t" \
  "v_mul_f32 v117, v117, v219\n\t" \
  "v_mul_f32 v118, v118, v219\n\t" \
  "v_mul_f32 v119, v119, v219\n\t" \
  "v_mul_f32 v124, v124, v219\n\t" \
  "v_mul_f32 v125, v125, v219\n\t" \
  "v_mul_f32 v126, v126, v219\n\t" \
  "v_mul_f32 v127, v127, v219\n\t" \
  "s_nop 3\n\t" \
  "s_branch .Lback6_%=\n\t" \
  ".Lrare7_%=:\n\t" \
  "s_nop 7\n\t" \
  "s_nop 7\n\t" \
  "v_max3_f32 v216, v176, v177, v178\n\t" \
  "v_max3_f32 v217, v179, v180, v181\n\t" \
  "v_max3_f32 v216, v216, v182, v183\n\t" \
  "v_max_f32 v216, v216, v217\n\t" \
  "v_mov_b32 v217, v216\n\t" \
  "s_nop 1\n\t" \
  "v_permlane16_swap_b32 v216, v217\n\t" \
  "s_nop 1\n\t" \
  "v_max_f32 v216, v216, v217\n\t" \
  "v_mov_b32 v217, v216\n\t" \
  "s_nop 1\n\t" \
  "v_permlane32_swap_b32 v216, v217\n\t" \
  "s_nop 1\n\t" \
  "v_max_f32 v216, v216, v217\n\t" \
  "v_add_f32 v218, 0x40c00000, v216\n\t" \
  "v_max_f32 v218, 0, v218\n\t" \
  "v_sub_f32 v176, v176, v218\n\t" \
  "v_sub_f32 v177, v177, v218\n\t" \
  "v_sub_f32 v178, v178, v218\n\t" \
  "v_sub_f32 v179, v179, v218\n\t" \
  "v_sub_f32 v180, v180, v218\n\t" \
  "v_sub_f32 v181, v181, v218\n\t" \
  "v_sub_f32 v182, v182, v218\n\t" \
  "v_sub_f32 v183, v183, v218\n\t" \
  "v_sub_f32 v200, v200, v218\n\t" \
  "v_sub_f32 v201, v201, v218\n\t" \
  "v_sub_f32 v202, v202, v218\n\t" \
  "v_sub_f32 v203, v203, v218\n\t" \
  "v_exp_f32 v208, v176\n\t" \
  "v_exp_f32 v209, v177\n\t" \
  "v_exp_f32 v210, v178\n\t" \
  "v_exp_f32 v211, v179\n\t" \
  "v_exp_f32 v212, v180\n\t" \
  "v_exp_f32 v213, v181\n\t" \
  "v_exp_f32 v214, v182\n\t" \
  "v_exp_f32 v215, v183\n\t" \
  "v_exp_f32 v219, -v218\n\t" \
  "s_nop 0\n\t" \
  "v_cvt_pk_bf16_f32 v192, v208, v209\n\t" \
  "v_cvt_pk_bf16_f32 v193, v210, v211\n\t" \
  "v_cvt_pk_bf16_f32 v194, v212, v213\n\t" \
  "v_cvt_pk_bf16_f32 v195, v214, v215\n\t" \
  "v_mul_f32 v128, v128, v219\n\t" \
  "v_mul_f32 v129, v129, v219\n\t" \
  "v_mul_f32 v130, v130, v219\n\t" \
  "v_mul_f32 v131, v131, v219\n\t" \
  "v_mul_f32 v64, v64, v219\n\t" \
  "v_mul_f32 v65, v65, v219\n\t" \
  "v_mul_f32 v66, v66, v219\n\t" \
  "v_mul_f32 v67, v67, v219\n\t" \
  "v_mul_f32 v72, v72, v219\n\t" \
  "v_mul_f32 v73, v73, v219\n\t" \
  "v_mul_f32 v74, v74, v219\n\t" \
  "v_mul_f32 v75, v75, v219\n\t" \
  "v_mul_f32 v80, v80, v219\n\t" \
  "v_mul_f32 v81, v81, v219\n\t" \
  "v_mul_f32 v82, v82, v219\n\t" \
  "v_mul_f32 v83, v83, v219\n\t" \
  "v_mul_f32 v88, v88, v219\n\t" \
  "v_mul_f32 v89, v89, v219\n\t" \
  "v_mul_f32 v90, v90, v219\n\t" \
  "v_mul_f32 v91, v91, v219\n\t" \
  "v_mul_f32 v96, v96, v219\n\t" \
  "v_mul_f32 v97, v97, v219\n\t" \
  "v_mul_f32 v98, v98, v219\n\t" \
  "v_mul_f32 v99, v99, v219\n\t" \
  "v_mul_f32 v104, v104, v219\n\t" \
  "v_mul_f32 v105, v105, v219\n\t" \
  "v_mul_f32 v106, v106, v219\n\t" \
  "v_mul_f32 v107, v107, v219\n\t" \
  "v_mul_f32 v112, v112, v219\n\t" \
  "v_mul_f32 v113, v113, v219\n\t" \
  "v_mul_f32 v114, v114, v219\n\t" \
  "v_mul_f32 v115, v115, v219\n\t" \
  "v_mul_f32 v120, v120, v219\n\t" \
  "v_mul_f32 v121, v121, v219\n\t" \
  "v_mul_f32 v122, v122, v219\n\t" \
  "v_mul_f32 v123, v123, v219\n\t" \
  "v_max3_f32 v216, v184, v185, v186\n\t" \
  "v_max3_f32 v217, v187, v188, v189\n\t" \
  "v_max3_f32 v216, v216, v190, v191\n\t" \
  "v_max_f32 v216, v216, v217\n\t" \
  "v_mov_b32 v217, v216\n\t" \
  "s_nop 1\n\t" \
  "v_permlane16_swap_b32 v216, v217\n\t" \
  "s_nop 1\n\t" \
  "v_max_f32 v216, v216, v217\n\t" \
  "v_mov_b32 v217, v216\n\t" \
  "s_nop 1\n\t" \
  "v_permlane32_swap_b32 v216, v217\n\t" \
  "s_nop 1\n\t" \
  "v_max_f32 v216, v216, v217\n\t" \
  "v_add_f32 v218, 0x40c00000, v216\n\t" \
  "v_max_f32 v218, 0, v218\n\t" \
  "v_sub_f32 v184, v184, v218\n\t" \
  "v_sub_f32 v185, v185, v218\n\t" \
  "v_sub_f32 v186, v186, v218\n\t" \
  "v_sub_f32 v187, v187, v218\n\t" \
  "v_sub_f32 v188, v188, v218\n\t" \
  "v_sub_f32 v189, v189, v218\n\t" \
  "v_sub_f32 v190, v190, v218\n\t" \
  "v_sub_f32 v191, v191, v218\n\t" \
  "v_sub_f32 v204, v204, v218\n\t" \
  "v_sub_f32 v205, v205, v218\n\t" \
  "v_sub_f32 v206, v206, v218\n\t" \
  "v_sub_f32 v207, v207, v218\n\t" \
  "v_exp_f32 v208, v184\n\t" \
  "v_exp_f32 v209, v185\n\t" \
  "v_exp_f32 v210, v186\n\t" \
  "v_exp_f32 v211, v187\n\t" \
  "v_exp_f32 v212, v188\n\t" \
  "v_exp_f32 v213, v189\n\t" \
  "v_exp_f32 v214, v190\n\t" \
  "v_exp_f32 v215, v191\n\t" \
  "v_exp_f32 v219, -v218\n\t" \
  "s_nop 0\n\t" \
  "v_cvt_pk_bf16_f32 v196, v208, v209\n\t" \
  "v_cvt_pk_bf16_f32 v197, v210, v211\n\t" \
  "v_cvt_pk_bf16_f32 v198, v212, v213\n\t" \
  "v_cvt_pk_bf16_f32 v199, v214, v215\n\t" \
  "v_mul_f32 v132, v132, v219\n\t" \
  "v_mul_f32 v133, v133, v219\n\t" \
  "v_mul_f32 v134, v134, v219\n\t" \
  "v_mul_f32 v135, v135, v219\n\t" \
  "v_mul_f32 v68, v68, v219\n\t" \
  "v_mul_f32 v69, v69, v219\n\t" \
  "v_mul_f32 v70, v70, v219\n\t" \
  "v_mul_f32 v71, v71, v219\n\t" \
  "v_mul_f32 v76, v76, v219\n\t" \
  "v_mul_f32 v77, v77, v219\n\t" \
  "v_mul_f32 v78, v78, v219\n\t" \
  "v_mul_f32 v79, v79, v219\n\t" \
  "v_mul_f32 v84, v84, v219\n\t" \
  "v_mul_f32 v85, v85, v219\n\t" \
  "v_mul_f32 v86, v86, v219\n\t" \
  "v_mul_f32 v87, v87, v219\n\t" \
  "v_mul_f32 v92, v92, v219\n\t" \
  "v_mul_f32 v93, v93, v219\n\t" \
  "v_mul_f32 v94, v94, v219\n\t" \
  "v_mul_f32 v95, v95, v219\n\t" \
  "v_mul_f32 v100, v100, v219\n\t" \
  "v_mul_f32 v101, v101, v219\n\t" \
  "v_mul_f32 v102, v102, v219\n\t" \
  "v_mul_f32 v103, v103, v219\n\t" \
  "v_mul_f32 v108, v108, v219\n\t" \
  "v_mul_f32 v109, v109, v219\n\t" \
  "v_mul_f32 v110, v110, v219\n\t" \
  "v_mul_f32 v111, v111, v219\n\t" \
  "v_mul_f32 v116, v116, v219\n\t" \
  "v_mul_f32 v117, v117, v219\n\t" \
  "v_mul_f32 v118, v118, v219\n\t" \
  "v_mul_f32 v119, v119, v219\n\t" \
  "v_mul_f32 v124, v124, v219\n\t" \
  "v_mul_f32 v125, v125, v219\n\t" \
  "v_mul_f32 v126, v126, v219\n\t" \
  "v_mul_f32 v127, v127, v219\n\t" \
  "s_nop 3\n\t" \
  "s_branch .Lback7_%=\n\t" \
  ".Lend_%=:\n\t" \
  "s_nop 1\n\t"
#define ASM_VCLOB "v64","v65","v66","v67","v68","v69","v70","v71","v72","v73","v74","v75","v76","v77","v78","v79","v80","v81","v82","v83","v84","v85","v86","v87","v88","v89","v90","v91","v92","v93","v94","v95","v96","v97","v98","v99","v100","v101","v102","v103","v104","v105","v106","v107","v108","v109","v110","v111","v112","v113","v114","v115","v116","v117","v118","v119","v120","v121","v122","v123","v124","v125","v126","v127","v128","v129","v130","v131","v132","v133","v134","v135","v136","v137","v138","v139","v140","v141","v142","v143","v144","v145","v146","v147","v148","v149","v150","v151","v152","v153","v154","v155","v156","v157","v158","v159","v160","v161","v162","v163","v164","v165","v166","v167","v168","v169","v170","v171","v172","v173","v174","v175","v176","v177","v178","v179","v180","v181","v182","v183","v184","v185","v186","v187","v188","v189","v190","v191","v192","v193","v194","v195","v196","v197","v198","v199","v200","v201","v202","v203","v204","v205","v206","v207","v208","v209","v210","v211","v212","v213","v214","v215","v216","v217","v218","v219","v220","v221","v222","v223","v224","v225","v226","v227","v228","v229","v230","v231","v232","v233","v234","v235","v236","v237","v238","v239","v240","v241","v242","v243","v244","v245","v246","v247","v248","v249","v250","v251","v252","v253","v254","v255"
#define ASM_ACLOB "a0","a1","a2","a3","a4","a5","a6","a7","a8","a9","a10","a11","a12","a13","a14","a15","a16","a17","a18","a19","a20","a21","a22","a23","a24","a25","a26","a27","a28","a29","a30","a31","a32","a33","a34","a35","a36","a37","a38","a39","a40","a41","a42","a43","a44","a45","a46","a47","a48","a49","a50","a51","a52","a53","a54","a55","a56","a57","a58","a59","a60","a61","a62","a63","a64","a65","a66","a67","a68","a69","a70","a71","a72","a73","a74","a75","a76","a77","a78","a79","a80","a81","a82","a83","a84","a85","a86","a87","a88","a89","a90","a91","a92","a93","a94","a95","a96","a97","a98","a99","a100","a101","a102","a103","a104","a105","a106","a107","a108","a109","a110","a111","a112","a113","a114","a115","a116","a117","a118","a119","a120","a121","a122","a123","a124","a125","a126","a127","a128","a129","a130","a131","a132","a133","a134","a135","a136","a137","a138","a139","a140","a141","a142","a143","a144","a145","a146","a147","a148","a149","a150","a151","a152","a153","a154","a155","a156","a157","a158","a159","a160","a161","a162","a163","a164","a165","a166","a167","a168","a169","a170","a171","a172","a173","a174","a175","a176","a177","a178","a179","a180","a181","a182","a183","a184","a185","a186","a187","a188","a189","a190","a191","a192","a193","a194","a195","a196","a197","a198","a199","a200","a201","a202","a203","a204","a205","a206","a207","a208","a209","a210","a211","a212","a213","a214","a215","a216","a217","a218","a219","a220","a221","a222","a223","a224","a225","a226","a227","a228","a229","a230","a231","a232","a233","a234","a235","a236","a237","a238","a239","a240","a241","a242","a243","a244","a245","a246","a247","a248","a249","a250","a251","a252","a253","a254","a255"

__global__ __launch_bounds__(256, 1) void fused_attn(Params p) {
  extern __shared__ __attribute__((aligned(16))) char smem[];
  const int w = __builtin_amdgcn_readfirstlane(threadIdx.x >> 6), rh = w & 1, pr = w >> 1;
  const int cu = blockIdx.x, xi = cu & 7, sidx = cu >> 3;
  const int br = sidx & 1, jj = (sidx >> 1) & 1, b = sidx >> 2, mem = xi * 2 + jj;
  const bool isA = br == 0;
  const int h = isA ? 4 * xi + w : 4 * xi + 2 * jj + pr, rows = isA ? jj : rh;
  const int grp = br * 8 + b, pstr = isA ? 32768 : 65536;
  u64 px, pk, pv, pq, po, pz, pout_, pws_;
  {
    const u64 ka_ = (u64)__builtin_amdgcn_kernarg_segment_ptr(); const unsigned o_ = br * 8;
    asm volatile("s_load_dwordx2 %0, %8, %9 offset:0\n\ts_load_dwordx2 %1, %8, %9 offset:16\n\ts_load_dwordx2 %2, %8, %9 offset:32\n\ts_load_dwordx2 %3, %8, %9 offset:48\n\t"
                 "s_load_dwordx2 %4, %8, %9 offset:64\n\ts_load_dwordx2 %5, %8, 72\n\ts_load_dwordx2 %6, %8, 80\n\ts_load_dwordx2 %7, %8, 88"
                 : "=&s"(px), "=&s"(pk), "=&s"(pv), "=&s"(pq), "=&s"(po), "=&s"(pz), "=&s"(pout_), "=&s"(pws_) : "s"(ka_), "s"(o_));
  }
  char *pws, *pout; rsrc_t rp;
  u64 tag;
  {
    const unsigned sm = (unsigned)(uintptr_t)(LAS char*)smem, ring = isA ? sm + RINGB + w * 3 * SLOT : sm + pr * RINGB;
    const int lane = threadIdx.x & 63, c = lane & 15, g = lane >> 4, q = c >> 2, pp = c & 3;
    const int rb = b & 3, rs = 2 * (b >> 2) + (isA ? jj : 0);
    const unsigned xch = isA ? sm + 4 * SLOT : sm + SPARE;
    const unsigned sw0 = (c ^ (g << 1)) * 16, sw1 = sw0 ^ 128;
    unsigned xv[4], st;
#pragma unroll
    for (int s = 0; s < 4; ++s) xv[s] = g * 1024 + (c ^ (4 * s + g)) * 16 + s * 3072;
    unsigned wv[4] = {g * 8192 + sw0, g * 8192 + sw0 + 31744, g * 8192 + sw1 + 2 * 31744, g * 8192 + sw1 + 3 * 31744};
    unsigned wa = rb * 0x100000u, wb = rs * 0x40000u + (isA ? 0 : rh * 0x20000u), mw = ring + (isA ? 0 : rh * 4096), mz = xch + w * SLOT;
    unsigned sx0 = rows * 32768 + (((isA ? w : pr) + rb) & 3) * 256;
    asm volatile("" : "+v"(xv[0]), "+v"(xv[1]), "+v"(xv[2]), "+v"(xv[3]), "+v"(wv[0]), "+v"(wv[1]), "+v"(wv[2]), "+v"(wv[3]));
    asm volatile("s_waitcnt lgkmcnt(0)" : "+s"(px), "+s"(pk), "+s"(pv), "+s"(pq), "+s"(po), "+s"(pz), "+s"(pout_), "+s"(pws_), "+s"(wa), "+s"(wb), "+s"(mw), "+s"(mz), "+s"(sx0));
    const rsrc_t rx = RSRC(px + b * 65536, 65536), rq = RSRC(pq + h * 256, (4 << 20) - h * 256);
    asm volatile(HEAD_ASM : [st] "=&s"(st)
                 : [xv0] "v"(xv[0]), [xv1] "v"(xv[1]), [xv2] "v"(xv[2]), [xv3] "v"(xv[3]), [wv0] "v"(wv[0]), [wv1] "v"(wv[1]), [wv2] "v"(wv[2]), [wv3] "v"(wv[3]),
                   [rx] "s"(rx), [rq] "s"(rq), [mw] "s"(mw), [mz] "s"(mz), [sx0] "s"(sx0), [wa] "s"(wa), [wb] "s"(wb) : "memory", "scc");
    {
      u64 pc; asm volatile("s_getpc_b64 %0" : "=s"(pc));
      const rsrc_t rc = RSRC((const char*)(pc & ~127ull), 54000);
      asm volatile("s_add_u32 m0, %0, %4\n\ts_nop 0\n\tbuffer_load_dwordx4 %1, %2, %3 offen lds" ::"s"(mw), "v"(lane * 16), "s"(rc), "s"((sidx * 4 + w) * 1024), "i"(2 * SLOT) : "scc", "memory");
    }
    __builtin_amdgcn_sched_barrier(0);
    const unsigned kvring = isA ? sm : ring, share = isA ? w * 2048 : rh * 4096;
    const int kvar = isA ? w : 2 * rh, pig = ((g & 1) << 1) | (g >> 1);
    unsigned ka[4], va[8], xa[4];
#pragma unroll
    for (int s = 0; s < 4; ++s) {
      ka[s] = kvring + q * 2048 + pp * 256 + (((4 * s + pig) ^ c) * 16);
      xa[s] = xch + (isA ? 0 : rh * SLOT) + c * 256 + (((4 * ((s + rs) & 3) + g) ^ c) * 16);
    }
#pragma unroll
    for (int f = 0; f < 8; ++f) va[f] = ring + (8 * g + q) * 256 + (((2 * f + (pp >> 1)) ^ ((q << 1) | ((g & 1) << 3))) * 16) + (pp & 1) * 8;
    const unsigned kv0 = g * 256 + (c ^ (4 * kvar + g)) * 16, kv1 = g * 256 + (c ^ (4 * (kvar + 1) + g)) * 16;
    const unsigned vv0 = g * 256 + (sw0 ^ ((kvar & 1) << 7)), vv1 = g * 256 + sw1;
    unsigned sk = share - 0x2000u, sv = share - 0x2000u, cnt; u64 tg;
    const unsigned ov[4] = {g * 1024 + sw0, g * 1024 + sw0 + 15360, g * 1024 + sw1 + 2048, g * 1024 + sw1 + 17408};
    const unsigned zv = lane * 16, oa = xch + w * SLOT + lane * 16, ob = xch + (isA ? 0 : (w ^ 2) * SLOT) + lane * 16, ev = isA ? c * 8 : lane * 128;
    const unsigned mkv = kvring + share, vdel = ring - kvring, sx1 = rh * 32768 + ((pr + 2 + rb) & 3) * 256;
    const unsigned so0 = isA ? w * 256 : jj * 262144 + pr * 131072 + pr * 512 + rh * 8192, so1 = jj * 262144 + (pr ^ 1) * 131072 + pr * 512 + rh * 8192;
    const unsigned sz = jj * 262144 + b * 32768 + w * 8192, sp = isA ? mem * 32768 + w * 8192 : mem * 65536 + pr * 32768 + rh * 8192;
    pws = (char*)pws_; pout = (char*)pout_;
    rp = RSRC(pws + WS_PART + (size_t)grp * 16 * 65536, 16 * 65536);
    const size_t kvo = (size_t)(br ? b * 32 + h : b * 8 + xi) * 262144;
    const rsrc_t rk = RSRC(pk + kvo, 262144), rv = RSRC(pv + kvo, 262144);
    const rsrc_t ro = RSRC(po + (size_t)xi * 524288, 524288), rz = RSRC(pz + (size_t)xi * 524288, 524288);
    const rsrc_t rsig = RSRC(pws + WS_SIG + b * 2048 + mem * 8, w == 0 ? 15 * 128 + 8 : 0), rg = RSRC(pws + WS_SIG + b * 2048 + mem * 128, 128), re = RSRC(pws + WS_EPOCH + b * 128, 8);
#define BODY_CALL(ASM) asm volatile(ASM \
      : [va0] "+v"(va[0]), [va1] "+v"(va[1]), [va2] "+v"(va[2]), [va3] "+v"(va[3]), [va4] "+v"(va[4]), [va5] "+v"(va[5]), [va6] "+v"(va[6]), [va7] "+v"(va[7]), \
        [sk] "+s"(sk), [sv] "+s"(sv), [wa] "+s"(wa), [wb] "+s"(wb), [st] "=&s"(st), [cnt] "=&s"(cnt), [tag] "=&s"(tg) \
      : [ka0] "v"(ka[0]), [ka1] "v"(ka[1]), [ka2] "v"(ka[2]), [ka3] "v"(ka[3]), [xa0] "v"(xa[0]), [xa1] "v"(xa[1]), [xa2] "v"(xa[2]), [xa3] "v"(xa[3]), \
        [xv0] "v"(xv[0]), [xv1] "v"(xv[1]), [xv2] "v"(xv[2]), [xv3] "v"(xv[3]), [kv0] "v"(kv0), [kv1] "v"(kv1), [vv0] "v"(vv0), [vv1] "v"(vv1), \
        [wv0] "v"(wv[0]), [wv1] "v"(wv[1]), [wv2] "v"(wv[2]), [wv3] "v"(wv[3]), [ov0] "v"(ov[0]), [ov1] "v"(ov[1]), [ov2] "v"(ov[2]), [ov3] "v"(ov[3]), \
        [zv] "v"(zv), [oa] "v"(oa), [ob] "v"(ob), [ev] "v"(ev), \
        [rk] "s"(rk), [rv] "s"(rv), [rx] "s"(rx), [rq] "s"(rq), [ro] "s"(ro), [rz] "s"(rz), [rp] "s"(rp), [rs] "s"(rsig), [rg] "s"(rg), [re] "s"(re), \
        [mkv] "s"(mkv), [mw] "s"(mw), [mz] "s"(mz), [sx1] "s"(sx1), [so0] "s"(so0), [so1] "s"(so1), [sz] "s"(sz), [sp] "s"(sp), [vdel] "s"(vdel), [wid] "s"(w) \
      : "memory", "scc", "vcc", ASM_VCLOB, ASM_ACLOB)
    if (isA) { BODY_CALL(BODY_A); } else { BODY_CALL(BODY_B); }
    tag = tg;
  }
  const int ln = lane_id();
  GAS u64* const flags = (GAS u64*)(pws + WS_FLAG) + grp * 16 * 16;
  u64 f = __hip_atomic_load(flags + mem * 16 + (ln & 15), __ATOMIC_RELAXED, __HIP_MEMORY_SCOPE_AGENT);
  u32x4 v[16];
#pragma unroll
  for (int q = 0; q < 16; ++q) v[q] = u32x4{0u, 0u, 0u, 0u};
  asm volatile("" : "+v"(v[0]), "+v"(v[1]), "+v"(v[2]), "+v"(v[3]), "+v"(v[4]), "+v"(v[5]), "+v"(v[6]), "+v"(v[7]),
               "+v"(v[8]), "+v"(v[9]), "+v"(v[10]), "+v"(v[11]), "+v"(v[12]), "+v"(v[13]), "+v"(v[14]), "+v"(v[15]));
  asm volatile("s_waitcnt vmcnt(0)" : "+v"(f) :: "memory");
  asm volatile("s_barrier" ::: "memory");
  if (w == 0 && ln < 16) __hip_atomic_store(flags + ln * 16 + mem, tag, __ATOMIC_RELAXED, __HIP_MEMORY_SCOPE_AGENT);
  {
    const int blk = (isA ? xi : mem) * 4 + w;
    const unsigned skip = isA ? (jj ? 0x5555u : 0xaaaau) : 0u;
    const bool upd = isA && mem == 0 && w == 0;
    unsigned have = upd ? 0u : skip;
    for (int spin = 0;; ++spin) {
      const unsigned now = ((unsigned)__builtin_amdgcn_ballot_w64(f == tag) & 0xffffu) | (1u << mem), fresh = now & ~have & ~skip;
      __builtin_amdgcn_fence(__ATOMIC_ACQUIRE, "wavefront");
#pragma unroll
      for (int q = 0; q < 16; ++q)
        if (fresh & (1u << q)) asm volatile("buffer_load_dwordx4 %0, %1, %2, %3 offen sc1" : "+v"(v[q]) : "v"(ln * 16), "s"(rp), "s"(q * pstr + blk * 1024) : "memory");
      have |= now;
      if (have == 0xffffu || spin >= (1 << 18)) break;
      f = __hip_atomic_load(flags + mem * 16 + (ln & 15), __ATOMIC_RELAXED, __HIP_MEMORY_SCOPE_AGENT);
    }
    if (upd && ln == 0) __hip_atomic_store((GAS u64*)(pws + WS_EPOCH + b * 128), tag, __ATOMIC_RELAXED, __HIP_MEMORY_SCOPE_AGENT);
    asm volatile("s_waitcnt vmcnt(0)" : "+v"(v[0]), "+v"(v[1]), "+v"(v[2]), "+v"(v[3]), "+v"(v[4]), "+v"(v[5]), "+v"(v[6]), "+v"(v[7]),
                 "+v"(v[8]), "+v"(v[9]), "+v"(v[10]), "+v"(v[11]), "+v"(v[12]), "+v"(v[13]), "+v"(v[14]), "+v"(v[15]) :: "memory");
    f32x4 x0 = {0.f, 0.f, 0.f, 0.f}, x1 = {0.f, 0.f, 0.f, 0.f};
#pragma unroll
    for (int q = 0; q < 16; ++q) {
      x0[0] += bflo(v[q][0]); x0[1] += bfhi(v[q][0]); x0[2] += bflo(v[q][1]); x0[3] += bfhi(v[q][1]);
      x1[0] += bflo(v[q][2]); x1[1] += bfhi(v[q][2]); x1[2] += bflo(v[q][3]); x1[3] += bfhi(v[q][3]);
    }
    const int n = isA ? blk >> 3 : blk >> 4, qb = isA ? 2 * jj + ((blk >> 2) & 1) : (blk >> 2) & 3, s = blk & 3;
    const auto s0 = __builtin_amdgcn_permlane16_swap(cvtpk(x0[0], x0[1]), cvtpk(x1[0], x1[1]), false, false);
    const auto s1 = __builtin_amdgcn_permlane16_swap(cvtpk(x0[2], x0[3]), cvtpk(x1[2], x1[3]), false, false);
    const int gg = ln >> 4, ch = ((gg & 1) << 1) | (gg >> 1);
    const rsrc_t rout = RSRC(pout + (size_t)b * 131072, 131072);
    __builtin_amdgcn_raw_buffer_store_b128(u32x4{s0[0], s1[0], s0[1], s1[1]}, rout, (ln & 15) * 2048 + ch * 16, qb * 32768 + br * 1024 + n * 256 + s * 64, 0);
  }
}

extern "C" void kernel_launch(void* const* d_in, const int* in_sizes, int n_in, void* d_out, int out_size, void* d_ws, size_t ws_size, hipStream_t stream) {
  Params p;
  for (int br = 0; br < 2; ++br) {
    p.x[br] = (const char*)d_in[5 * br + 0]; p.k[br] = (const char*)d_in[5 * br + 1]; p.v[br] = (const char*)d_in[5 * br + 2];
    p.wq[br] = (const char*)d_in[5 * br + 3]; p.wo[br] = (const char*)d_in[5 * br + 4];
  }
  p.out = (char*)d_out; p.ws = (char*)d_ws;
  (void)hipFuncSetAttribute((const void*)fused_attn, hipFuncAttributeMaxDynamicSharedMemorySize, LDS_BYTES);
  hipLaunchKernelGGL(fused_attn, dim3(256), dim3(256), LDS_BYTES, stream, p);
}
```
